```python
import torch

def _softmax(x, axis=-1):
    return torch.softmax(x, dim=axis)
def _gelu(x):
    return torch.nn.functional.gelu(x, approximate="tanh")
def _silu(x):
    return torch.nn.functional.silu(x)
def _sigmoid(x):
    return torch.sigmoid(x)
def _layernorm(x, axis=-1, eps=1e-5):
    m = x.mean(dim=axis, keepdim=True)
    v = x.var(dim=axis, keepdim=True, unbiased=False)
    return (x - m) / torch.sqrt(v + eps)
def _scatter_add(tgt, idx, src, axis=0):
    return tgt.clone().index_add_(axis, idx, src)
def _segment_sum(src, seg, n):
    out = torch.zeros((n,) + src.shape[1:], dtype=src.dtype)
    return out.index_add_(0, seg, src)
def _pad(x, pw):
    flat = tuple(v for pair in reversed(pw) for v in pair)
    return torch.nn.functional.pad(x, flat)

kernel_name = 'concat_heads__mha_block_cached__gqa_block_cached__near_ridge'

def setup_inputs():

    _w = (lambda a: (lambda t: t.to(torch.bfloat16) if t.is_floating_point() else t)(torch.as_tensor(a)))
    def _A_setup():
        B, Sq, D = 16, 160, 640
        H, Sk, Dh = 32, 512, 192
        return {
            "x": _w((lambda *s: torch.randn(*s, dtype=torch.bfloat16) * 0.8)(B, Sq, D)),
            "k_cache": _w((lambda *s: torch.randn(*s, dtype=torch.bfloat16) * 0.8)(B, H, Sk, Dh)),
            "v_cache": _w((lambda *s: torch.randn(*s, dtype=torch.bfloat16) * 0.05)(B, H, Sk, Dh)),
            "wq": _w((lambda *s: torch.randn(*s, dtype=torch.bfloat16) * 0.8)(D, H * Dh) * (D ** -0.5)),
            "wo": _w((lambda *s: torch.randn(*s, dtype=torch.bfloat16) * 0.05)(H * Dh, D) * ((H * Dh) ** -0.5)),
        }
    _A_in = _A_setup()

    def _B_setup():
        B, Sq, D = 16, 160, 640
        Hq, Hkv, Sk, Dh = 32, 32, 512, 192
        assert Hq % Hkv == 0, (Hq, Hkv)
        return {
            "x": _w((lambda *s: torch.randn(*s, dtype=torch.bfloat16) * 0.8)(B, Sq, D)),
            "k_cache": _w((lambda *s: torch.randn(*s, dtype=torch.bfloat16) * 0.8)(B, Hkv, Sk, Dh)),
            "v_cache": _w((lambda *s: torch.randn(*s, dtype=torch.bfloat16) * 0.05)(B, Hkv, Sk, Dh)),
            "wq": _w((lambda *s: torch.randn(*s, dtype=torch.bfloat16) * 0.8)(D, Hq * Dh) * (D ** -0.5)),
            "wo": _w((lambda *s: torch.randn(*s, dtype=torch.bfloat16) * 0.05)(Hq * Dh, D) * ((Hq * Dh) ** -0.5)),
        }
    _B_in = _B_setup()

    return {
        "A_x": _A_in["x"],
        "A_k_cache": _A_in["k_cache"],
        "A_v_cache": _A_in["v_cache"],
        "A_wq": _A_in["wq"],
        "A_wo": _A_in["wo"],
        "B_x": _B_in["x"],
        "B_k_cache": _B_in["k_cache"],
        "B_v_cache": _B_in["v_cache"],
        "B_wq": _B_in["wq"],
        "B_wo": _B_in["wo"],
    }

def reference(A_x, A_k_cache, A_v_cache, A_wq, A_wo, B_x, B_k_cache, B_v_cache, B_wq, B_wo):
    def _A_ref(x, k_cache, v_cache, wq, wo):
        B, Sq, D = x.shape
        H, Dh = 32, 192
        q = (lambda x, *d: x.permute(*d))((x @ wq).reshape(B, Sq, H, Dh), 0, 2, 1, 3)
        scores = torch.einsum("bhqd,bhkd->bhqk", q, k_cache) * (Dh ** -0.5)
        p = _softmax(scores, axis=-1)
        o = torch.einsum("bhqk,bhkd->bhqd", p, v_cache)
        return (lambda x, *d: x.permute(*d))(o, 0, 2, 1, 3).reshape(B, Sq, H * Dh) @ wo

    def _B_ref(x, k_cache, v_cache, wq, wo):
        B, Sq, D = x.shape
        Hq, Dh = 32, 192
        Hkv = k_cache.shape[1]
        rep = Hq // Hkv
        q = (lambda x, *d: x.permute(*d))((x @ wq).reshape(B, Sq, Hq, Dh), 0, 2, 1, 3)
        k = (lambda x, n, axis: torch.repeat_interleave(x, n, dim=axis))(k_cache, rep, axis=1)
        v = (lambda x, n, axis: torch.repeat_interleave(x, n, dim=axis))(v_cache, rep, axis=1)
        scores = torch.einsum("bhqd,bhkd->bhqk", q, k) * (Dh ** -0.5)
        p = _softmax(scores, axis=-1)
        o = torch.einsum("bhqk,bhkd->bhqd", p, v)
        return (lambda x, *d: x.permute(*d))(o, 0, 2, 1, 3).reshape(B, Sq, Hq * Dh) @ wo

    a = _A_ref(A_x, A_k_cache, A_v_cache, A_wq, A_wo)
    b = _B_ref(B_x, B_k_cache, B_v_cache, B_wq, B_wo)
    return torch.concatenate([a, b], axis=-1)
```

```cpp
#include <hip/hip_runtime.h>
#include <hip/hip_bf16.h>
typedef unsigned short u16;
__device__ __forceinline__ float bf2f(u16 x){ return __uint_as_float(((unsigned)x)<<16); }
__device__ __forceinline__ u16 f2bf(float f){ unsigned u=__float_as_uint(f); u += 0x7FFF + ((u>>16)&1); return (u16)(u>>16); }
constexpr int B=16,SQ=160,D=640,H=32,SK=512,DH=192,HD=H*DH;
__global__ void gemm_naive(const u16* A,const u16* W,u16* C,int M,int N,int K,int ldc){
  __shared__ float As[16][17], Ws[16][17];
  int tx=threadIdx.x&15, ty=threadIdx.x>>4; int n=blockIdx.x*16+tx, m=blockIdx.y*16+ty; float acc=0;
  for(int k0=0;k0<K;k0+=16){ As[ty][tx]=bf2f(A[(size_t)m*K+k0+tx]); Ws[ty][tx]=bf2f(W[(size_t)(k0+ty)*N+n]); __syncthreads();
    for(int k=0;k<16;++k) acc+=As[ty][k]*Ws[k][tx]; __syncthreads(); }
  C[(size_t)m*ldc+n]=f2bf(acc);
}
__global__ void attn_naive(const u16* Q,const u16* K,const u16* V,u16* O){
  __shared__ float q[DH]; __shared__ float s[SK]; __shared__ float red[256];
  int qi=blockIdx.x, h=blockIdx.y, b=blockIdx.z, t=threadIdx.x;
  if(t<DH) q[t]=bf2f(Q[((size_t)b*SQ+qi)*HD+h*DH+t]); __syncthreads();
  const u16* Kp=K+((size_t)b*H+h)*SK*DH; const u16* Vp=V+((size_t)b*H+h)*SK*DH;
  float mx=-1e30f;
  for(int k=t;k<SK;k+=256){ float a=0; for(int d=0;d<DH;++d) a+=q[d]*bf2f(Kp[(size_t)k*DH+d]); a*=0.07216878364870322f; s[k]=a; mx=fmaxf(mx,a);}
  red[t]=mx; __syncthreads(); for(int o=128;o>0;o>>=1){ if(t<o) red[t]=fmaxf(red[t],red[t+o]); __syncthreads(); } mx=red[0]; __syncthreads();
  float sm=0; for(int k=t;k<SK;k+=256){ float e=__expf(s[k]-mx); s[k]=e; sm+=e; }
  red[t]=sm; __syncthreads(); for(int o=128;o>0;o>>=1){ if(t<o) red[t]+=red[t+o]; __syncthreads(); } sm=red[0];
  if(t<DH){ float a=0; for(int k=0;k<SK;++k) a+=s[k]*bf2f(Vp[(size_t)k*DH+t]); O[((size_t)b*SQ+qi)*HD+h*DH+t]=f2bf(a/sm); }
}
extern "C" void kernel_launch(void* const* d_in, const int* in_sizes, int n_in, void* d_out, int out_size, void* d_ws, size_t ws_size, hipStream_t stream){
  u16* Qw=(u16*)d_ws; u16* Ow=Qw+(size_t)B*SQ*HD;
  for(int br=0;br<2;++br){
    const u16* x=(const u16*)d_in[5*br+0]; const u16* k=(const u16*)d_in[5*br+1]; const u16* v=(const u16*)d_in[5*br+2]; const u16* wq=(const u16*)d_in[5*br+3]; const u16* wo=(const u16*)d_in[5*br+4];
    gemm_naive<<<dim3(HD/16,B*SQ/16),256,0,stream>>>(x,wq,Qw,B*SQ,HD,D,HD);
    attn_naive<<<dim3(SQ,H,B),256,0,stream>>>(Qw,k,v,Ow);
    gemm_naive<<<dim3(D/16,B*SQ/16),256,0,stream>>>(Ow,wo,(u16*)d_out+br*D,B*SQ,D,HD,2*D);
  }
}
```

```cpp
#include <hip/hip_runtime.h>
#include <hip/hip_bf16.h>
typedef unsigned short u16;
__device__ __forceinline__ float bf2f(u16 x){ return __uint_as_float(((unsigned)x)<<16); }
__device__ __forceinline__ u16 f2bf(float f){ unsigned u=__float_as_uint(f); u += 0x7FFF + ((u>>16)&1); return (u16)(u>>16); }

extern "C" __global__ __launch_bounds__(256, 1) void fused_attn(const void* xA, const void* kA, const void* vA, const void* wqA, const void* woA,
                                                     const void* xB, const void* kB, const void* vB, const void* wqB, const void* woB, void* out, void* ws) {
  __shared__ __attribute__((aligned(16))) char lds[163840];
  const unsigned wid = __builtin_amdgcn_readfirstlane(threadIdx.x >> 6);
  asm volatile(
R"ASM(
LSTART_%=:
s_load_dwordx16 s[76:91],%0,0
s_load_dwordx8 s[92:99],%0,64
s_mov_b32 s16,%1
s_mov_b32 s17,%2
s_lshr_b32 s18,s16,1
s_and_b32 s19,s16,1
s_and_b32 s64,s17,7
s_lshr_b32 s65,s17,3
s_lshr_b32 s66,s64,2
s_lshl_b32 s66,s66,3
s_lshr_b32 s67,s65,2
s_add_u32 s48,s66,s67
s_and_b32 s66,s64,3
s_lshl_b32 s66,s66,2
s_and_b32 s67,s65,3
s_add_u32 s49,s66,s67
s_bfe_u32 s50,s17,65540
s_and_b32 s33,s17,3
s_and_b32 s34,s49,1
s_mul_i32 s64,s18,12288
s_mul_i32 s65,s19,6144
s_add_u32 s56,s64,s65
s_add_u32 s57,s56,73728
s_mul_i32 s59,s16,5120
s_add_u32 s59,s59,98336
s_mul_i32 s64,s18,6144
s_mul_i32 s65,s19,12296
s_add_u32 s58,s64,s65
s_mov_b32 s62,s50
s_mov_b32 s60,0
s_mov_b32 s61,0
s_mov_b32 s63,0
s_mov_b32 s5,0x2b800000
s_mov_b32 s23,131072
s_mov_b32 s22,0
s_mov_b32 s27,131072
s_mov_b32 s26,0
s_mov_b32 s31,131072
s_mov_b32 s30,0
s_mov_b32 s39,131072
s_mov_b32 s38,0
s_mov_b32 s43,131072
s_mov_b32 s42,0
s_mov_b32 s47,131072
s_mov_b32 s46,0
s_mov_b32 s11,131072
s_mov_b32 s10,0
s_mov_b32 s15,131072
s_mov_b32 s14,0
v_mbcnt_lo_u32_b32 v254,-1,0
v_mbcnt_hi_u32_b32 v254,-1,v254
v_add_u32 v240,0,v254
v_mul_u32_u24 v255,2731,v240
v_lshrrev_b32 v255,16,v255
v_mul_u32_u24 v243,24,v255
v_sub_u32 v240,v240,v243
v_and_b32 v243,2,v255
v_lshlrev_b32 v243,1,v243
v_xor_b32 v240,v240,v243
v_lshlrev_b32 v240,4,v240
v_mul_u32_u24 v255,12288,v255
v_add_u32 v240,v240,v255
s_mul_i32 s64,s19,98304
s_add_u32 s64,s64,2048
v_add_u32 v240,s64,v240
v_add_u32 v241,64,v254
v_mul_u32_u24 v255,2731,v241
v_lshrrev_b32 v255,16,v255
v_mul_u32_u24 v243,24,v255
v_sub_u32 v241,v241,v243
v_and_b32 v243,2,v255
v_lshlrev_b32 v243,1,v243
v_xor_b32 v241,v241,v243
v_lshlrev_b32 v241,4,v241
v_mul_u32_u24 v255,12288,v255
v_add_u32 v241,v241,v255
s_mul_i32 s64,s19,98304
s_add_u32 s64,s64,1024
v_add_u32 v241,s64,v241
v_add_u32 v242,128,v254
v_mul_u32_u24 v255,2731,v242
v_lshrrev_b32 v255,16,v255
v_mul_u32_u24 v243,24,v255
v_sub_u32 v242,v242,v243
v_and_b32 v243,2,v255
v_lshlrev_b32 v243,1,v243
v_xor_b32 v242,v242,v243
v_lshlrev_b32 v242,4,v242
v_mul_u32_u24 v255,12288,v255
v_add_u32 v242,v242,v255
s_mul_i32 s64,s19,98304
s_add_u32 s64,s64,0
v_add_u32 v242,s64,v242
v_lshrrev_b32 v255,3,v254
v_and_b32 v243,6,v255
v_xor_b32 v243,v243,v254
v_and_b32 v243,7,v243
v_lshlrev_b32 v243,4,v243
v_mul_u32_u24 v255,1280,v255
v_add_u32 v243,v243,v255
s_waitcnt lgkmcnt(0)
s_mov_b64 s[28:29],s[98:99]
s_mov_b64 s[36:37],s[98:99]
s_mov_b64 s[40:41],s[98:99]
s_mov_b64 s[44:45],s[98:99]
s_mov_b64 s[8:9],s[98:99]
s_mov_b64 s[12:13],s[98:99]
s_cmp_eq_u32 s62,0
s_cselect_b64 s[20:21],s[76:77],s[86:87]
s_mul_i32 s64,s48,204800
s_add_u32 s20,s20,s64
s_addc_u32 s21,s21,0
s_mov_b32 s22,204800
s_cmp_eq_u32 s62,0
s_cselect_b64 s[24:25],s[82:83],s[92:93]
s_lshl_b32 s64,s49,1
s_add_u32 s64,s64,s18
s_mul_i32 s64,s64,384
s_add_u32 s24,s24,s64
s_addc_u32 s25,s25,0
s_sub_u32 s24,s24,2048
s_subb_u32 s25,s25,0
s_mov_b32 s26,7854464
s_mov_b32 s35,s62
s_mov_b32 s52,0
s_mul_i32 s53,s16,51200
s_mul_i32 s59,s16,5120
s_add_u32 s59,s59,98336
s_mul_i32 s64,s18,6144
s_mul_i32 s65,s19,12296
s_add_u32 s58,s64,s65
s_add_u32 m0,s58,0
s_nop 0
buffer_load_dwordx4 v240,s[24:27],s52 offen lds
buffer_load_dwordx4 v241,s[24:27],s52 offen offset:1024 lds
buffer_load_dwordx4 v242,s[24:27],s52 offen offset:2048 lds
s_add_u32 m0,s58,3072
s_add_u32 s66,s52,196608
buffer_load_dwordx4 v240,s[24:27],s66 offen lds
buffer_load_dwordx4 v241,s[24:27],s66 offen offset:1024 lds
buffer_load_dwordx4 v242,s[24:27],s66 offen offset:2048 lds
s_add_u32 s52,s52,393216
s_add_u32 m0,s59,40960
s_nop 0
buffer_load_dwordx4 v243,s[20:23],s53 offen lds
s_add_u32 s67,s53,9216
buffer_load_dwordx4 v243,s[20:23],s67 offen offset:1024 lds
s_add_u32 s67,s53,18432
buffer_load_dwordx4 v243,s[20:23],s67 offen offset:2048 lds
s_add_u32 s67,s53,27648
buffer_load_dwordx4 v243,s[20:23],s67 offen offset:3072 lds
s_add_u32 m0,s59,45056
s_add_u32 s67,s53,40960
buffer_load_dwordx4 v243,s[20:23],s67 offen lds
s_add_u32 s53,s53,128
s_add_u32 m0,s58,24584
s_nop 0
buffer_load_dwordx4 v240,s[24:27],s52 offen lds
buffer_load_dwordx4 v241,s[24:27],s52 offen offset:1024 lds
buffer_load_dwordx4 v242,s[24:27],s52 offen offset:2048 lds
s_add_u32 m0,s58,27656
s_add_u32 s66,s52,196608
buffer_load_dwordx4 v240,s[24:27],s66 offen lds
buffer_load_dwordx4 v241,s[24:27],s66 offen offset:1024 lds
buffer_load_dwordx4 v242,s[24:27],s66 offen offset:2048 lds
s_add_u32 s52,s52,393216
s_add_u32 m0,s59,0
s_nop 0
buffer_load_dwordx4 v243,s[20:23],s53 offen lds
s_add_u32 s67,s53,9216
buffer_load_dwordx4 v243,s[20:23],s67 offen offset:1024 lds
s_add_u32 s67,s53,18432
buffer_load_dwordx4 v243,s[20:23],s67 offen offset:2048 lds
s_add_u32 s67,s53,27648
buffer_load_dwordx4 v243,s[20:23],s67 offen offset:3072 lds
s_add_u32 m0,s59,4096
s_add_u32 s67,s53,40960
buffer_load_dwordx4 v243,s[20:23],s67 offen lds
s_add_u32 s53,s53,128
s_add_u32 m0,s58,49168
s_nop 0
buffer_load_dwordx4 v240,s[24:27],s52 offen lds
buffer_load_dwordx4 v241,s[24:27],s52 offen offset:1024 lds
buffer_load_dwordx4 v242,s[24:27],s52 offen offset:2048 lds
s_add_u32 m0,s58,52240
s_add_u32 s66,s52,196608
buffer_load_dwordx4 v240,s[24:27],s66 offen lds
buffer_load_dwordx4 v241,s[24:27],s66 offen offset:1024 lds
buffer_load_dwordx4 v242,s[24:27],s66 offen offset:2048 lds
s_add_u32 s52,s52,393216
s_getpc_b64 s[44:45]
LPC0_%=:
s_sub_u32 s44,s44,LPC0_%=-LSTART_%=
s_subb_u32 s45,s45,0
s_mov_b32 s46,LCODEEND_%=-LSTART_%=
s_lshr_b32 s64,s17,3
s_lshl_b32 s64,s64,2
s_add_u32 s64,s64,s16
s_mul_i32 s64,s64,768
v_mbcnt_lo_u32_b32 v254,-1,0
v_mbcnt_hi_u32_b32 v254,-1,v254
v_min_u32 v254,5,v254
v_lshlrev_b32 v254,7,v254
buffer_load_dword a250,v254,s[44:47],s64 offen
v_mbcnt_lo_u32_b32 v254,-1,0
v_mbcnt_hi_u32_b32 v254,-1,v254
v_add_u32 v250,0,v254
v_mul_u32_u24 v255,2731,v250
v_lshrrev_b32 v255,16,v255
v_and_b32 v255,6,v255
v_xor_b32 v250,v250,v255
v_subrev_u32 v250,0,v250
v_lshlrev_b32 v250,4,v250
v_add_u32 v251,64,v254
v_mul_u32_u24 v255,2731,v251
v_lshrrev_b32 v255,16,v255
v_and_b32 v255,6,v255
v_xor_b32 v251,v251,v255
v_subrev_u32 v251,64,v251
v_lshlrev_b32 v251,4,v251
v_add_u32 v252,128,v254
v_mul_u32_u24 v255,2731,v252
v_lshrrev_b32 v255,16,v255
v_and_b32 v255,6,v255
v_xor_b32 v252,v252,v255
v_subrev_u32 v252,128,v252
v_lshlrev_b32 v252,4,v252
s_lshl_b32 s68,s48,5
s_lshl_b32 s69,s49,1
s_add_u32 s68,s68,s69
s_add_u32 s68,s68,s18
s_mul_i32 s69,s68,196608
s_cmp_eq_u32 s62,0
s_cselect_b64 s[28:29],s[78:79],s[88:89]
s_add_u32 s28,s28,s69
s_addc_u32 s29,s29,0
s_cmp_eq_u32 s62,0
s_cselect_b64 s[40:41],s[80:81],s[90:91]
s_add_u32 s40,s40,s69
s_addc_u32 s41,s41,0
s_mov_b32 s30,196608
s_mov_b32 s42,196608
s_mul_i32 s54,s19,6144
s_mov_b32 s55,s54
LQM_%=:
v_mbcnt_lo_u32_b32 v254,-1,0
v_mbcnt_hi_u32_b32 v254,-1,v254
v_lshrrev_b32 v248,4,v254
v_add_u32 v248,0,v248
v_and_b32 v255,6,v254
v_xor_b32 v248,v248,v255
v_lshlrev_b32 v248,4,v248
v_and_b32 v255,15,v254
v_lshlrev_b32 v255,7,v255
v_add_u32 v248,v248,v255
s_mul_i32 s64,s19,10240
s_add_u32 s64,s64,98336
v_add_u32 v248,s64,v248
v_lshrrev_b32 v249,4,v254
v_add_u32 v249,4,v249
v_and_b32 v255,6,v254
v_xor_b32 v249,v249,v255
v_lshlrev_b32 v249,4,v249
v_and_b32 v255,15,v254
v_lshlrev_b32 v255,7,v255
v_add_u32 v249,v249,v255
s_mul_i32 s64,s19,10240
s_add_u32 s64,s64,98336
v_add_u32 v249,s64,v249
v_bfe_u32 v244,v254,4,1
v_mul_u32_u24 v244,12296,v244
v_lshrrev_b32 v255,5,v254
v_mul_u32_u24 v255,3072,v255
v_add_u32 v244,v244,v255
v_bfe_u32 v255,v254,2,2
v_mul_u32_u24 v255,384,v255
v_add_u32 v244,v244,v255
v_and_b32 v255,3,v254
v_lshlrev_b32 v255,4,v255
v_add_u32 v244,v244,v255
s_mul_i32 s64,s18,6144
v_add_u32 v244,s64,v244
v_bfe_u32 v255,v254,3,1
v_lshlrev_b32 v255,6,v255
v_xor_b32 v245,64,v255
v_add_u32 v245,v245,v244
v_add_u32 v244,v244,v255
v_add_u32 v246,49168,v244
v_add_u32 v247,49168,v245
v_mbcnt_lo_u32_b32 v253,-1,0
v_mbcnt_hi_u32_b32 v253,-1,v253
v_lshlrev_b32 v253,4,v253
v_mbcnt_lo_u32_b32 v255,-1,0
v_mbcnt_hi_u32_b32 v255,-1,v255
v_min_u32 v254,11,v255
v_lshlrev_b32 v254,7,v254
v_min_u32 v255,19,v255
v_mul_u32_u24 v255,1280,v255
s_and_b32 s64,s48,7
s_lshl_b32 s64,s64,1
s_add_u32 s64,s64,s34
s_lshl_b32 s64,s64,2
s_add_u32 s64,s64,s16
s_add_u32 s64,s64,96
s_mul_i32 s74,s64,12288
s_lshl_b32 s75,s34,2
s_add_u32 s75,s75,s16
s_mul_i32 s75,s75,25600
s_cmp_eq_u32 s35,0
s_cselect_b64 s[12:13],s[76:77],s[86:87]
s_mul_i32 s65,s48,204800
s_add_u32 s12,s12,s65
s_addc_u32 s13,s13,0
s_cmp_eq_u32 s35,0
s_cselect_b64 s[44:45],s[82:83],s[92:93]
s_mul_i32 s65,s33,3072
s_mul_i32 s66,s50,1536
s_add_u32 s65,s65,s66
s_add_u32 s44,s44,s65
s_addc_u32 s45,s45,0
s_mov_b32 s7,204800
s_mov_b32 s6,7853568
s_cmp_eq_u32 s61,0
s_cselect_b32 s46,s6,0
buffer_load_dword a250,v254,s[44:47],s74 offen
s_add_u32 s74,s74,786432
buffer_load_dword a250,v254,s[44:47],s74 offen
s_add_u32 s74,s74,786432
s_waitcnt vmcnt(19)
s_barrier
ds_read_b128 a[0:3],v248 offset:40960
ds_read_b128 a[4:7],v248 offset:43008
ds_read_b128 a[8:11],v248 offset:45056
ds_read_b128 a[12:15],v248 offset:47104
ds_read_b128 a[16:19],v248 offset:49152
ds_read_b64_tr_b16 a[200:201],v244 offset:256
ds_read_b64_tr_b16 a[202:203],v244 offset:1792
ds_read_b64_tr_b16 a[204:205],v244 offset:264
ds_read_b64_tr_b16 a[206:207],v244 offset:1800
ds_read_b64_tr_b16 a[208:209],v245 offset:256
ds_read_b64_tr_b16 a[210:211],v245 offset:1792
ds_read_b64_tr_b16 a[212:213],v245 offset:264
ds_read_b64_tr_b16 a[214:215],v245 offset:1800
ds_read_b64_tr_b16 a[216:217],v244
ds_read_b64_tr_b16 a[218:219],v244 offset:1536
ds_read_b64_tr_b16 a[220:221],v244 offset:8
ds_read_b64_tr_b16 a[222:223],v244 offset:1544
s_waitcnt lgkmcnt(0)
v_mfma_f32_16x16x32_bf16 v[160:163],a[200:203],a[0:3],0
v_mfma_f32_16x16x32_bf16 v[164:167],a[200:203],a[4:7],0
buffer_load_dword a250,v254,s[44:47],s74 offen
v_mfma_f32_16x16x32_bf16 v[168:171],a[200:203],a[8:11],0
s_add_u32 m0,s59,20480
ds_read_b64_tr_b16 a[224:225],v245
ds_read_b64_tr_b16 a[226:227],v245 offset:1536
v_mfma_f32_16x16x32_bf16 v[172:175],a[200:203],a[12:15],0
s_add_u32 s74,s74,786432
buffer_load_dwordx4 v243,s[20:23],s53 offen lds
v_mfma_f32_16x16x32_bf16 v[176:179],a[200:203],a[16:19],0
s_add_u32 m0,s59,20480
ds_read_b64_tr_b16 a[228:229],v245 offset:8
ds_read_b64_tr_b16 a[230:231],v245 offset:1544
v_mfma_f32_16x16x32_bf16 v[180:183],a[204:207],a[0:3],0
s_add_u32 s67,s53,9216
ds_read_b64_tr_b16 a[232:233],v244 offset:128
v_mfma_f32_16x16x32_bf16 v[184:187],a[204:207],a[4:7],0
buffer_load_dwordx4 v243,s[20:23],s67 offen offset:1024 lds
v_mfma_f32_16x16x32_bf16 v[188:191],a[204:207],a[8:11],0
s_add_u32 m0,s59,20480
ds_read_b64_tr_b16 a[234:235],v244 offset:1664
ds_read_b64_tr_b16 a[236:237],v244 offset:136
v_mfma_f32_16x16x32_bf16 v[192:195],a[204:207],a[12:15],0
s_add_u32 s67,s53,18432
ds_read_b64_tr_b16 a[238:239],v244 offset:1672
v_mfma_f32_16x16x32_bf16 v[196:199],a[204:207],a[16:19],0
buffer_load_dwordx4 v243,s[20:23],s67 offen offset:2048 lds
v_mfma_f32_16x16x32_bf16 v[200:203],a[208:211],a[0:3],0
s_add_u32 m0,s59,20480
ds_read_b64_tr_b16 a[240:241],v245 offset:128
ds_read_b64_tr_b16 a[242:243],v245 offset:1664
v_mfma_f32_16x16x32_bf16 v[204:207],a[208:211],a[4:7],0
s_add_u32 s67,s53,27648
ds_read_b64_tr_b16 a[244:245],v245 offset:136
v_mfma_f32_16x16x32_bf16 v[208:211],a[208:211],a[8:11],0
buffer_load_dwordx4 v243,s[20:23],s67 offen offset:3072 lds
v_mfma_f32_16x16x32_bf16 v[212:215],a[208:211],a[12:15],0
s_add_u32 m0,s59,24576
ds_read_b64_tr_b16 a[246:247],v245 offset:1672
v_mfma_f32_16x16x32_bf16 v[216:219],a[208:211],a[16:19],0
s_add_u32 s67,s53,40960
v_mfma_f32_16x16x32_bf16 v[220:223],a[212:215],a[0:3],0
buffer_load_dwordx4 v243,s[20:23],s67 offen lds
v_mfma_f32_16x16x32_bf16 v[224:227],a[212:215],a[4:7],0
s_add_u32 s53,s53,128
v_mfma_f32_16x16x32_bf16 v[228:231],a[212:215],a[8:11],0
v_mfma_f32_16x16x32_bf16 v[232:235],a[212:215],a[12:15],0
v_mfma_f32_16x16x32_bf16 v[236:239],a[212:215],a[16:19],0
s_mov_b32 s72,11264
v_mfma_f32_16x16x32_bf16 v[0:3],a[216:219],a[0:3],0
buffer_store_dwordx4 a[104:107],v253,s[36:39],s72 offen sc1
v_mfma_f32_16x16x32_bf16 v[4:7],a[216:219],a[4:7],0
v_mfma_f32_16x16x32_bf16 v[8:11],a[216:219],a[8:11],0
v_mfma_f32_16x16x32_bf16 v[12:15],a[216:219],a[12:15],0
v_mfma_f32_16x16x32_bf16 v[16:19],a[216:219],a[16:19],0
s_mov_b32 s72,12288
v_mfma_f32_16x16x32_bf16 v[20:23],a[220:223],a[0:3],0
buffer_store_dwordx4 a[108:111],v253,s[36:39],s72 offen sc1
v_mfma_f32_16x16x32_bf16 v[24:27],a[220:223],a[4:7],0
v_mfma_f32_16x16x32_bf16 v[28:31],a[220:223],a[8:11],0
v_mfma_f32_16x16x32_bf16 v[32:35],a[220:223],a[12:15],0
v_mfma_f32_16x16x32_bf16 v[36:39],a[220:223],a[16:19],0
s_waitcnt vmcnt(21) lgkmcnt(0)
s_barrier
v_mfma_f32_16x16x32_bf16 v[40:43],a[224:227],a[0:3],0
ds_read_b128 a[20:23],v249 offset:40960
v_mfma_f32_16x16x32_bf16 v[44:47],a[224:227],a[4:7],0
ds_read_b128 a[24:27],v249 offset:43008
v_mfma_f32_16x16x32_bf16 v[48:51],a[224:227],a[8:11],0
s_mov_b32 s72,13312
ds_read_b128 a[28:31],v249 offset:45056
v_mfma_f32_16x16x32_bf16 v[52:55],a[224:227],a[12:15],0
buffer_store_dwordx4 a[112:115],v253,s[36:39],s72 offen sc1
v_mfma_f32_16x16x32_bf16 v[56:59],a[224:227],a[16:19],0
ds_read_b128 a[32:35],v249 offset:47104
ds_read_b128 a[36:39],v249 offset:49152
v_mfma_f32_16x16x32_bf16 v[60:63],a[228:231],a[0:3],0
ds_read_b64_tr_b16 a[200:201],v244 offset:24840
v_mfma_f32_16x16x32_bf16 v[64:67],a[228:231],a[4:7],0
ds_read_b64_tr_b16 a[202:203],v244 offset:26376
v_mfma_f32_16x16x32_bf16 v[68:71],a[228:231],a[8:11],0
ds_read_b64_tr_b16 a[204:205],v244 offset:24848
v_mfma_f32_16x16x32_bf16 v[72:75],a[228:231],a[12:15],0
s_mov_b32 s72,14336
ds_read_b64_tr_b16 a[206:207],v244 offset:26384
v_mfma_f32_16x16x32_bf16 v[76:79],a[228:231],a[16:19],0
buffer_store_dwordx4 a[116:119],v253,s[36:39],s72 offen sc1
v_mfma_f32_16x16x32_bf16 v[80:83],a[232:235],a[0:3],0
ds_read_b64_tr_b16 a[208:209],v245 offset:24840
ds_read_b64_tr_b16 a[210:211],v245 offset:26376
v_mfma_f32_16x16x32_bf16 v[84:87],a[232:235],a[4:7],0
ds_read_b64_tr_b16 a[212:213],v245 offset:24848
v_mfma_f32_16x16x32_bf16 v[88:91],a[232:235],a[8:11],0
ds_read_b64_tr_b16 a[214:215],v245 offset:26384
v_mfma_f32_16x16x32_bf16 v[92:95],a[232:235],a[12:15],0
ds_read_b64_tr_b16 a[216:217],v244 offset:24584
v_mfma_f32_16x16x32_bf16 v[96:99],a[232:235],a[16:19],0
s_mov_b32 s72,20480
ds_read_b64_tr_b16 a[218:219],v244 offset:26120
v_mfma_f32_16x16x32_bf16 v[100:103],a[236:239],a[0:3],0
buffer_store_dwordx4 a[120:123],v253,s[36:39],s72 offen sc1
v_mfma_f32_16x16x32_bf16 v[104:107],a[236:239],a[4:7],0
ds_read_b64_tr_b16 a[220:221],v244 offset:24592
ds_read_b64_tr_b16 a[222:223],v244 offset:26128
v_mfma_f32_16x16x32_bf16 v[108:111],a[236:239],a[8:11],0
v_mfma_f32_16x16x32_bf16 v[112:115],a[236:239],a[12:15],0
v_mfma_f32_16x16x32_bf16 v[116:119],a[236:239],a[16:19],0
v_mfma_f32_16x16x32_bf16 v[120:123],a[240:243],a[0:3],0
s_mov_b32 s72,21504
v_mfma_f32_16x16x32_bf16 v[124:127],a[240:243],a[4:7],0
buffer_store_dwordx4 a[124:127],v253,s[36:39],s72 offen sc1
v_mfma_f32_16x16x32_bf16 v[128:131],a[240:243],a[8:11],0
v_mfma_f32_16x16x32_bf16 v[132:135],a[240:243],a[12:15],0
v_mfma_f32_16x16x32_bf16 v[136:139],a[240:243],a[16:19],0
v_mfma_f32_16x16x32_bf16 v[140:143],a[244:247],a[0:3],0
v_mfma_f32_16x16x32_bf16 v[144:147],a[244:247],a[4:7],0
v_mfma_f32_16x16x32_bf16 v[148:151],a[244:247],a[8:11],0
v_mfma_f32_16x16x32_bf16 v[152:155],a[244:247],a[12:15],0
v_mfma_f32_16x16x32_bf16 v[156:159],a[244:247],a[16:19],0
s_waitcnt lgkmcnt(0)
v_mfma_f32_16x16x32_bf16 v[160:163],a[200:203],a[20:23],v[160:163]
v_mfma_f32_16x16x32_bf16 v[164:167],a[200:203],a[24:27],v[164:167]
ds_read_b64_tr_b16 a[224:225],v245 offset:24584
v_mfma_f32_16x16x32_bf16 v[168:171],a[200:203],a[28:31],v[168:171]
ds_read_b64_tr_b16 a[226:227],v245 offset:26120
v_mfma_f32_16x16x32_bf16 v[172:175],a[200:203],a[32:35],v[172:175]
s_add_u32 m0,s58,73752
ds_read_b64_tr_b16 a[228:229],v245 offset:24592
v_mfma_f32_16x16x32_bf16 v[176:179],a[200:203],a[36:39],v[176:179]
buffer_load_dwordx4 v240,s[24:27],s52 offen lds
v_mfma_f32_16x16x32_bf16 v[180:183],a[204:207],a[20:23],v[180:183]
ds_read_b64_tr_b16 a[230:231],v245 offset:26128
ds_read_b64_tr_b16 a[232:233],v244 offset:24712
v_mfma_f32_16x16x32_bf16 v[184:187],a[204:207],a[24:27],v[184:187]
ds_read_b64_tr_b16 a[234:235],v244 offset:26248
v_mfma_f32_16x16x32_bf16 v[188:191],a[204:207],a[28:31],v[188:191]
s_add_u32 m0,s58,73752
ds_read_b64_tr_b16 a[236:237],v244 offset:24720
v_mfma_f32_16x16x32_bf16 v[192:195],a[204:207],a[32:35],v[192:195]
buffer_load_dwordx4 v241,s[24:27],s52 offen offset:1024 lds
v_mfma_f32_16x16x32_bf16 v[196:199],a[204:207],a[36:39],v[196:199]
ds_read_b64_tr_b16 a[238:239],v244 offset:26256
ds_read_b64_tr_b16 a[240:241],v245 offset:24712
v_mfma_f32_16x16x32_bf16 v[200:203],a[208:211],a[20:23],v[200:203]
ds_read_b64_tr_b16 a[242:243],v245 offset:26248
v_mfma_f32_16x16x32_bf16 v[204:207],a[208:211],a[24:27],v[204:207]
s_add_u32 m0,s58,73752
ds_read_b64_tr_b16 a[244:245],v245 offset:24720
v_mfma_f32_16x16x32_bf16 v[208:211],a[208:211],a[28:31],v[208:211]
buffer_load_dwordx4 v242,s[24:27],s52 offen offset:2048 lds
v_mfma_f32_16x16x32_bf16 v[212:215],a[208:211],a[32:35],v[212:215]
ds_read_b64_tr_b16 a[246:247],v245 offset:26256
v_mfma_f32_16x16x32_bf16 v[216:219],a[208:211],a[36:39],v[216:219]
s_add_u32 m0,s58,76824
v_mfma_f32_16x16x32_bf16 v[220:223],a[212:215],a[20:23],v[220:223]
s_add_u32 s66,s52,196608
v_mfma_f32_16x16x32_bf16 v[224:227],a[212:215],a[24:27],v[224:227]
buffer_load_dwordx4 v240,s[24:27],s66 offen lds
v_mfma_f32_16x16x32_bf16 v[228:231],a[212:215],a[28:31],v[228:231]
v_mfma_f32_16x16x32_bf16 v[232:235],a[212:215],a[32:35],v[232:235]
v_mfma_f32_16x16x32_bf16 v[236:239],a[212:215],a[36:39],v[236:239]
s_add_u32 m0,s58,76824
v_mfma_f32_16x16x32_bf16 v[0:3],a[216:219],a[20:23],v[0:3]
buffer_load_dwordx4 v241,s[24:27],s66 offen offset:1024 lds
v_mfma_f32_16x16x32_bf16 v[4:7],a[216:219],a[24:27],v[4:7]
v_mfma_f32_16x16x32_bf16 v[8:11],a[216:219],a[28:31],v[8:11]
v_mfma_f32_16x16x32_bf16 v[12:15],a[216:219],a[32:35],v[12:15]
s_add_u32 m0,s58,76824
v_mfma_f32_16x16x32_bf16 v[16:19],a[216:219],a[36:39],v[16:19]
buffer_load_dwordx4 v242,s[24:27],s66 offen offset:2048 lds
v_mfma_f32_16x16x32_bf16 v[20:23],a[220:223],a[20:23],v[20:23]
s_add_u32 s52,s52,393216
v_mfma_f32_16x16x32_bf16 v[24:27],a[220:223],a[24:27],v[24:27]
v_mfma_f32_16x16x32_bf16 v[28:31],a[220:223],a[28:31],v[28:31]
v_mfma_f32_16x16x32_bf16 v[32:35],a[220:223],a[32:35],v[32:35]
v_mfma_f32_16x16x32_bf16 v[36:39],a[220:223],a[36:39],v[36:39]
s_waitcnt vmcnt(20) lgkmcnt(0)
s_barrier
v_mfma_f32_16x16x32_bf16 v[40:43],a[224:227],a[20:23],v[40:43]
s_add_u32 m0,s58,0
ds_read_b128 a[0:3],v248
v_mfma_f32_16x16x32_bf16 v[44:47],a[224:227],a[24:27],v[44:47]
buffer_load_dwordx4 v240,s[24:27],s52 offen lds
v_mfma_f32_16x16x32_bf16 v[48:51],a[224:227],a[28:31],v[48:51]
ds_read_b128 a[4:7],v248 offset:2048
ds_read_b128 a[8:11],v248 offset:4096
v_mfma_f32_16x16x32_bf16 v[52:55],a[224:227],a[32:35],v[52:55]
ds_read_b128 a[12:15],v248 offset:6144
v_mfma_f32_16x16x32_bf16 v[56:59],a[224:227],a[36:39],v[56:59]
s_add_u32 m0,s58,0
ds_read_b128 a[16:19],v248 offset:8192
v_mfma_f32_16x16x32_bf16 v[60:63],a[228:231],a[20:23],v[60:63]
buffer_load_dwordx4 v241,s[24:27],s52 offen offset:1024 lds
v_mfma_f32_16x16x32_bf16 v[64:67],a[228:231],a[24:27],v[64:67]
ds_read_b64_tr_b16 a[200:201],v246 offset:256
ds_read_b64_tr_b16 a[202:203],v246 offset:1792
v_mfma_f32_16x16x32_bf16 v[68:71],a[228:231],a[28:31],v[68:71]
ds_read_b64_tr_b16 a[204:205],v246 offset:264
v_mfma_f32_16x16x32_bf16 v[72:75],a[228:231],a[32:35],v[72:75]
s_add_u32 m0,s58,0
ds_read_b64_tr_b16 a[206:207],v246 offset:1800
v_mfma_f32_16x16x32_bf16 v[76:79],a[228:231],a[36:39],v[76:79]
buffer_load_dwordx4 v242,s[24:27],s52 offen offset:2048 lds
v_mfma_f32_16x16x32_bf16 v[80:83],a[232:235],a[20:23],v[80:83]
ds_read_b64_tr_b16 a[208:209],v247 offset:256
ds_read_b64_tr_b16 a[210:211],v247 offset:1792
v_mfma_f32_16x16x32_bf16 v[84:87],a[232:235],a[24:27],v[84:87]
s_add_u32 m0,s58,3072
ds_read_b64_tr_b16 a[212:213],v247 offset:264
v_mfma_f32_16x16x32_bf16 v[88:91],a[232:235],a[28:31],v[88:91]
s_add_u32 s66,s52,196608
ds_read_b64_tr_b16 a[214:215],v247 offset:1800
v_mfma_f32_16x16x32_bf16 v[92:95],a[232:235],a[32:35],v[92:95]
buffer_load_dwordx4 v240,s[24:27],s66 offen lds
v_mfma_f32_16x16x32_bf16 v[96:99],a[232:235],a[36:39],v[96:99]
ds_read_b64_tr_b16 a[216:217],v246
ds_read_b64_tr_b16 a[218:219],v246 offset:1536
v_mfma_f32_16x16x32_bf16 v[100:103],a[236:239],a[20:23],v[100:103]
ds_read_b64_tr_b16 a[220:221],v246 offset:8
v_mfma_f32_16x16x32_bf16 v[104:107],a[236:239],a[24:27],v[104:107]
s_add_u32 m0,s58,3072
ds_read_b64_tr_b16 a[222:223],v246 offset:1544
v_mfma_f32_16x16x32_bf16 v[108:111],a[236:239],a[28:31],v[108:111]
buffer_load_dwordx4 v241,s[24:27],s66 offen offset:1024 lds
v_mfma_f32_16x16x32_bf16 v[112:115],a[236:239],a[32:35],v[112:115]
v_mfma_f32_16x16x32_bf16 v[116:119],a[236:239],a[36:39],v[116:119]
v_mfma_f32_16x16x32_bf16 v[120:123],a[240:243],a[20:23],v[120:123]
s_add_u32 m0,s58,3072
v_mfma_f32_16x16x32_bf16 v[124:127],a[240:243],a[24:27],v[124:127]
buffer_load_dwordx4 v242,s[24:27],s66 offen offset:2048 lds
v_mfma_f32_16x16x32_bf16 v[128:131],a[240:243],a[28:31],v[128:131]
s_add_u32 s52,s52,393216
v_mfma_f32_16x16x32_bf16 v[132:135],a[240:243],a[32:35],v[132:135]
v_mfma_f32_16x16x32_bf16 v[136:139],a[240:243],a[36:39],v[136:139]
v_mfma_f32_16x16x32_bf16 v[140:143],a[244:247],a[20:23],v[140:143]
v_mfma_f32_16x16x32_bf16 v[144:147],a[244:247],a[24:27],v[144:147]
v_mfma_f32_16x16x32_bf16 v[148:151],a[244:247],a[28:31],v[148:151]
v_mfma_f32_16x16x32_bf16 v[152:155],a[244:247],a[32:35],v[152:155]
v_mfma_f32_16x16x32_bf16 v[156:159],a[244:247],a[36:39],v[156:159]
s_waitcnt lgkmcnt(0)
v_mfma_f32_16x16x32_bf16 v[160:163],a[200:203],a[0:3],v[160:163]
v_mfma_f32_16x16x32_bf16 v[164:167],a[200:203],a[4:7],v[164:167]
buffer_load_dword a250,v254,s[44:47],s74 offen
v_mfma_f32_16x16x32_bf16 v[168:171],a[200:203],a[8:11],v[168:171]
s_add_u32 m0,s59,40960
ds_read_b64_tr_b16 a[224:225],v247
ds_read_b64_tr_b16 a[226:227],v247 offset:1536
v_mfma_f32_16x16x32_bf16 v[172:175],a[200:203],a[12:15],v[172:175]
s_add_u32 s74,s74,786432
buffer_load_dwordx4 v243,s[20:23],s53 offen lds
v_mfma_f32_16x16x32_bf16 v[176:179],a[200:203],a[16:19],v[176:179]
s_add_u32 m0,s59,40960
ds_read_b64_tr_b16 a[228:229],v247 offset:8
ds_read_b64_tr_b16 a[230:231],v247 offset:1544
v_mfma_f32_16x16x32_bf16 v[180:183],a[204:207],a[0:3],v[180:183]
s_add_u32 s67,s53,9216
ds_read_b64_tr_b16 a[232:233],v246 offset:128
v_mfma_f32_16x16x32_bf16 v[184:187],a[204:207],a[4:7],v[184:187]
buffer_load_dwordx4 v243,s[20:23],s67 offen offset:1024 lds
v_mfma_f32_16x16x32_bf16 v[188:191],a[204:207],a[8:11],v[188:191]
s_add_u32 m0,s59,40960
ds_read_b64_tr_b16 a[234:235],v246 offset:1664
ds_read_b64_tr_b16 a[236:237],v246 offset:136
v_mfma_f32_16x16x32_bf16 v[192:195],a[204:207],a[12:15],v[192:195]
s_add_u32 s67,s53,18432
ds_read_b64_tr_b16 a[238:239],v246 offset:1672
v_mfma_f32_16x16x32_bf16 v[196:199],a[204:207],a[16:19],v[196:199]
buffer_load_dwordx4 v243,s[20:23],s67 offen offset:2048 lds
v_mfma_f32_16x16x32_bf16 v[200:203],a[208:211],a[0:3],v[200:203]
s_add_u32 m0,s59,40960
ds_read_b64_tr_b16 a[240:241],v247 offset:128
ds_read_b64_tr_b16 a[242:243],v247 offset:1664
v_mfma_f32_16x16x32_bf16 v[204:207],a[208:211],a[4:7],v[204:207]
s_add_u32 s67,s53,27648
ds_read_b64_tr_b16 a[244:245],v247 offset:136
v_mfma_f32_16x16x32_bf16 v[208:211],a[208:211],a[8:11],v[208:211]
buffer_load_dwordx4 v243,s[20:23],s67 offen offset:3072 lds
v_mfma_f32_16x16x32_bf16 v[212:215],a[208:211],a[12:15],v[212:215]
s_add_u32 m0,s59,45056
ds_read_b64_tr_b16 a[246:247],v247 offset:1672
v_mfma_f32_16x16x32_bf16 v[216:219],a[208:211],a[16:19],v[216:219]
s_add_u32 s67,s53,40960
v_mfma_f32_16x16x32_bf16 v[220:223],a[212:215],a[0:3],v[220:223]
buffer_load_dwordx4 v243,s[20:23],s67 offen lds
v_mfma_f32_16x16x32_bf16 v[224:227],a[212:215],a[4:7],v[224:227]
s_add_u32 s53,s53,128
v_mfma_f32_16x16x32_bf16 v[228:231],a[212:215],a[8:11],v[228:231]
v_mfma_f32_16x16x32_bf16 v[232:235],a[212:215],a[12:15],v[232:235]
v_mfma_f32_16x16x32_bf16 v[236:239],a[212:215],a[16:19],v[236:239]
s_mov_b32 s72,22528
v_mfma_f32_16x16x32_bf16 v[0:3],a[216:219],a[0:3],v[0:3]
buffer_store_dwordx4 a[128:131],v253,s[36:39],s72 offen sc1
v_mfma_f32_16x16x32_bf16 v[4:7],a[216:219],a[4:7],v[4:7]
v_mfma_f32_16x16x32_bf16 v[8:11],a[216:219],a[8:11],v[8:11]
v_mfma_f32_16x16x32_bf16 v[12:15],a[216:219],a[12:15],v[12:15]
v_mfma_f32_16x16x32_bf16 v[16:19],a[216:219],a[16:19],v[16:19]
s_mov_b32 s72,23552
v_mfma_f32_16x16x32_bf16 v[20:23],a[220:223],a[0:3],v[20:23]
buffer_store_dwordx4 a[132:135],v253,s[36:39],s72 offen sc1
v_mfma_f32_16x16x32_bf16 v[24:27],a[220:223],a[4:7],v[24:27]
v_mfma_f32_16x16x32_bf16 v[28:31],a[220:223],a[8:11],v[28:31]
v_mfma_f32_16x16x32_bf16 v[32:35],a[220:223],a[12:15],v[32:35]
v_mfma_f32_16x16x32_bf16 v[36:39],a[220:223],a[16:19],v[36:39]
s_waitcnt vmcnt(14) lgkmcnt(0)
s_barrier
v_mfma_f32_16x16x32_bf16 v[40:43],a[224:227],a[0:3],v[40:43]
ds_read_b128 a[20:23],v249
v_mfma_f32_16x16x32_bf16 v[44:47],a[224:227],a[4:7],v[44:47]
ds_read_b128 a[24:27],v249 offset:2048
v_mfma_f32_16x16x32_bf16 v[48:51],a[224:227],a[8:11],v[48:51]
s_mov_b32 s72,24576
ds_read_b128 a[28:31],v249 offset:4096
v_mfma_f32_16x16x32_bf16 v[52:55],a[224:227],a[12:15],v[52:55]
buffer_store_dwordx4 a[136:139],v253,s[36:39],s72 offen sc1
v_mfma_f32_16x16x32_bf16 v[56:59],a[224:227],a[16:19],v[56:59]
ds_read_b128 a[32:35],v249 offset:6144
ds_read_b128 a[36:39],v249 offset:8192
v_mfma_f32_16x16x32_bf16 v[60:63],a[228:231],a[0:3],v[60:63]
ds_read_b64_tr_b16 a[200:201],v246 offset:24840
v_mfma_f32_16x16x32_bf16 v[64:67],a[228:231],a[4:7],v[64:67]
ds_read_b64_tr_b16 a[202:203],v246 offset:26376
v_mfma_f32_16x16x32_bf16 v[68:71],a[228:231],a[8:11],v[68:71]
ds_read_b64_tr_b16 a[204:205],v246 offset:24848
v_mfma_f32_16x16x32_bf16 v[72:75],a[228:231],a[12:15],v[72:75]
s_mov_b32 s72,30720
ds_read_b64_tr_b16 a[206:207],v246 offset:26384
v_mfma_f32_16x16x32_bf16 v[76:79],a[228:231],a[16:19],v[76:79]
buffer_store_dwordx4 a[140:143],v253,s[36:39],s72 offen sc1
v_mfma_f32_16x16x32_bf16 v[80:83],a[232:235],a[0:3],v[80:83]
ds_read_b64_tr_b16 a[208:209],v247 offset:24840
ds_read_b64_tr_b16 a[210:211],v247 offset:26376
v_mfma_f32_16x16x32_bf16 v[84:87],a[232:235],a[4:7],v[84:87]
ds_read_b64_tr_b16 a[212:213],v247 offset:24848
v_mfma_f32_16x16x32_bf16 v[88:91],a[232:235],a[8:11],v[88:91]
ds_read_b64_tr_b16 a[214:215],v247 offset:26384
v_mfma_f32_16x16x32_bf16 v[92:95],a[232:235],a[12:15],v[92:95]
ds_read_b64_tr_b16 a[216:217],v246 offset:24584
v_mfma_f32_16x16x32_bf16 v[96:99],a[232:235],a[16:19],v[96:99]
s_mov_b32 s72,31744
ds_read_b64_tr_b16 a[218:219],v246 offset:26120
v_mfma_f32_16x16x32_bf16 v[100:103],a[236:239],a[0:3],v[100:103]
buffer_store_dwordx4 a[144:147],v253,s[36:39],s72 offen sc1
v_mfma_f32_16x16x32_bf16 v[104:107],a[236:239],a[4:7],v[104:107]
ds_read_b64_tr_b16 a[220:221],v246 offset:24592
ds_read_b64_tr_b16 a[222:223],v246 offset:26128
v_mfma_f32_16x16x32_bf16 v[108:111],a[236:239],a[8:11],v[108:111]
v_mfma_f32_16x16x32_bf16 v[112:115],a[236:239],a[12:15],v[112:115]
v_mfma_f32_16x16x32_bf16 v[116:119],a[236:239],a[16:19],v[116:119]
v_mfma_f32_16x16x32_bf16 v[120:123],a[240:243],a[0:3],v[120:123]
s_mov_b32 s72,32768
v_mfma_f32_16x16x32_bf16 v[124:127],a[240:243],a[4:7],v[124:127]
buffer_store_dwordx4 a[148:151],v253,s[36:39],s72 offen sc1
v_mfma_f32_16x16x32_bf16 v[128:131],a[240:243],a[8:11],v[128:131]
v_mfma_f32_16x16x32_bf16 v[132:135],a[240:243],a[12:15],v[132:135]
v_mfma_f32_16x16x32_bf16 v[136:139],a[240:243],a[16:19],v[136:139]
v_mfma_f32_16x16x32_bf16 v[140:143],a[244:247],a[0:3],v[140:143]
v_mfma_f32_16x16x32_bf16 v[144:147],a[244:247],a[4:7],v[144:147]
v_mfma_f32_16x16x32_bf16 v[148:151],a[244:247],a[8:11],v[148:151]
v_mfma_f32_16x16x32_bf16 v[152:155],a[244:247],a[12:15],v[152:155]
v_mfma_f32_16x16x32_bf16 v[156:159],a[244:247],a[16:19],v[156:159]
s_waitcnt lgkmcnt(0)
v_mfma_f32_16x16x32_bf16 v[160:163],a[200:203],a[20:23],v[160:163]
v_mfma_f32_16x16x32_bf16 v[164:167],a[200:203],a[24:27],v[164:167]
ds_read_b64_tr_b16 a[224:225],v247 offset:24584
v_mfma_f32_16x16x32_bf16 v[168:171],a[200:203],a[28:31],v[168:171]
ds_read_b64_tr_b16 a[226:227],v247 offset:26120
v_mfma_f32_16x16x32_bf16 v[172:175],a[200:203],a[32:35],v[172:175]
s_add_u32 m0,s58,24584
ds_read_b64_tr_b16 a[228:229],v247 offset:24592
v_mfma_f32_16x16x32_bf16 v[176:179],a[200:203],a[36:39],v[176:179]
buffer_load_dwordx4 v240,s[24:27],s52 offen lds
v_mfma_f32_16x16x32_bf16 v[180:183],a[204:207],a[20:23],v[180:183]
ds_read_b64_tr_b16 a[230:231],v247 offset:26128
ds_read_b64_tr_b16 a[232:233],v246 offset:24712
v_mfma_f32_16x16x32_bf16 v[184:187],a[204:207],a[24:27],v[184:187]
ds_read_b64_tr_b16 a[234:235],v246 offset:26248
v_mfma_f32_16x16x32_bf16 v[188:191],a[204:207],a[28:31],v[188:191]
s_add_u32 m0,s58,24584
ds_read_b64_tr_b16 a[236:237],v246 offset:24720
v_mfma_f32_16x16x32_bf16 v[192:195],a[204:207],a[32:35],v[192:195]
buffer_load_dwordx4 v241,s[24:27],s52 offen offset:1024 lds
v_mfma_f32_16x16x32_bf16 v[196:199],a[204:207],a[36:39],v[196:199]
ds_read_b64_tr_b16 a[238:239],v246 offset:26256
ds_read_b64_tr_b16 a[240:241],v247 offset:24712
v_mfma_f32_16x16x32_bf16 v[200:203],a[208:211],a[20:23],v[200:203]
ds_read_b64_tr_b16 a[242:243],v247 offset:26248
v_mfma_f32_16x16x32_bf16 v[204:207],a[208:211],a[24:27],v[204:207]
s_add_u32 m0,s58,24584
ds_read_b64_tr_b16 a[244:245],v247 offset:24720
v_mfma_f32_16x16x32_bf16 v[208:211],a[208:211],a[28:31],v[208:211]
buffer_load_dwordx4 v242,s[24:27],s52 offen offset:2048 lds
v_mfma_f32_16x16x32_bf16 v[212:215],a[208:211],a[32:35],v[212:215]
ds_read_b64_tr_b16 a[246:247],v247 offset:26256
v_mfma_f32_16x16x32_bf16 v[216:219],a[208:211],a[36:39],v[216:219]
s_add_u32 m0,s58,27656
v_mfma_f32_16x16x32_bf16 v[220:223],a[212:215],a[20:23],v[220:223]
s_add_u32 s66,s52,196608
v_mfma_f32_16x16x32_bf16 v[224:227],a[212:215],a[24:27],v[224:227]
buffer_load_dwordx4 v240,s[24:27],s66 offen lds
v_mfma_f32_16x16x32_bf16 v[228:231],a[212:215],a[28:31],v[228:231]
v_mfma_f32_16x16x32_bf16 v[232:235],a[212:215],a[32:35],v[232:235]
v_mfma_f32_16x16x32_bf16 v[236:239],a[212:215],a[36:39],v[236:239]
s_add_u32 m0,s58,27656
v_mfma_f32_16x16x32_bf16 v[0:3],a[216:219],a[20:23],v[0:3]
buffer_load_dwordx4 v241,s[24:27],s66 offen offset:1024 lds
v_mfma_f32_16x16x32_bf16 v[4:7],a[216:219],a[24:27],v[4:7]
v_mfma_f32_16x16x32_bf16 v[8:11],a[216:219],a[28:31],v[8:11]
v_mfma_f32_16x16x32_bf16 v[12:15],a[216:219],a[32:35],v[12:15]
s_add_u32 m0,s58,27656
v_mfma_f32_16x16x32_bf16 v[16:19],a[216:219],a[36:39],v[16:19]
buffer_load_dwordx4 v242,s[24:27],s66 offen offset:2048 lds
v_mfma_f32_16x16x32_bf16 v[20:23],a[220:223],a[20:23],v[20:23]
s_add_u32 s52,s52,393216
v_mfma_f32_16x16x32_bf16 v[24:27],a[220:223],a[24:27],v[24:27]
v_mfma_f32_16x16x32_bf16 v[28:31],a[220:223],a[28:31],v[28:31]
v_mfma_f32_16x16x32_bf16 v[32:35],a[220:223],a[32:35],v[32:35]
v_mfma_f32_16x16x32_bf16 v[36:39],a[220:223],a[36:39],v[36:39]
s_waitcnt vmcnt(18) lgkmcnt(0)
s_barrier
v_mfma_f32_16x16x32_bf16 v[40:43],a[224:227],a[20:23],v[40:43]
s_add_u32 m0,s58,49168
ds_read_b128 a[0:3],v248 offset:20480
v_mfma_f32_16x16x32_bf16 v[44:47],a[224:227],a[24:27],v[44:47]
buffer_load_dwordx4 v240,s[24:27],s52 offen lds
v_mfma_f32_16x16x32_bf16 v[48:51],a[224:227],a[28:31],v[48:51]
ds_read_b128 a[4:7],v248 offset:22528
ds_read_b128 a[8:11],v248 offset:24576
v_mfma_f32_16x16x32_bf16 v[52:55],a[224:227],a[32:35],v[52:55]
ds_read_b128 a[12:15],v248 offset:26624
v_mfma_f32_16x16x32_bf16 v[56:59],a[224:227],a[36:39],v[56:59]
s_add_u32 m0,s58,49168
ds_read_b128 a[16:19],v248 offset:28672
v_mfma_f32_16x16x32_bf16 v[60:63],a[228:231],a[20:23],v[60:63]
buffer_load_dwordx4 v241,s[24:27],s52 offen offset:1024 lds
v_mfma_f32_16x16x32_bf16 v[64:67],a[228:231],a[24:27],v[64:67]
ds_read_b64_tr_b16 a[200:201],v244 offset:256
ds_read_b64_tr_b16 a[202:203],v244 offset:1792
v_mfma_f32_16x16x32_bf16 v[68:71],a[228:231],a[28:31],v[68:71]
ds_read_b64_tr_b16 a[204:205],v244 offset:264
v_mfma_f32_16x16x32_bf16 v[72:75],a[228:231],a[32:35],v[72:75]
s_add_u32 m0,s58,49168
ds_read_b64_tr_b16 a[206:207],v244 offset:1800
v_mfma_f32_16x16x32_bf16 v[76:79],a[228:231],a[36:39],v[76:79]
buffer_load_dwordx4 v242,s[24:27],s52 offen offset:2048 lds
v_mfma_f32_16x16x32_bf16 v[80:83],a[232:235],a[20:23],v[80:83]
ds_read_b64_tr_b16 a[208:209],v245 offset:256
ds_read_b64_tr_b16 a[210:211],v245 offset:1792
v_mfma_f32_16x16x32_bf16 v[84:87],a[232:235],a[24:27],v[84:87]
s_add_u32 m0,s58,52240
ds_read_b64_tr_b16 a[212:213],v245 offset:264
v_mfma_f32_16x16x32_bf16 v[88:91],a[232:235],a[28:31],v[88:91]
s_add_u32 s66,s52,196608
ds_read_b64_tr_b16 a[214:215],v245 offset:1800
v_mfma_f32_16x16x32_bf16 v[92:95],a[232:235],a[32:35],v[92:95]
buffer_load_dwordx4 v240,s[24:27],s66 offen lds
v_mfma_f32_16x16x32_bf16 v[96:99],a[232:235],a[36:39],v[96:99]
ds_read_b64_tr_b16 a[216:217],v244
ds_read_b64_tr_b16 a[218:219],v244 offset:1536
v_mfma_f32_16x16x32_bf16 v[100:103],a[236:239],a[20:23],v[100:103]
ds_read_b64_tr_b16 a[220:221],v244 offset:8
v_mfma_f32_16x16x32_bf16 v[104:107],a[236:239],a[24:27],v[104:107]
s_add_u32 m0,s58,52240
ds_read_b64_tr_b16 a[222:223],v244 offset:1544
v_mfma_f32_16x16x32_bf16 v[108:111],a[236:239],a[28:31],v[108:111]
buffer_load_dwordx4 v241,s[24:27],s66 offen offset:1024 lds
v_mfma_f32_16x16x32_bf16 v[112:115],a[236:239],a[32:35],v[112:115]
v_mfma_f32_16x16x32_bf16 v[116:119],a[236:239],a[36:39],v[116:119]
v_mfma_f32_16x16x32_bf16 v[120:123],a[240:243],a[20:23],v[120:123]
s_add_u32 m0,s58,52240
v_mfma_f32_16x16x32_bf16 v[124:127],a[240:243],a[24:27],v[124:127]
buffer_load_dwordx4 v242,s[24:27],s66 offen offset:2048 lds
v_mfma_f32_16x16x32_bf16 v[128:131],a[240:243],a[28:31],v[128:131]
s_add_u32 s52,s52,393216
v_mfma_f32_16x16x32_bf16 v[132:135],a[240:243],a[32:35],v[132:135]
v_mfma_f32_16x16x32_bf16 v[136:139],a[240:243],a[36:39],v[136:139]
v_mfma_f32_16x16x32_bf16 v[140:143],a[244:247],a[20:23],v[140:143]
v_mfma_f32_16x16x32_bf16 v[144:147],a[244:247],a[24:27],v[144:147]
v_mfma_f32_16x16x32_bf16 v[148:151],a[244:247],a[28:31],v[148:151]
v_mfma_f32_16x16x32_bf16 v[152:155],a[244:247],a[32:35],v[152:155]
v_mfma_f32_16x16x32_bf16 v[156:159],a[244:247],a[36:39],v[156:159]
s_waitcnt lgkmcnt(0)
v_mfma_f32_16x16x32_bf16 v[160:163],a[200:203],a[0:3],v[160:163]
v_mfma_f32_16x16x32_bf16 v[164:167],a[200:203],a[4:7],v[164:167]
buffer_load_dword a250,v254,s[44:47],s74 offen
v_mfma_f32_16x16x32_bf16 v[168:171],a[200:203],a[8:11],v[168:171]
s_add_u32 m0,s59,0
ds_read_b64_tr_b16 a[224:225],v245
ds_read_b64_tr_b16 a[226:227],v245 offset:1536
v_mfma_f32_16x16x32_bf16 v[172:175],a[200:203],a[12:15],v[172:175]
s_add_u32 s74,s74,786432
buffer_load_dwordx4 v243,s[20:23],s53 offen lds
v_mfma_f32_16x16x32_bf16 v[176:179],a[200:203],a[16:19],v[176:179]
s_add_u32 m0,s59,0
ds_read_b64_tr_b16 a[228:229],v245 offset:8
ds_read_b64_tr_b16 a[230:231],v245 offset:1544
v_mfma_f32_16x16x32_bf16 v[180:183],a[204:207],a[0:3],v[180:183]
s_add_u32 s67,s53,9216
ds_read_b64_tr_b16 a[232:233],v244 offset:128
v_mfma_f32_16x16x32_bf16 v[184:187],a[204:207],a[4:7],v[184:187]
buffer_load_dwordx4 v243,s[20:23],s67 offen offset:1024 lds
v_mfma_f32_16x16x32_bf16 v[188:191],a[204:207],a[8:11],v[188:191]
s_add_u32 m0,s59,0
ds_read_b64_tr_b16 a[234:235],v244 offset:1664
ds_read_b64_tr_b16 a[236:237],v244 offset:136
v_mfma_f32_16x16x32_bf16 v[192:195],a[204:207],a[12:15],v[192:195]
s_add_u32 s67,s53,18432
ds_read_b64_tr_b16 a[238:239],v244 offset:1672
v_mfma_f32_16x16x32_bf16 v[196:199],a[204:207],a[16:19],v[196:199]
buffer_load_dwordx4 v243,s[20:23],s67 offen offset:2048 lds
v_mfma_f32_16x16x32_bf16 v[200:203],a[208:211],a[0:3],v[200:203]
s_add_u32 m0,s59,0
ds_read_b64_tr_b16 a[240:241],v245 offset:128
ds_read_b64_tr_b16 a[242:243],v245 offset:1664
v_mfma_f32_16x16x32_bf16 v[204:207],a[208:211],a[4:7],v[204:207]
s_add_u32 s67,s53,27648
ds_read_b64_tr_b16 a[244:245],v245 offset:136
v_mfma_f32_16x16x32_bf16 v[208:211],a[208:211],a[8:11],v[208:211]
buffer_load_dwordx4 v243,s[20:23],s67 offen offset:3072 lds
v_mfma_f32_16x16x32_bf16 v[212:215],a[208:211],a[12:15],v[212:215]
s_add_u32 m0,s59,4096
ds_read_b64_tr_b16 a[246:247],v245 offset:1672
v_mfma_f32_16x16x32_bf16 v[216:219],a[208:211],a[16:19],v[216:219]
s_add_u32 s67,s53,40960
v_mfma_f32_16x16x32_bf16 v[220:223],a[212:215],a[0:3],v[220:223]
buffer_load_dwordx4 v243,s[20:23],s67 offen lds
v_mfma_f32_16x16x32_bf16 v[224:227],a[212:215],a[4:7],v[224:227]
s_add_u32 s53,s53,128
v_mfma_f32_16x16x32_bf16 v[228:231],a[212:215],a[8:11],v[228:231]
v_mfma_f32_16x16x32_bf16 v[232:235],a[212:215],a[12:15],v[232:235]
v_mfma_f32_16x16x32_bf16 v[236:239],a[212:215],a[16:19],v[236:239]
s_mov_b32 s72,33792
v_mfma_f32_16x16x32_bf16 v[0:3],a[216:219],a[0:3],v[0:3]
buffer_store_dwordx4 a[152:155],v253,s[36:39],s72 offen sc1
v_mfma_f32_16x16x32_bf16 v[4:7],a[216:219],a[4:7],v[4:7]
v_mfma_f32_16x16x32_bf16 v[8:11],a[216:219],a[8:11],v[8:11]
v_mfma_f32_16x16x32_bf16 v[12:15],a[216:219],a[12:15],v[12:15]
v_mfma_f32_16x16x32_bf16 v[16:19],a[216:219],a[16:19],v[16:19]
s_mov_b32 s72,34816
v_mfma_f32_16x16x32_bf16 v[20:23],a[220:223],a[0:3],v[20:23]
buffer_store_dwordx4 a[156:159],v253,s[36:39],s72 offen sc1
v_mfma_f32_16x16x32_bf16 v[24:27],a[220:223],a[4:7],v[24:27]
v_mfma_f32_16x16x32_bf16 v[28:31],a[220:223],a[8:11],v[28:31]
v_mfma_f32_16x16x32_bf16 v[32:35],a[220:223],a[12:15],v[32:35]
v_mfma_f32_16x16x32_bf16 v[36:39],a[220:223],a[16:19],v[36:39]
s_waitcnt vmcnt(14) lgkmcnt(0)
s_barrier
v_mfma_f32_16x16x32_bf16 v[40:43],a[224:227],a[0:3],v[40:43]
ds_read_b128 a[20:23],v249 offset:20480
v_mfma_f32_16x16x32_bf16 v[44:47],a[224:227],a[4:7],v[44:47]
ds_read_b128 a[24:27],v249 offset:22528
v_mfma_f32_16x16x32_bf16 v[48:51],a[224:227],a[8:11],v[48:51]
s_mov_b32 s72,40960
ds_read_b128 a[28:31],v249 offset:24576
v_mfma_f32_16x16x32_bf16 v[52:55],a[224:227],a[12:15],v[52:55]
buffer_store_dwordx4 a[160:163],v253,s[36:39],s72 offen sc1
v_mfma_f32_16x16x32_bf16 v[56:59],a[224:227],a[16:19],v[56:59]
ds_read_b128 a[32:35],v249 offset:26624
ds_read_b128 a[36:39],v249 offset:28672
v_mfma_f32_16x16x32_bf16 v[60:63],a[228:231],a[0:3],v[60:63]
ds_read_b64_tr_b16 a[200:201],v244 offset:24840
v_mfma_f32_16x16x32_bf16 v[64:67],a[228:231],a[4:7],v[64:67]
ds_read_b64_tr_b16 a[202:203],v244 offset:26376
v_mfma_f32_16x16x32_bf16 v[68:71],a[228:231],a[8:11],v[68:71]
ds_read_b64_tr_b16 a[204:205],v244 offset:24848
v_mfma_f32_16x16x32_bf16 v[72:75],a[228:231],a[12:15],v[72:75]
s_mov_b32 s72,41984
ds_read_b64_tr_b16 a[206:207],v244 offset:26384
v_mfma_f32_16x16x32_bf16 v[76:79],a[228:231],a[16:19],v[76:79]
buffer_store_dwordx4 a[164:167],v253,s[36:39],s72 offen sc1
v_mfma_f32_16x16x32_bf16 v[80:83],a[232:235],a[0:3],v[80:83]
ds_read_b64_tr_b16 a[208:209],v245 offset:24840
ds_read_b64_tr_b16 a[210:211],v245 offset:26376
v_mfma_f32_16x16x32_bf16 v[84:87],a[232:235],a[4:7],v[84:87]
ds_read_b64_tr_b16 a[212:213],v245 offset:24848
v_mfma_f32_16x16x32_bf16 v[88:91],a[232:235],a[8:11],v[88:91]
ds_read_b64_tr_b16 a[214:215],v245 offset:26384
v_mfma_f32_16x16x32_bf16 v[92:95],a[232:235],a[12:15],v[92:95]
ds_read_b64_tr_b16 a[216:217],v244 offset:24584
v_mfma_f32_16x16x32_bf16 v[96:99],a[232:235],a[16:19],v[96:99]
s_mov_b32 s72,43008
ds_read_b64_tr_b16 a[218:219],v244 offset:26120
v_mfma_f32_16x16x32_bf16 v[100:103],a[236:239],a[0:3],v[100:103]
buffer_store_dwordx4 a[168:171],v253,s[36:39],s72 offen sc1
v_mfma_f32_16x16x32_bf16 v[104:107],a[236:239],a[4:7],v[104:107]
ds_read_b64_tr_b16 a[220:221],v244 offset:24592
ds_read_b64_tr_b16 a[222:223],v244 offset:26128
v_mfma_f32_16x16x32_bf16 v[108:111],a[236:239],a[8:11],v[108:111]
v_mfma_f32_16x16x32_bf16 v[112:115],a[236:239],a[12:15],v[112:115]
v_mfma_f32_16x16x32_bf16 v[116:119],a[236:239],a[16:19],v[116:119]
v_mfma_f32_16x16x32_bf16 v[120:123],a[240:243],a[0:3],v[120:123]
s_mov_b32 s72,44032
v_mfma_f32_16x16x32_bf16 v[124:127],a[240:243],a[4:7],v[124:127]
buffer_store_dwordx4 a[172:175],v253,s[36:39],s72 offen sc1
v_mfma_f32_16x16x32_bf16 v[128:131],a[240:243],a[8:11],v[128:131]
v_mfma_f32_16x16x32_bf16 v[132:135],a[240:243],a[12:15],v[132:135]
v_mfma_f32_16x16x32_bf16 v[136:139],a[240:243],a[16:19],v[136:139]
v_mfma_f32_16x16x32_bf16 v[140:143],a[244:247],a[0:3],v[140:143]
v_mfma_f32_16x16x32_bf16 v[144:147],a[244:247],a[4:7],v[144:147]
v_mfma_f32_16x16x32_bf16 v[148:151],a[244:247],a[8:11],v[148:151]
v_mfma_f32_16x16x32_bf16 v[152:155],a[244:247],a[12:15],v[152:155]
v_mfma_f32_16x16x32_bf16 v[156:159],a[244:247],a[16:19],v[156:159]
s_waitcnt lgkmcnt(0)
v_mfma_f32_16x16x32_bf16 v[160:163],a[200:203],a[20:23],v[160:163]
v_mfma_f32_16x16x32_bf16 v[164:167],a[200:203],a[24:27],v[164:167]
ds_read_b64_tr_b16 a[224:225],v245 offset:24584
v_mfma_f32_16x16x32_bf16 v[168:171],a[200:203],a[28:31],v[168:171]
ds_read_b64_tr_b16 a[226:227],v245 offset:26120
v_mfma_f32_16x16x32_bf16 v[172:175],a[200:203],a[32:35],v[172:175]
s_add_u32 m0,s58,73752
ds_read_b64_tr_b16 a[228:229],v245 offset:24592
v_mfma_f32_16x16x32_bf16 v[176:179],a[200:203],a[36:39],v[176:179]
buffer_load_dwordx4 v240,s[24:27],s52 offen lds
v_mfma_f32_16x16x32_bf16 v[180:183],a[204:207],a[20:23],v[180:183]
ds_read_b64_tr_b16 a[230:231],v245 offset:26128
ds_read_b64_tr_b16 a[232:233],v244 offset:24712
v_mfma_f32_16x16x32_bf16 v[184:187],a[204:207],a[24:27],v[184:187]
ds_read_b64_tr_b16 a[234:235],v244 offset:26248
v_mfma_f32_16x16x32_bf16 v[188:191],a[204:207],a[28:31],v[188:191]
s_add_u32 m0,s58,73752
ds_read_b64_tr_b16 a[236:237],v244 offset:24720
v_mfma_f32_16x16x32_bf16 v[192:195],a[204:207],a[32:35],v[192:195]
buffer_load_dwordx4 v241,s[24:27],s52 offen offset:1024 lds
v_mfma_f32_16x16x32_bf16 v[196:199],a[204:207],a[36:39],v[196:199]
ds_read_b64_tr_b16 a[238:239],v244 offset:26256
ds_read_b64_tr_b16 a[240:241],v245 offset:24712
v_mfma_f32_16x16x32_bf16 v[200:203],a[208:211],a[20:23],v[200:203]
ds_read_b64_tr_b16 a[242:243],v245 offset:26248
v_mfma_f32_16x16x32_bf16 v[204:207],a[208:211],a[24:27],v[204:207]
s_add_u32 m0,s58,73752
ds_read_b64_tr_b16 a[244:245],v245 offset:24720
v_mfma_f32_16x16x32_bf16 v[208:211],a[208:211],a[28:31],v[208:211]
buffer_load_dwordx4 v242,s[24:27],s52 offen offset:2048 lds
v_mfma_f32_16x16x32_bf16 v[212:215],a[208:211],a[32:35],v[212:215]
ds_read_b64_tr_b16 a[246:247],v245 offset:26256
v_mfma_f32_16x16x32_bf16 v[216:219],a[208:211],a[36:39],v[216:219]
s_add_u32 m0,s58,76824
v_mfma_f32_16x16x32_bf16 v[220:223],a[212:215],a[20:23],v[220:223]
s_add_u32 s66,s52,196608
v_mfma_f32_16x16x32_bf16 v[224:227],a[212:215],a[24:27],v[224:227]
buffer_load_dwordx4 v240,s[24:27],s66 offen lds
v_mfma_f32_16x16x32_bf16 v[228:231],a[212:215],a[28:31],v[228:231]
v_mfma_f32_16x16x32_bf16 v[232:235],a[212:215],a[32:35],v[232:235]
v_mfma_f32_16x16x32_bf16 v[236:239],a[212:215],a[36:39],v[236:239]
s_add_u32 m0,s58,76824
v_mfma_f32_16x16x32_bf16 v[0:3],a[216:219],a[20:23],v[0:3]
buffer_load_dwordx4 v241,s[24:27],s66 offen offset:1024 lds
v_mfma_f32_16x16x32_bf16 v[4:7],a[216:219],a[24:27],v[4:7]
v_mfma_f32_16x16x32_bf16 v[8:11],a[216:219],a[28:31],v[8:11]
v_mfma_f32_16x16x32_bf16 v[12:15],a[216:219],a[32:35],v[12:15]
s_add_u32 m0,s58,76824
v_mfma_f32_16x16x32_bf16 v[16:19],a[216:219],a[36:39],v[16:19]
buffer_load_dwordx4 v242,s[24:27],s66 offen offset:2048 lds
v_mfma_f32_16x16x32_bf16 v[20:23],a[220:223],a[20:23],v[20:23]
s_add_u32 s52,s52,393216
v_mfma_f32_16x16x32_bf16 v[24:27],a[220:223],a[24:27],v[24:27]
v_mfma_f32_16x16x32_bf16 v[28:31],a[220:223],a[28:31],v[28:31]
v_mfma_f32_16x16x32_bf16 v[32:35],a[220:223],a[32:35],v[32:35]
v_mfma_f32_16x16x32_bf16 v[36:39],a[220:223],a[36:39],v[36:39]
s_waitcnt vmcnt(18) lgkmcnt(0)
s_barrier
v_mfma_f32_16x16x32_bf16 v[40:43],a[224:227],a[20:23],v[40:43]
s_add_u32 m0,s58,0
ds_read_b128 a[0:3],v248 offset:40960
v_mfma_f32_16x16x32_bf16 v[44:47],a[224:227],a[24:27],v[44:47]
buffer_load_dwordx4 v240,s[24:27],s52 offen lds
v_mfma_f32_16x16x32_bf16 v[48:51],a[224:227],a[28:31],v[48:51]
ds_read_b128 a[4:7],v248 offset:43008
ds_read_b128 a[8:11],v248 offset:45056
v_mfma_f32_16x16x32_bf16 v[52:55],a[224:227],a[32:35],v[52:55]
ds_read_b128 a[12:15],v248 offset:47104
v_mfma_f32_16x16x32_bf16 v[56:59],a[224:227],a[36:39],v[56:59]
s_add_u32 m0,s58,0
ds_read_b128 a[16:19],v248 offset:49152
v_mfma_f32_16x16x32_bf16 v[60:63],a[228:231],a[20:23],v[60:63]
buffer_load_dwordx4 v241,s[24:27],s52 offen offset:1024 lds
v_mfma_f32_16x16x32_bf16 v[64:67],a[228:231],a[24:27],v[64:67]
ds_read_b64_tr_b16 a[200:201],v246 offset:256
ds_read_b64_tr_b16 a[202:203],v246 offset:1792
v_mfma_f32_16x16x32_bf16 v[68:71],a[228:231],a[28:31],v[68:71]
ds_read_b64_tr_b16 a[204:205],v246 offset:264
v_mfma_f32_16x16x32_bf16 v[72:75],a[228:231],a[32:35],v[72:75]
s_add_u32 m0,s58,0
ds_read_b64_tr_b16 a[206:207],v246 offset:1800
v_mfma_f32_16x16x32_bf16 v[76:79],a[228:231],a[36:39],v[76:79]
buffer_load_dwordx4 v242,s[24:27],s52 offen offset:2048 lds
v_mfma_f32_16x16x32_bf16 v[80:83],a[232:235],a[20:23],v[80:83]
ds_read_b64_tr_b16 a[208:209],v247 offset:256
ds_read_b64_tr_b16 a[210:211],v247 offset:1792
v_mfma_f32_16x16x32_bf16 v[84:87],a[232:235],a[24:27],v[84:87]
s_add_u32 m0,s58,3072
ds_read_b64_tr_b16 a[212:213],v247 offset:264
v_mfma_f32_16x16x32_bf16 v[88:91],a[232:235],a[28:31],v[88:91]
s_add_u32 s66,s52,196608
ds_read_b64_tr_b16 a[214:215],v247 offset:1800
v_mfma_f32_16x16x32_bf16 v[92:95],a[232:235],a[32:35],v[92:95]
buffer_load_dwordx4 v240,s[24:27],s66 offen lds
v_mfma_f32_16x16x32_bf16 v[96:99],a[232:235],a[36:39],v[96:99]
ds_read_b64_tr_b16 a[216:217],v246
ds_read_b64_tr_b16 a[218:219],v246 offset:1536
v_mfma_f32_16x16x32_bf16 v[100:103],a[236:239],a[20:23],v[100:103]
ds_read_b64_tr_b16 a[220:221],v246 offset:8
v_mfma_f32_16x16x32_bf16 v[104:107],a[236:239],a[24:27],v[104:107]
s_add_u32 m0,s58,3072
ds_read_b64_tr_b16 a[222:223],v246 offset:1544
v_mfma_f32_16x16x32_bf16 v[108:111],a[236:239],a[28:31],v[108:111]
buffer_load_dwordx4 v241,s[24:27],s66 offen offset:1024 lds
v_mfma_f32_16x16x32_bf16 v[112:115],a[236:239],a[32:35],v[112:115]
v_mfma_f32_16x16x32_bf16 v[116:119],a[236:239],a[36:39],v[116:119]
v_mfma_f32_16x16x32_bf16 v[120:123],a[240:243],a[20:23],v[120:123]
s_add_u32 m0,s58,3072
v_mfma_f32_16x16x32_bf16 v[124:127],a[240:243],a[24:27],v[124:127]
buffer_load_dwordx4 v242,s[24:27],s66 offen offset:2048 lds
v_mfma_f32_16x16x32_bf16 v[128:131],a[240:243],a[28:31],v[128:131]
s_add_u32 s52,s52,393216
v_mfma_f32_16x16x32_bf16 v[132:135],a[240:243],a[32:35],v[132:135]
v_mfma_f32_16x16x32_bf16 v[136:139],a[240:243],a[36:39],v[136:139]
v_mfma_f32_16x16x32_bf16 v[140:143],a[244:247],a[20:23],v[140:143]
v_mfma_f32_16x16x32_bf16 v[144:147],a[244:247],a[24:27],v[144:147]
v_mfma_f32_16x16x32_bf16 v[148:151],a[244:247],a[28:31],v[148:151]
v_mfma_f32_16x16x32_bf16 v[152:155],a[244:247],a[32:35],v[152:155]
v_mfma_f32_16x16x32_bf16 v[156:159],a[244:247],a[36:39],v[156:159]
s_waitcnt lgkmcnt(0)
v_mfma_f32_16x16x32_bf16 v[160:163],a[200:203],a[0:3],v[160:163]
v_mfma_f32_16x16x32_bf16 v[164:167],a[200:203],a[4:7],v[164:167]
buffer_load_dword a250,v254,s[44:47],s74 offen
v_mfma_f32_16x16x32_bf16 v[168:171],a[200:203],a[8:11],v[168:171]
s_add_u32 m0,s59,20480
ds_read_b64_tr_b16 a[224:225],v247
ds_read_b64_tr_b16 a[226:227],v247 offset:1536
v_mfma_f32_16x16x32_bf16 v[172:175],a[200:203],a[12:15],v[172:175]
s_add_u32 s74,s74,786432
buffer_load_dwordx4 v243,s[20:23],s53 offen lds
v_mfma_f32_16x16x32_bf16 v[176:179],a[200:203],a[16:19],v[176:179]
s_add_u32 m0,s59,20480
ds_read_b64_tr_b16 a[228:229],v247 offset:8
ds_read_b64_tr_b16 a[230:231],v247 offset:1544
v_mfma_f32_16x16x32_bf16 v[180:183],a[204:207],a[0:3],v[180:183]
s_add_u32 s67,s53,9216
ds_read_b64_tr_b16 a[232:233],v246 offset:128
v_mfma_f32_16x16x32_bf16 v[184:187],a[204:207],a[4:7],v[184:187]
buffer_load_dwordx4 v243,s[20:23],s67 offen offset:1024 lds
v_mfma_f32_16x16x32_bf16 v[188:191],a[204:207],a[8:11],v[188:191]
s_add_u32 m0,s59,20480
ds_read_b64_tr_b16 a[234:235],v246 offset:1664
ds_read_b64_tr_b16 a[236:237],v246 offset:136
v_mfma_f32_16x16x32_bf16 v[192:195],a[204:207],a[12:15],v[192:195]
s_add_u32 s67,s53,18432
ds_read_b64_tr_b16 a[238:239],v246 offset:1672
v_mfma_f32_16x16x32_bf16 v[196:199],a[204:207],a[16:19],v[196:199]
buffer_load_dwordx4 v243,s[20:23],s67 offen offset:2048 lds
v_mfma_f32_16x16x32_bf16 v[200:203],a[208:211],a[0:3],v[200:203]
s_add_u32 m0,s59,20480
ds_read_b64_tr_b16 a[240:241],v247 offset:128
ds_read_b64_tr_b16 a[242:243],v247 offset:1664
v_mfma_f32_16x16x32_bf16 v[204:207],a[208:211],a[4:7],v[204:207]
s_add_u32 s67,s53,27648
ds_read_b64_tr_b16 a[244:245],v247 offset:136
v_mfma_f32_16x16x32_bf16 v[208:211],a[208:211],a[8:11],v[208:211]
buffer_load_dwordx4 v243,s[20:23],s67 offen offset:3072 lds
v_mfma_f32_16x16x32_bf16 v[212:215],a[208:211],a[12:15],v[212:215]
s_add_u32 m0,s59,24576
ds_read_b64_tr_b16 a[246:247],v247 offset:1672
v_mfma_f32_16x16x32_bf16 v[216:219],a[208:211],a[16:19],v[216:219]
s_add_u32 s67,s53,40960
v_mfma_f32_16x16x32_bf16 v[220:223],a[212:215],a[0:3],v[220:223]
buffer_load_dwordx4 v243,s[20:23],s67 offen lds
v_mfma_f32_16x16x32_bf16 v[224:227],a[212:215],a[4:7],v[224:227]
s_add_u32 s53,s53,128
v_mfma_f32_16x16x32_bf16 v[228:231],a[212:215],a[8:11],v[228:231]
v_mfma_f32_16x16x32_bf16 v[232:235],a[212:215],a[12:15],v[232:235]
v_mfma_f32_16x16x32_bf16 v[236:239],a[212:215],a[16:19],v[236:239]
s_mov_b32 s72,45056
v_mfma_f32_16x16x32_bf16 v[0:3],a[216:219],a[0:3],v[0:3]
buffer_store_dwordx4 a[176:179],v253,s[36:39],s72 offen sc1
v_mfma_f32_16x16x32_bf16 v[4:7],a[216:219],a[4:7],v[4:7]
v_mfma_f32_16x16x32_bf16 v[8:11],a[216:219],a[8:11],v[8:11]
v_mfma_f32_16x16x32_bf16 v[12:15],a[216:219],a[12:15],v[12:15]
v_mfma_f32_16x16x32_bf16 v[16:19],a[216:219],a[16:19],v[16:19]
s_mov_b32 s72,51200
v_mfma_f32_16x16x32_bf16 v[20:23],a[220:223],a[0:3],v[20:23]
buffer_store_dwordx4 a[180:183],v253,s[36:39],s72 offen sc1
v_mfma_f32_16x16x32_bf16 v[24:27],a[220:223],a[4:7],v[24:27]
v_mfma_f32_16x16x32_bf16 v[28:31],a[220:223],a[8:11],v[28:31]
v_mfma_f32_16x16x32_bf16 v[32:35],a[220:223],a[12:15],v[32:35]
v_mfma_f32_16x16x32_bf16 v[36:39],a[220:223],a[16:19],v[36:39]
s_waitcnt vmcnt(14) lgkmcnt(0)
s_barrier
v_mfma_f32_16x16x32_bf16 v[40:43],a[224:227],a[0:3],v[40:43]
ds_read_b128 a[20:23],v249 offset:40960
v_mfma_f32_16x16x32_bf16 v[44:47],a[224:227],a[4:7],v[44:47]
ds_read_b128 a[24:27],v249 offset:43008
v_mfma_f32_16x16x32_bf16 v[48:51],a[224:227],a[8:11],v[48:51]
s_mov_b32 s72,52224
ds_read_b128 a[28:31],v249 offset:45056
v_mfma_f32_16x16x32_bf16 v[52:55],a[224:227],a[12:15],v[52:55]
buffer_store_dwordx4 a[184:187],v253,s[36:39],s72 offen sc1
v_mfma_f32_16x16x32_bf16 v[56:59],a[224:227],a[16:19],v[56:59]
ds_read_b128 a[32:35],v249 offset:47104
ds_read_b128 a[36:39],v249 offset:49152
v_mfma_f32_16x16x32_bf16 v[60:63],a[228:231],a[0:3],v[60:63]
ds_read_b64_tr_b16 a[200:201],v246 offset:24840
v_mfma_f32_16x16x32_bf16 v[64:67],a[228:231],a[4:7],v[64:67]
ds_read_b64_tr_b16 a[202:203],v246 offset:26376
v_mfma_f32_16x16x32_bf16 v[68:71],a[228:231],a[8:11],v[68:71]
ds_read_b64_tr_b16 a[204:205],v246 offset:24848
v_mfma_f32_16x16x32_bf16 v[72:75],a[228:231],a[12:15],v[72:75]
s_mov_b32 s72,53248
ds_read_b64_tr_b16 a[206:207],v246 offset:26384
v_mfma_f32_16x16x32_bf16 v[76:79],a[228:231],a[16:19],v[76:79]
buffer_store_dwordx4 a[188:191],v253,s[36:39],s72 offen sc1
v_mfma_f32_16x16x32_bf16 v[80:83],a[232:235],a[0:3],v[80:83]
ds_read_b64_tr_b16 a[208:209],v247 offset:24840
ds_read_b64_tr_b16 a[210:211],v247 offset:26376
v_mfma_f32_16x16x32_bf16 v[84:87],a[232:235],a[4:7],v[84:87]
ds_read_b64_tr_b16 a[212:213],v247 offset:24848
v_mfma_f32_16x16x32_bf16 v[88:91],a[232:235],a[8:11],v[88:91]
ds_read_b64_tr_b16 a[214:215],v247 offset:26384
v_mfma_f32_16x16x32_bf16 v[92:95],a[232:235],a[12:15],v[92:95]
ds_read_b64_tr_b16 a[216:217],v246 offset:24584
v_mfma_f32_16x16x32_bf16 v[96:99],a[232:235],a[16:19],v[96:99]
s_mov_b32 s72,54272
ds_read_b64_tr_b16 a[218:219],v246 offset:26120
v_mfma_f32_16x16x32_bf16 v[100:103],a[236:239],a[0:3],v[100:103]
buffer_store_dwordx4 a[192:195],v253,s[36:39],s72 offen sc1
v_mfma_f32_16x16x32_bf16 v[104:107],a[236:239],a[4:7],v[104:107]
ds_read_b64_tr_b16 a[220:221],v246 offset:24592
ds_read_b64_tr_b16 a[222:223],v246 offset:26128
v_mfma_f32_16x16x32_bf16 v[108:111],a[236:239],a[8:11],v[108:111]
v_mfma_f32_16x16x32_bf16 v[112:115],a[236:239],a[12:15],v[112:115]
v_mfma_f32_16x16x32_bf16 v[116:119],a[236:239],a[16:19],v[116:119]
v_mfma_f32_16x16x32_bf16 v[120:123],a[240:243],a[0:3],v[120:123]
s_mov_b32 s72,55296
v_mfma_f32_16x16x32_bf16 v[124:127],a[240:243],a[4:7],v[124:127]
buffer_store_dwordx4 a[196:199],v253,s[36:39],s72 offen sc1
v_mfma_f32_16x16x32_bf16 v[128:131],a[240:243],a[8:11],v[128:131]
v_mfma_f32_16x16x32_bf16 v[132:135],a[240:243],a[12:15],v[132:135]
v_mfma_f32_16x16x32_bf16 v[136:139],a[240:243],a[16:19],v[136:139]
v_mfma_f32_16x16x32_bf16 v[140:143],a[244:247],a[0:3],v[140:143]
v_mfma_f32_16x16x32_bf16 v[144:147],a[244:247],a[4:7],v[144:147]
v_mfma_f32_16x16x32_bf16 v[148:151],a[244:247],a[8:11],v[148:151]
v_mfma_f32_16x16x32_bf16 v[152:155],a[244:247],a[12:15],v[152:155]
v_mfma_f32_16x16x32_bf16 v[156:159],a[244:247],a[16:19],v[156:159]
s_waitcnt lgkmcnt(0)
v_mfma_f32_16x16x32_bf16 v[160:163],a[200:203],a[20:23],v[160:163]
v_mfma_f32_16x16x32_bf16 v[164:167],a[200:203],a[24:27],v[164:167]
ds_read_b64_tr_b16 a[224:225],v247 offset:24584
v_mfma_f32_16x16x32_bf16 v[168:171],a[200:203],a[28:31],v[168:171]
ds_read_b64_tr_b16 a[226:227],v247 offset:26120
v_mfma_f32_16x16x32_bf16 v[172:175],a[200:203],a[32:35],v[172:175]
s_add_u32 m0,s58,24584
ds_read_b64_tr_b16 a[228:229],v247 offset:24592
v_mfma_f32_16x16x32_bf16 v[176:179],a[200:203],a[36:39],v[176:179]
buffer_load_dwordx4 v240,s[24:27],s52 offen lds
v_mfma_f32_16x16x32_bf16 v[180:183],a[204:207],a[20:23],v[180:183]
ds_read_b64_tr_b16 a[230:231],v247 offset:26128
ds_read_b64_tr_b16 a[232:233],v246 offset:24712
v_mfma_f32_16x16x32_bf16 v[184:187],a[204:207],a[24:27],v[184:187]
ds_read_b64_tr_b16 a[234:235],v246 offset:26248
v_mfma_f32_16x16x32_bf16 v[188:191],a[204:207],a[28:31],v[188:191]
s_add_u32 m0,s58,24584
ds_read_b64_tr_b16 a[236:237],v246 offset:24720
v_mfma_f32_16x16x32_bf16 v[192:195],a[204:207],a[32:35],v[192:195]
buffer_load_dwordx4 v241,s[24:27],s52 offen offset:1024 lds
v_mfma_f32_16x16x32_bf16 v[196:199],a[204:207],a[36:39],v[196:199]
ds_read_b64_tr_b16 a[238:239],v246 offset:26256
ds_read_b64_tr_b16 a[240:241],v247 offset:24712
v_mfma_f32_16x16x32_bf16 v[200:203],a[208:211],a[20:23],v[200:203]
ds_read_b64_tr_b16 a[242:243],v247 offset:26248
v_mfma_f32_16x16x32_bf16 v[204:207],a[208:211],a[24:27],v[204:207]
s_add_u32 m0,s58,24584
ds_read_b64_tr_b16 a[244:245],v247 offset:24720
v_mfma_f32_16x16x32_bf16 v[208:211],a[208:211],a[28:31],v[208:211]
buffer_load_dwordx4 v242,s[24:27],s52 offen offset:2048 lds
v_mfma_f32_16x16x32_bf16 v[212:215],a[208:211],a[32:35],v[212:215]
ds_read_b64_tr_b16 a[246:247],v247 offset:26256
v_mfma_f32_16x16x32_bf16 v[216:219],a[208:211],a[36:39],v[216:219]
s_add_u32 m0,s58,27656
v_mfma_f32_16x16x32_bf16 v[220:223],a[212:215],a[20:23],v[220:223]
s_add_u32 s66,s52,196608
v_mfma_f32_16x16x32_bf16 v[224:227],a[212:215],a[24:27],v[224:227]
buffer_load_dwordx4 v240,s[24:27],s66 offen lds
v_mfma_f32_16x16x32_bf16 v[228:231],a[212:215],a[28:31],v[228:231]
v_mfma_f32_16x16x32_bf16 v[232:235],a[212:215],a[32:35],v[232:235]
v_mfma_f32_16x16x32_bf16 v[236:239],a[212:215],a[36:39],v[236:239]
s_add_u32 m0,s58,27656
v_mfma_f32_16x16x32_bf16 v[0:3],a[216:219],a[20:23],v[0:3]
buffer_load_dwordx4 v241,s[24:27],s66 offen offset:1024 lds
v_mfma_f32_16x16x32_bf16 v[4:7],a[216:219],a[24:27],v[4:7]
v_mfma_f32_16x16x32_bf16 v[8:11],a[216:219],a[28:31],v[8:11]
v_mfma_f32_16x16x32_bf16 v[12:15],a[216:219],a[32:35],v[12:15]
s_add_u32 m0,s58,27656
v_mfma_f32_16x16x32_bf16 v[16:19],a[216:219],a[36:39],v[16:19]
buffer_load_dwordx4 v242,s[24:27],s66 offen offset:2048 lds
v_mfma_f32_16x16x32_bf16 v[20:23],a[220:223],a[20:23],v[20:23]
s_add_u32 s52,s52,393216
v_mfma_f32_16x16x32_bf16 v[24:27],a[220:223],a[24:27],v[24:27]
v_mfma_f32_16x16x32_bf16 v[28:31],a[220:223],a[28:31],v[28:31]
v_mfma_f32_16x16x32_bf16 v[32:35],a[220:223],a[32:35],v[32:35]
v_mfma_f32_16x16x32_bf16 v[36:39],a[220:223],a[36:39],v[36:39]
s_waitcnt vmcnt(18) lgkmcnt(0)
s_barrier
v_mfma_f32_16x16x32_bf16 v[40:43],a[224:227],a[20:23],v[40:43]
s_add_u32 m0,s58,49168
ds_read_b128 a[0:3],v248
v_mfma_f32_16x16x32_bf16 v[44:47],a[224:227],a[24:27],v[44:47]
buffer_load_dwordx4 v240,s[24:27],s52 offen lds
v_mfma_f32_16x16x32_bf16 v[48:51],a[224:227],a[28:31],v[48:51]
ds_read_b128 a[4:7],v248 offset:2048
ds_read_b128 a[8:11],v248 offset:4096
v_mfma_f32_16x16x32_bf16 v[52:55],a[224:227],a[32:35],v[52:55]
ds_read_b128 a[12:15],v248 offset:6144
v_mfma_f32_16x16x32_bf16 v[56:59],a[224:227],a[36:39],v[56:59]
s_add_u32 m0,s58,49168
ds_read_b128 a[16:19],v248 offset:8192
v_mfma_f32_16x16x32_bf16 v[60:63],a[228:231],a[20:23],v[60:63]
buffer_load_dwordx4 v241,s[24:27],s52 offen offset:1024 lds
v_mfma_f32_16x16x32_bf16 v[64:67],a[228:231],a[24:27],v[64:67]
ds_read_b64_tr_b16 a[200:201],v244 offset:256
ds_read_b64_tr_b16 a[202:203],v244 offset:1792
v_mfma_f32_16x16x32_bf16 v[68:71],a[228:231],a[28:31],v[68:71]
ds_read_b64_tr_b16 a[204:205],v244 offset:264
v_mfma_f32_16x16x32_bf16 v[72:75],a[228:231],a[32:35],v[72:75]
s_add_u32 m0,s58,49168
ds_read_b64_tr_b16 a[206:207],v244 offset:1800
v_mfma_f32_16x16x32_bf16 v[76:79],a[228:231],a[36:39],v[76:79]
buffer_load_dwordx4 v242,s[24:27],s52 offen offset:2048 lds
v_mfma_f32_16x16x32_bf16 v[80:83],a[232:235],a[20:23],v[80:83]
ds_read_b64_tr_b16 a[208:209],v245 offset:256
ds_read_b64_tr_b16 a[210:211],v245 offset:1792
v_mfma_f32_16x16x32_bf16 v[84:87],a[232:235],a[24:27],v[84:87]
s_add_u32 m0,s58,52240
ds_read_b64_tr_b16 a[212:213],v245 offset:264
v_mfma_f32_16x16x32_bf16 v[88:91],a[232:235],a[28:31],v[88:91]
s_add_u32 s66,s52,196608
ds_read_b64_tr_b16 a[214:215],v245 offset:1800
v_mfma_f32_16x16x32_bf16 v[92:95],a[232:235],a[32:35],v[92:95]
buffer_load_dwordx4 v240,s[24:27],s66 offen lds
v_mfma_f32_16x16x32_bf16 v[96:99],a[232:235],a[36:39],v[96:99]
ds_read_b64_tr_b16 a[216:217],v244
ds_read_b64_tr_b16 a[218:219],v244 offset:1536
v_mfma_f32_16x16x32_bf16 v[100:103],a[236:239],a[20:23],v[100:103]
ds_read_b64_tr_b16 a[220:221],v244 offset:8
v_mfma_f32_16x16x32_bf16 v[104:107],a[236:239],a[24:27],v[104:107]
s_add_u32 m0,s58,52240
ds_read_b64_tr_b16 a[222:223],v244 offset:1544
v_mfma_f32_16x16x32_bf16 v[108:111],a[236:239],a[28:31],v[108:111]
buffer_load_dwordx4 v241,s[24:27],s66 offen offset:1024 lds
v_mfma_f32_16x16x32_bf16 v[112:115],a[236:239],a[32:35],v[112:115]
v_mfma_f32_16x16x32_bf16 v[116:119],a[236:239],a[36:39],v[116:119]
v_mfma_f32_16x16x32_bf16 v[120:123],a[240:243],a[20:23],v[120:123]
s_add_u32 m0,s58,52240
v_mfma_f32_16x16x32_bf16 v[124:127],a[240:243],a[24:27],v[124:127]
buffer_load_dwordx4 v242,s[24:27],s66 offen offset:2048 lds
v_mfma_f32_16x16x32_bf16 v[128:131],a[240:243],a[28:31],v[128:131]
s_add_u32 s52,s52,393216
v_mfma_f32_16x16x32_bf16 v[132:135],a[240:243],a[32:35],v[132:135]
s_mov_b32 s46,s6
v_mfma_f32_16x16x32_bf16 v[136:139],a[240:243],a[36:39],v[136:139]
v_mfma_f32_16x16x32_bf16 v[140:143],a[244:247],a[20:23],v[140:143]
v_mfma_f32_16x16x32_bf16 v[144:147],a[244:247],a[24:27],v[144:147]
v_mfma_f32_16x16x32_bf16 v[148:151],a[244:247],a[28:31],v[148:151]
v_mfma_f32_16x16x32_bf16 v[152:155],a[244:247],a[32:35],v[152:155]
v_mfma_f32_16x16x32_bf16 v[156:159],a[244:247],a[36:39],v[156:159]
s_waitcnt lgkmcnt(0)
v_mfma_f32_16x16x32_bf16 v[160:163],a[200:203],a[0:3],v[160:163]
v_mfma_f32_16x16x32_bf16 v[164:167],a[200:203],a[4:7],v[164:167]
buffer_load_dword a250,v254,s[44:47],s74 offen
v_mfma_f32_16x16x32_bf16 v[168:171],a[200:203],a[8:11],v[168:171]
s_add_u32 m0,s59,40960
ds_read_b64_tr_b16 a[224:225],v245
ds_read_b64_tr_b16 a[226:227],v245 offset:1536
v_mfma_f32_16x16x32_bf16 v[172:175],a[200:203],a[12:15],v[172:175]
s_add_u32 s74,s74,786432
buffer_load_dwordx4 v243,s[20:23],s53 offen lds
v_mfma_f32_16x16x32_bf16 v[176:179],a[200:203],a[16:19],v[176:179]
s_add_u32 m0,s59,40960
ds_read_b64_tr_b16 a[228:229],v245 offset:8
ds_read_b64_tr_b16 a[230:231],v245 offset:1544
v_mfma_f32_16x16x32_bf16 v[180:183],a[204:207],a[0:3],v[180:183]
s_add_u32 s67,s53,9216
ds_read_b64_tr_b16 a[232:233],v244 offset:128
v_mfma_f32_16x16x32_bf16 v[184:187],a[204:207],a[4:7],v[184:187]
buffer_load_dwordx4 v243,s[20:23],s67 offen offset:1024 lds
v_mfma_f32_16x16x32_bf16 v[188:191],a[204:207],a[8:11],v[188:191]
s_add_u32 m0,s59,40960
ds_read_b64_tr_b16 a[234:235],v244 offset:1664
ds_read_b64_tr_b16 a[236:237],v244 offset:136
v_mfma_f32_16x16x32_bf16 v[192:195],a[204:207],a[12:15],v[192:195]
s_add_u32 s67,s53,18432
ds_read_b64_tr_b16 a[238:239],v244 offset:1672
v_mfma_f32_16x16x32_bf16 v[196:199],a[204:207],a[16:19],v[196:199]
buffer_load_dwordx4 v243,s[20:23],s67 offen offset:2048 lds
v_mfma_f32_16x16x32_bf16 v[200:203],a[208:211],a[0:3],v[200:203]
s_add_u32 m0,s59,40960
ds_read_b64_tr_b16 a[240:241],v245 offset:128
ds_read_b64_tr_b16 a[242:243],v245 offset:1664
v_mfma_f32_16x16x32_bf16 v[204:207],a[208:211],a[4:7],v[204:207]
s_add_u32 s67,s53,27648
ds_read_b64_tr_b16 a[244:245],v245 offset:136
v_mfma_f32_16x16x32_bf16 v[208:211],a[208:211],a[8:11],v[208:211]
buffer_load_dwordx4 v243,s[20:23],s67 offen offset:3072 lds
v_mfma_f32_16x16x32_bf16 v[212:215],a[208:211],a[12:15],v[212:215]
s_add_u32 m0,s59,45056
ds_read_b64_tr_b16 a[246:247],v245 offset:1672
v_mfma_f32_16x16x32_bf16 v[216:219],a[208:211],a[16:19],v[216:219]
s_add_u32 s67,s53,40960
v_mfma_f32_16x16x32_bf16 v[220:223],a[212:215],a[0:3],v[220:223]
buffer_load_dwordx4 v243,s[20:23],s67 offen lds
v_mfma_f32_16x16x32_bf16 v[224:227],a[212:215],a[4:7],v[224:227]
s_add_u32 s53,s53,128
v_mfma_f32_16x16x32_bf16 v[228:231],a[212:215],a[8:11],v[228:231]
v_mfma_f32_16x16x32_bf16 v[232:235],a[212:215],a[12:15],v[232:235]
v_mfma_f32_16x16x32_bf16 v[236:239],a[212:215],a[16:19],v[236:239]
v_mfma_f32_16x16x32_bf16 v[0:3],a[216:219],a[0:3],v[0:3]
v_mfma_f32_16x16x32_bf16 v[4:7],a[216:219],a[4:7],v[4:7]
v_mfma_f32_16x16x32_bf16 v[8:11],a[216:219],a[8:11],v[8:11]
v_mfma_f32_16x16x32_bf16 v[12:15],a[216:219],a[12:15],v[12:15]
v_mfma_f32_16x16x32_bf16 v[16:19],a[216:219],a[16:19],v[16:19]
v_mfma_f32_16x16x32_bf16 v[20:23],a[220:223],a[0:3],v[20:23]
v_mfma_f32_16x16x32_bf16 v[24:27],a[220:223],a[4:7],v[24:27]
v_mfma_f32_16x16x32_bf16 v[28:31],a[220:223],a[8:11],v[28:31]
v_mfma_f32_16x16x32_bf16 v[32:35],a[220:223],a[12:15],v[32:35]
v_mfma_f32_16x16x32_bf16 v[36:39],a[220:223],a[16:19],v[36:39]
s_waitcnt vmcnt(12) lgkmcnt(0)
s_barrier
v_mfma_f32_16x16x32_bf16 v[40:43],a[224:227],a[0:3],v[40:43]
ds_read_b128 a[20:23],v249
v_mfma_f32_16x16x32_bf16 v[44:47],a[224:227],a[4:7],v[44:47]
ds_read_b128 a[24:27],v249 offset:2048
v_mfma_f32_16x16x32_bf16 v[48:51],a[224:227],a[8:11],v[48:51]
ds_read_b128 a[28:31],v249 offset:4096
v_mfma_f32_16x16x32_bf16 v[52:55],a[224:227],a[12:15],v[52:55]
ds_read_b128 a[32:35],v249 offset:6144
v_mfma_f32_16x16x32_bf16 v[56:59],a[224:227],a[16:19],v[56:59]
ds_read_b128 a[36:39],v249 offset:8192
v_mfma_f32_16x16x32_bf16 v[60:63],a[228:231],a[0:3],v[60:63]
ds_read_b64_tr_b16 a[200:201],v244 offset:24840
v_mfma_f32_16x16x32_bf16 v[64:67],a[228:231],a[4:7],v[64:67]
ds_read_b64_tr_b16 a[202:203],v244 offset:26376
v_mfma_f32_16x16x32_bf16 v[68:71],a[228:231],a[8:11],v[68:71]
ds_read_b64_tr_b16 a[204:205],v244 offset:24848
v_mfma_f32_16x16x32_bf16 v[72:75],a[228:231],a[12:15],v[72:75]
ds_read_b64_tr_b16 a[206:207],v244 offset:26384
v_mfma_f32_16x16x32_bf16 v[76:79],a[228:231],a[16:19],v[76:79]
ds_read_b64_tr_b16 a[208:209],v245 offset:24840
v_mfma_f32_16x16x32_bf16 v[80:83],a[232:235],a[0:3],v[80:83]
ds_read_b64_tr_b16 a[210:211],v245 offset:26376
v_mfma_f32_16x16x32_bf16 v[84:87],a[232:235],a[4:7],v[84:87]
ds_read_b64_tr_b16 a[212:213],v245 offset:24848
v_mfma_f32_16x16x32_bf16 v[88:91],a[232:235],a[8:11],v[88:91]
ds_read_b64_tr_b16 a[214:215],v245 offset:26384
v_mfma_f32_16x16x32_bf16 v[92:95],a[232:235],a[12:15],v[92:95]
ds_read_b64_tr_b16 a[216:217],v244 offset:24584
v_mfma_f32_16x16x32_bf16 v[96:99],a[232:235],a[16:19],v[96:99]
ds_read_b64_tr_b16 a[218:219],v244 offset:26120
v_mfma_f32_16x16x32_bf16 v[100:103],a[236:239],a[0:3],v[100:103]
ds_read_b64_tr_b16 a[220:221],v244 offset:24592
v_mfma_f32_16x16x32_bf16 v[104:107],a[236:239],a[4:7],v[104:107]
ds_read_b64_tr_b16 a[222:223],v244 offset:26128
v_mfma_f32_16x16x32_bf16 v[108:111],a[236:239],a[8:11],v[108:111]
v_mfma_f32_16x16x32_bf16 v[112:115],a[236:239],a[12:15],v[112:115]
v_mfma_f32_16x16x32_bf16 v[116:119],a[236:239],a[16:19],v[116:119]
v_mfma_f32_16x16x32_bf16 v[120:123],a[240:243],a[0:3],v[120:123]
v_mfma_f32_16x16x32_bf16 v[124:127],a[240:243],a[4:7],v[124:127]
v_mfma_f32_16x16x32_bf16 v[128:131],a[240:243],a[8:11],v[128:131]
v_mfma_f32_16x16x32_bf16 v[132:135],a[240:243],a[12:15],v[132:135]
v_mfma_f32_16x16x32_bf16 v[136:139],a[240:243],a[16:19],v[136:139]
v_mfma_f32_16x16x32_bf16 v[140:143],a[244:247],a[0:3],v[140:143]
v_mfma_f32_16x16x32_bf16 v[144:147],a[244:247],a[4:7],v[144:147]
v_mfma_f32_16x16x32_bf16 v[148:151],a[244:247],a[8:11],v[148:151]
v_mfma_f32_16x16x32_bf16 v[152:155],a[244:247],a[12:15],v[152:155]
v_mfma_f32_16x16x32_bf16 v[156:159],a[244:247],a[16:19],v[156:159]
s_waitcnt lgkmcnt(0)
v_mfma_f32_16x16x32_bf16 v[160:163],a[200:203],a[20:23],v[160:163]
v_mfma_f32_16x16x32_bf16 v[164:167],a[200:203],a[24:27],v[164:167]
ds_read_b64_tr_b16 a[224:225],v245 offset:24584
v_mfma_f32_16x16x32_bf16 v[168:171],a[200:203],a[28:31],v[168:171]
ds_read_b64_tr_b16 a[226:227],v245 offset:26120
v_mfma_f32_16x16x32_bf16 v[172:175],a[200:203],a[32:35],v[172:175]
s_add_u32 m0,s58,73752
ds_read_b64_tr_b16 a[228:229],v245 offset:24592
v_mfma_f32_16x16x32_bf16 v[176:179],a[200:203],a[36:39],v[176:179]
buffer_load_dwordx4 v240,s[24:27],s52 offen lds
v_mfma_f32_16x16x32_bf16 v[180:183],a[204:207],a[20:23],v[180:183]
ds_read_b64_tr_b16 a[230:231],v245 offset:26128
ds_read_b64_tr_b16 a[232:233],v244 offset:24712
v_mfma_f32_16x16x32_bf16 v[184:187],a[204:207],a[24:27],v[184:187]
ds_read_b64_tr_b16 a[234:235],v244 offset:26248
v_mfma_f32_16x16x32_bf16 v[188:191],a[204:207],a[28:31],v[188:191]
s_add_u32 m0,s58,73752
ds_read_b64_tr_b16 a[236:237],v244 offset:24720
v_mfma_f32_16x16x32_bf16 v[192:195],a[204:207],a[32:35],v[192:195]
buffer_load_dwordx4 v241,s[24:27],s52 offen offset:1024 lds
v_mfma_f32_16x16x32_bf16 v[196:199],a[204:207],a[36:39],v[196:199]
ds_read_b64_tr_b16 a[238:239],v244 offset:26256
ds_read_b64_tr_b16 a[240:241],v245 offset:24712
v_mfma_f32_16x16x32_bf16 v[200:203],a[208:211],a[20:23],v[200:203]
ds_read_b64_tr_b16 a[242:243],v245 offset:26248
v_mfma_f32_16x16x32_bf16 v[204:207],a[208:211],a[24:27],v[204:207]
s_add_u32 m0,s58,73752
ds_read_b64_tr_b16 a[244:245],v245 offset:24720
v_mfma_f32_16x16x32_bf16 v[208:211],a[208:211],a[28:31],v[208:211]
buffer_load_dwordx4 v242,s[24:27],s52 offen offset:2048 lds
v_mfma_f32_16x16x32_bf16 v[212:215],a[208:211],a[32:35],v[212:215]
ds_read_b64_tr_b16 a[246:247],v245 offset:26256
v_mfma_f32_16x16x32_bf16 v[216:219],a[208:211],a[36:39],v[216:219]
s_add_u32 m0,s58,76824
v_mfma_f32_16x16x32_bf16 v[220:223],a[212:215],a[20:23],v[220:223]
s_add_u32 s66,s52,196608
v_mfma_f32_16x16x32_bf16 v[224:227],a[212:215],a[24:27],v[224:227]
buffer_load_dwordx4 v240,s[24:27],s66 offen lds
v_mfma_f32_16x16x32_bf16 v[228:231],a[212:215],a[28:31],v[228:231]
v_mfma_f32_16x16x32_bf16 v[232:235],a[212:215],a[32:35],v[232:235]
v_mfma_f32_16x16x32_bf16 v[236:239],a[212:215],a[36:39],v[236:239]
s_add_u32 m0,s58,76824
v_mfma_f32_16x16x32_bf16 v[0:3],a[216:219],a[20:23],v[0:3]
buffer_load_dwordx4 v241,s[24:27],s66 offen offset:1024 lds
v_mfma_f32_16x16x32_bf16 v[4:7],a[216:219],a[24:27],v[4:7]
v_mfma_f32_16x16x32_bf16 v[8:11],a[216:219],a[28:31],v[8:11]
v_mfma_f32_16x16x32_bf16 v[12:15],a[216:219],a[32:35],v[12:15]
s_add_u32 m0,s58,76824
v_mfma_f32_16x16x32_bf16 v[16:19],a[216:219],a[36:39],v[16:19]
buffer_load_dwordx4 v242,s[24:27],s66 offen offset:2048 lds
v_mfma_f32_16x16x32_bf16 v[20:23],a[220:223],a[20:23],v[20:23]
s_add_u32 s52,s52,393216
v_mfma_f32_16x16x32_bf16 v[24:27],a[220:223],a[24:27],v[24:27]
v_mfma_f32_16x16x32_bf16 v[28:31],a[220:223],a[28:31],v[28:31]
v_mfma_f32_16x16x32_bf16 v[32:35],a[220:223],a[32:35],v[32:35]
v_mfma_f32_16x16x32_bf16 v[36:39],a[220:223],a[36:39],v[36:39]
s_waitcnt vmcnt(12) lgkmcnt(0)
s_barrier
v_mfma_f32_16x16x32_bf16 v[40:43],a[224:227],a[20:23],v[40:43]
s_add_u32 m0,s58,0
ds_read_b128 a[0:3],v248 offset:20480
v_mfma_f32_16x16x32_bf16 v[44:47],a[224:227],a[24:27],v[44:47]
buffer_load_dwordx4 v240,s[24:27],s52 offen lds
v_mfma_f32_16x16x32_bf16 v[48:51],a[224:227],a[28:31],v[48:51]
ds_read_b128 a[4:7],v248 offset:22528
ds_read_b128 a[8:11],v248 offset:24576
v_mfma_f32_16x16x32_bf16 v[52:55],a[224:227],a[32:35],v[52:55]
ds_read_b128 a[12:15],v248 offset:26624
v_mfma_f32_16x16x32_bf16 v[56:59],a[224:227],a[36:39],v[56:59]
s_add_u32 m0,s58,0
ds_read_b128 a[16:19],v248 offset:28672
v_mfma_f32_16x16x32_bf16 v[60:63],a[228:231],a[20:23],v[60:63]
buffer_load_dwordx4 v241,s[24:27],s52 offen offset:1024 lds
v_mfma_f32_16x16x32_bf16 v[64:67],a[228:231],a[24:27],v[64:67]
ds_read_b64_tr_b16 a[200:201],v246 offset:256
ds_read_b64_tr_b16 a[202:203],v246 offset:1792
v_mfma_f32_16x16x32_bf16 v[68:71],a[228:231],a[28:31],v[68:71]
ds_read_b64_tr_b16 a[204:205],v246 offset:264
v_mfma_f32_16x16x32_bf16 v[72:75],a[228:231],a[32:35],v[72:75]
s_add_u32 m0,s58,0
ds_read_b64_tr_b16 a[206:207],v246 offset:1800
v_mfma_f32_16x16x32_bf16 v[76:79],a[228:231],a[36:39],v[76:79]
buffer_load_dwordx4 v242,s[24:27],s52 offen offset:2048 lds
v_mfma_f32_16x16x32_bf16 v[80:83],a[232:235],a[20:23],v[80:83]
ds_read_b64_tr_b16 a[208:209],v247 offset:256
ds_read_b64_tr_b16 a[210:211],v247 offset:1792
v_mfma_f32_16x16x32_bf16 v[84:87],a[232:235],a[24:27],v[84:87]
s_add_u32 m0,s58,3072
ds_read_b64_tr_b16 a[212:213],v247 offset:264
v_mfma_f32_16x16x32_bf16 v[88:91],a[232:235],a[28:31],v[88:91]
s_add_u32 s66,s52,196608
ds_read_b64_tr_b16 a[214:215],v247 offset:1800
v_mfma_f32_16x16x32_bf16 v[92:95],a[232:235],a[32:35],v[92:95]
buffer_load_dwordx4 v240,s[24:27],s66 offen lds
v_mfma_f32_16x16x32_bf16 v[96:99],a[232:235],a[36:39],v[96:99]
ds_read_b64_tr_b16 a[216:217],v246
ds_read_b64_tr_b16 a[218:219],v246 offset:1536
v_mfma_f32_16x16x32_bf16 v[100:103],a[236:239],a[20:23],v[100:103]
ds_read_b64_tr_b16 a[220:221],v246 offset:8
v_mfma_f32_16x16x32_bf16 v[104:107],a[236:239],a[24:27],v[104:107]
s_add_u32 m0,s58,3072
ds_read_b64_tr_b16 a[222:223],v246 offset:1544
v_mfma_f32_16x16x32_bf16 v[108:111],a[236:239],a[28:31],v[108:111]
buffer_load_dwordx4 v241,s[24:27],s66 offen offset:1024 lds
v_mfma_f32_16x16x32_bf16 v[112:115],a[236:239],a[32:35],v[112:115]
v_mfma_f32_16x16x32_bf16 v[116:119],a[236:239],a[36:39],v[116:119]
v_mfma_f32_16x16x32_bf16 v[120:123],a[240:243],a[20:23],v[120:123]
s_add_u32 m0,s58,3072
v_mfma_f32_16x16x32_bf16 v[124:127],a[240:243],a[24:27],v[124:127]
buffer_load_dwordx4 v242,s[24:27],s66 offen offset:2048 lds
v_mfma_f32_16x16x32_bf16 v[128:131],a[240:243],a[28:31],v[128:131]
s_add_u32 s52,s52,393216
v_mfma_f32_16x16x32_bf16 v[132:135],a[240:243],a[32:35],v[132:135]
v_mfma_f32_16x16x32_bf16 v[136:139],a[240:243],a[36:39],v[136:139]
v_mfma_f32_16x16x32_bf16 v[140:143],a[244:247],a[20:23],v[140:143]
v_mfma_f32_16x16x32_bf16 v[144:147],a[244:247],a[24:27],v[144:147]
v_mfma_f32_16x16x32_bf16 v[148:151],a[244:247],a[28:31],v[148:151]
v_mfma_f32_16x16x32_bf16 v[152:155],a[244:247],a[32:35],v[152:155]
v_mfma_f32_16x16x32_bf16 v[156:159],a[244:247],a[36:39],v[156:159]
s_waitcnt lgkmcnt(0)
v_mfma_f32_16x16x32_bf16 v[160:163],a[200:203],a[0:3],v[160:163]
v_mfma_f32_16x16x32_bf16 v[164:167],a[200:203],a[4:7],v[164:167]
buffer_load_dword a250,v254,s[44:47],s74 offen
v_mfma_f32_16x16x32_bf16 v[168:171],a[200:203],a[8:11],v[168:171]
s_add_u32 m0,s59,0
ds_read_b64_tr_b16 a[224:225],v247
ds_read_b64_tr_b16 a[226:227],v247 offset:1536
v_mfma_f32_16x16x32_bf16 v[172:175],a[200:203],a[12:15],v[172:175]
s_add_u32 s74,s74,786432
buffer_load_dwordx4 v243,s[20:23],s53 offen lds
v_mfma_f32_16x16x32_bf16 v[176:179],a[200:203],a[16:19],v[176:179]
s_add_u32 m0,s59,0
ds_read_b64_tr_b16 a[228:229],v247 offset:8
ds_read_b64_tr_b16 a[230:231],v247 offset:1544
v_mfma_f32_16x16x32_bf16 v[180:183],a[204:207],a[0:3],v[180:183]
s_add_u32 s67,s53,9216
ds_read_b64_tr_b16 a[232:233],v246 offset:128
v_mfma_f32_16x16x32_bf16 v[184:187],a[204:207],a[4:7],v[184:187]
buffer_load_dwordx4 v243,s[20:23],s67 offen offset:1024 lds
v_mfma_f32_16x16x32_bf16 v[188:191],a[204:207],a[8:11],v[188:191]
s_add_u32 m0,s59,0
ds_read_b64_tr_b16 a[234:235],v246 offset:1664
ds_read_b64_tr_b16 a[236:237],v246 offset:136
v_mfma_f32_16x16x32_bf16 v[192:195],a[204:207],a[12:15],v[192:195]
s_add_u32 s67,s53,18432
ds_read_b64_tr_b16 a[238:239],v246 offset:1672
v_mfma_f32_16x16x32_bf16 v[196:199],a[204:207],a[16:19],v[196:199]
buffer_load_dwordx4 v243,s[20:23],s67 offen offset:2048 lds
v_mfma_f32_16x16x32_bf16 v[200:203],a[208:211],a[0:3],v[200:203]
s_add_u32 m0,s59,0
ds_read_b64_tr_b16 a[240:241],v247 offset:128
ds_read_b64_tr_b16 a[242:243],v247 offset:1664
v_mfma_f32_16x16x32_bf16 v[204:207],a[208:211],a[4:7],v[204:207]
s_add_u32 s67,s53,27648
ds_read_b64_tr_b16 a[244:245],v247 offset:136
v_mfma_f32_16x16x32_bf16 v[208:211],a[208:211],a[8:11],v[208:211]
buffer_load_dwordx4 v243,s[20:23],s67 offen offset:3072 lds
v_mfma_f32_16x16x32_bf16 v[212:215],a[208:211],a[12:15],v[212:215]
s_add_u32 m0,s59,4096
ds_read_b64_tr_b16 a[246:247],v247 offset:1672
v_mfma_f32_16x16x32_bf16 v[216:219],a[208:211],a[16:19],v[216:219]
s_add_u32 s67,s53,40960
v_mfma_f32_16x16x32_bf16 v[220:223],a[212:215],a[0:3],v[220:223]
buffer_load_dwordx4 v243,s[20:23],s67 offen lds
v_mfma_f32_16x16x32_bf16 v[224:227],a[212:215],a[4:7],v[224:227]
s_add_u32 s53,s53,128
v_mfma_f32_16x16x32_bf16 v[228:231],a[212:215],a[8:11],v[228:231]
v_mfma_f32_16x16x32_bf16 v[232:235],a[212:215],a[12:15],v[232:235]
v_mfma_f32_16x16x32_bf16 v[236:239],a[212:215],a[16:19],v[236:239]
v_mfma_f32_16x16x32_bf16 v[0:3],a[216:219],a[0:3],v[0:3]
v_mfma_f32_16x16x32_bf16 v[4:7],a[216:219],a[4:7],v[4:7]
v_mfma_f32_16x16x32_bf16 v[8:11],a[216:219],a[8:11],v[8:11]
v_mfma_f32_16x16x32_bf16 v[12:15],a[216:219],a[12:15],v[12:15]
v_mfma_f32_16x16x32_bf16 v[16:19],a[216:219],a[16:19],v[16:19]
v_mfma_f32_16x16x32_bf16 v[20:23],a[220:223],a[0:3],v[20:23]
v_mfma_f32_16x16x32_bf16 v[24:27],a[220:223],a[4:7],v[24:27]
v_mfma_f32_16x16x32_bf16 v[28:31],a[220:223],a[8:11],v[28:31]
v_mfma_f32_16x16x32_bf16 v[32:35],a[220:223],a[12:15],v[32:35]
v_mfma_f32_16x16x32_bf16 v[36:39],a[220:223],a[16:19],v[36:39]
s_waitcnt vmcnt(12) lgkmcnt(0)
s_barrier
v_mfma_f32_16x16x32_bf16 v[40:43],a[224:227],a[0:3],v[40:43]
ds_read_b128 a[20:23],v249 offset:20480
v_mfma_f32_16x16x32_bf16 v[44:47],a[224:227],a[4:7],v[44:47]
ds_read_b128 a[24:27],v249 offset:22528
v_mfma_f32_16x16x32_bf16 v[48:51],a[224:227],a[8:11],v[48:51]
ds_read_b128 a[28:31],v249 offset:24576
v_mfma_f32_16x16x32_bf16 v[52:55],a[224:227],a[12:15],v[52:55]
ds_read_b128 a[32:35],v249 offset:26624
v_mfma_f32_16x16x32_bf16 v[56:59],a[224:227],a[16:19],v[56:59]
ds_read_b128 a[36:39],v249 offset:28672
v_mfma_f32_16x16x32_bf16 v[60:63],a[228:231],a[0:3],v[60:63]
ds_read_b64_tr_b16 a[200:201],v246 offset:24840
v_mfma_f32_16x16x32_bf16 v[64:67],a[228:231],a[4:7],v[64:67]
ds_read_b64_tr_b16 a[202:203],v246 offset:26376
v_mfma_f32_16x16x32_bf16 v[68:71],a[228:231],a[8:11],v[68:71]
ds_read_b64_tr_b16 a[204:205],v246 offset:24848
v_mfma_f32_16x16x32_bf16 v[72:75],a[228:231],a[12:15],v[72:75]
ds_read_b64_tr_b16 a[206:207],v246 offset:26384
v_mfma_f32_16x16x32_bf16 v[76:79],a[228:231],a[16:19],v[76:79]
ds_read_b64_tr_b16 a[208:209],v247 offset:24840
v_mfma_f32_16x16x32_bf16 v[80:83],a[232:235],a[0:3],v[80:83]
ds_read_b64_tr_b16 a[210:211],v247 offset:26376
v_mfma_f32_16x16x32_bf16 v[84:87],a[232:235],a[4:7],v[84:87]
ds_read_b64_tr_b16 a[212:213],v247 offset:24848
v_mfma_f32_16x16x32_bf16 v[88:91],a[232:235],a[8:11],v[88:91]
ds_read_b64_tr_b16 a[214:215],v247 offset:26384
v_mfma_f32_16x16x32_bf16 v[92:95],a[232:235],a[12:15],v[92:95]
ds_read_b64_tr_b16 a[216:217],v246 offset:24584
v_mfma_f32_16x16x32_bf16 v[96:99],a[232:235],a[16:19],v[96:99]
ds_read_b64_tr_b16 a[218:219],v246 offset:26120
v_mfma_f32_16x16x32_bf16 v[100:103],a[236:239],a[0:3],v[100:103]
ds_read_b64_tr_b16 a[220:221],v246 offset:24592
v_mfma_f32_16x16x32_bf16 v[104:107],a[236:239],a[4:7],v[104:107]
ds_read_b64_tr_b16 a[222:223],v246 offset:26128
v_mfma_f32_16x16x32_bf16 v[108:111],a[236:239],a[8:11],v[108:111]
v_mfma_f32_16x16x32_bf16 v[112:115],a[236:239],a[12:15],v[112:115]
v_mfma_f32_16x16x32_bf16 v[116:119],a[236:239],a[16:19],v[116:119]
v_mfma_f32_16x16x32_bf16 v[120:123],a[240:243],a[0:3],v[120:123]
v_mfma_f32_16x16x32_bf16 v[124:127],a[240:243],a[4:7],v[124:127]
v_mfma_f32_16x16x32_bf16 v[128:131],a[240:243],a[8:11],v[128:131]
v_mfma_f32_16x16x32_bf16 v[132:135],a[240:243],a[12:15],v[132:135]
v_mfma_f32_16x16x32_bf16 v[136:139],a[240:243],a[16:19],v[136:139]
v_mfma_f32_16x16x32_bf16 v[140:143],a[244:247],a[0:3],v[140:143]
v_mfma_f32_16x16x32_bf16 v[144:147],a[244:247],a[4:7],v[144:147]
v_mfma_f32_16x16x32_bf16 v[148:151],a[244:247],a[8:11],v[148:151]
v_mfma_f32_16x16x32_bf16 v[152:155],a[244:247],a[12:15],v[152:155]
v_mfma_f32_16x16x32_bf16 v[156:159],a[244:247],a[16:19],v[156:159]
s_waitcnt lgkmcnt(0)
v_mfma_f32_16x16x32_bf16 v[160:163],a[200:203],a[20:23],v[160:163]
v_mfma_f32_16x16x32_bf16 v[164:167],a[200:203],a[24:27],v[164:167]
ds_read_b64_tr_b16 a[224:225],v247 offset:24584
v_mfma_f32_16x16x32_bf16 v[168:171],a[200:203],a[28:31],v[168:171]
ds_read_b64_tr_b16 a[226:227],v247 offset:26120
v_mfma_f32_16x16x32_bf16 v[172:175],a[200:203],a[32:35],v[172:175]
s_add_u32 m0,s58,24584
ds_read_b64_tr_b16 a[228:229],v247 offset:24592
v_mfma_f32_16x16x32_bf16 v[176:179],a[200:203],a[36:39],v[176:179]
buffer_load_dwordx4 v240,s[24:27],s52 offen lds
v_mfma_f32_16x16x32_bf16 v[180:183],a[204:207],a[20:23],v[180:183]
ds_read_b64_tr_b16 a[230:231],v247 offset:26128
ds_read_b64_tr_b16 a[232:233],v246 offset:24712
v_mfma_f32_16x16x32_bf16 v[184:187],a[204:207],a[24:27],v[184:187]
ds_read_b64_tr_b16 a[234:235],v246 offset:26248
v_mfma_f32_16x16x32_bf16 v[188:191],a[204:207],a[28:31],v[188:191]
s_add_u32 m0,s58,24584
ds_read_b64_tr_b16 a[236:237],v246 offset:24720
v_mfma_f32_16x16x32_bf16 v[192:195],a[204:207],a[32:35],v[192:195]
buffer_load_dwordx4 v241,s[24:27],s52 offen offset:1024 lds
v_mfma_f32_16x16x32_bf16 v[196:199],a[204:207],a[36:39],v[196:199]
ds_read_b64_tr_b16 a[238:239],v246 offset:26256
ds_read_b64_tr_b16 a[240:241],v247 offset:24712
v_mfma_f32_16x16x32_bf16 v[200:203],a[208:211],a[20:23],v[200:203]
ds_read_b64_tr_b16 a[242:243],v247 offset:26248
v_mfma_f32_16x16x32_bf16 v[204:207],a[208:211],a[24:27],v[204:207]
s_add_u32 m0,s58,24584
ds_read_b64_tr_b16 a[244:245],v247 offset:24720
v_mfma_f32_16x16x32_bf16 v[208:211],a[208:211],a[28:31],v[208:211]
buffer_load_dwordx4 v242,s[24:27],s52 offen offset:2048 lds
v_mfma_f32_16x16x32_bf16 v[212:215],a[208:211],a[32:35],v[212:215]
ds_read_b64_tr_b16 a[246:247],v247 offset:26256
v_mfma_f32_16x16x32_bf16 v[216:219],a[208:211],a[36:39],v[216:219]
s_add_u32 m0,s58,27656
v_mfma_f32_16x16x32_bf16 v[220:223],a[212:215],a[20:23],v[220:223]
s_add_u32 s66,s52,196608
v_mfma_f32_16x16x32_bf16 v[224:227],a[212:215],a[24:27],v[224:227]
buffer_load_dwordx4 v240,s[24:27],s66 offen lds
v_mfma_f32_16x16x32_bf16 v[228:231],a[212:215],a[28:31],v[228:231]
v_mfma_f32_16x16x32_bf16 v[232:235],a[212:215],a[32:35],v[232:235]
v_mfma_f32_16x16x32_bf16 v[236:239],a[212:215],a[36:39],v[236:239]
s_add_u32 m0,s58,27656
v_mfma_f32_16x16x32_bf16 v[0:3],a[216:219],a[20:23],v[0:3]
buffer_load_dwordx4 v241,s[24:27],s66 offen offset:1024 lds
v_mfma_f32_16x16x32_bf16 v[4:7],a[216:219],a[24:27],v[4:7]
v_mfma_f32_16x16x32_bf16 v[8:11],a[216:219],a[28:31],v[8:11]
v_mfma_f32_16x16x32_bf16 v[12:15],a[216:219],a[32:35],v[12:15]
s_add_u32 m0,s58,27656
v_mfma_f32_16x16x32_bf16 v[16:19],a[216:219],a[36:39],v[16:19]
buffer_load_dwordx4 v242,s[24:27],s66 offen offset:2048 lds
v_mfma_f32_16x16x32_bf16 v[20:23],a[220:223],a[20:23],v[20:23]
s_add_u32 s52,s52,393216
v_mfma_f32_16x16x32_bf16 v[24:27],a[220:223],a[24:27],v[24:27]
v_mfma_f32_16x16x32_bf16 v[28:31],a[220:223],a[28:31],v[28:31]
v_mfma_f32_16x16x32_bf16 v[32:35],a[220:223],a[32:35],v[32:35]
v_mfma_f32_16x16x32_bf16 v[36:39],a[220:223],a[36:39],v[36:39]
s_waitcnt vmcnt(12) lgkmcnt(0)
s_barrier
v_mfma_f32_16x16x32_bf16 v[40:43],a[224:227],a[20:23],v[40:43]
s_add_u32 m0,s58,49168
ds_read_b128 a[0:3],v248 offset:40960
v_mfma_f32_16x16x32_bf16 v[44:47],a[224:227],a[24:27],v[44:47]
buffer_load_dwordx4 v240,s[24:27],s52 offen lds
v_mfma_f32_16x16x32_bf16 v[48:51],a[224:227],a[28:31],v[48:51]
ds_read_b128 a[4:7],v248 offset:43008
ds_read_b128 a[8:11],v248 offset:45056
v_mfma_f32_16x16x32_bf16 v[52:55],a[224:227],a[32:35],v[52:55]
ds_read_b128 a[12:15],v248 offset:47104
v_mfma_f32_16x16x32_bf16 v[56:59],a[224:227],a[36:39],v[56:59]
s_add_u32 m0,s58,49168
ds_read_b128 a[16:19],v248 offset:49152
v_mfma_f32_16x16x32_bf16 v[60:63],a[228:231],a[20:23],v[60:63]
buffer_load_dwordx4 v241,s[24:27],s52 offen offset:1024 lds
v_mfma_f32_16x16x32_bf16 v[64:67],a[228:231],a[24:27],v[64:67]
ds_read_b64_tr_b16 a[200:201],v244 offset:256
ds_read_b64_tr_b16 a[202:203],v244 offset:1792
v_mfma_f32_16x16x32_bf16 v[68:71],a[228:231],a[28:31],v[68:71]
ds_read_b64_tr_b16 a[204:205],v244 offset:264
v_mfma_f32_16x16x32_bf16 v[72:75],a[228:231],a[32:35],v[72:75]
s_add_u32 m0,s58,49168
ds_read_b64_tr_b16 a[206:207],v244 offset:1800
v_mfma_f32_16x16x32_bf16 v[76:79],a[228:231],a[36:39],v[76:79]
buffer_load_dwordx4 v242,s[24:27],s52 offen offset:2048 lds
v_mfma_f32_16x16x32_bf16 v[80:83],a[232:235],a[20:23],v[80:83]
ds_read_b64_tr_b16 a[208:209],v245 offset:256
ds_read_b64_tr_b16 a[210:211],v245 offset:1792
v_mfma_f32_16x16x32_bf16 v[84:87],a[232:235],a[24:27],v[84:87]
s_add_u32 m0,s58,52240
ds_read_b64_tr_b16 a[212:213],v245 offset:264
v_mfma_f32_16x16x32_bf16 v[88:91],a[232:235],a[28:31],v[88:91]
s_add_u32 s66,s52,196608
ds_read_b64_tr_b16 a[214:215],v245 offset:1800
v_mfma_f32_16x16x32_bf16 v[92:95],a[232:235],a[32:35],v[92:95]
buffer_load_dwordx4 v240,s[24:27],s66 offen lds
v_mfma_f32_16x16x32_bf16 v[96:99],a[232:235],a[36:39],v[96:99]
ds_read_b64_tr_b16 a[216:217],v244
ds_read_b64_tr_b16 a[218:219],v244 offset:1536
v_mfma_f32_16x16x32_bf16 v[100:103],a[236:239],a[20:23],v[100:103]
ds_read_b64_tr_b16 a[220:221],v244 offset:8
v_mfma_f32_16x16x32_bf16 v[104:107],a[236:239],a[24:27],v[104:107]
s_add_u32 m0,s58,52240
ds_read_b64_tr_b16 a[222:223],v244 offset:1544
v_mfma_f32_16x16x32_bf16 v[108:111],a[236:239],a[28:31],v[108:111]
buffer_load_dwordx4 v241,s[24:27],s66 offen offset:1024 lds
v_mfma_f32_16x16x32_bf16 v[112:115],a[236:239],a[32:35],v[112:115]
v_mfma_f32_16x16x32_bf16 v[116:119],a[236:239],a[36:39],v[116:119]
v_mfma_f32_16x16x32_bf16 v[120:123],a[240:243],a[20:23],v[120:123]
s_add_u32 m0,s58,52240
v_mfma_f32_16x16x32_bf16 v[124:127],a[240:243],a[24:27],v[124:127]
buffer_load_dwordx4 v242,s[24:27],s66 offen offset:2048 lds
v_mfma_f32_16x16x32_bf16 v[128:131],a[240:243],a[28:31],v[128:131]
s_add_u32 s52,s52,393216
v_mfma_f32_16x16x32_bf16 v[132:135],a[240:243],a[32:35],v[132:135]
v_mfma_f32_16x16x32_bf16 v[136:139],a[240:243],a[36:39],v[136:139]
v_mfma_f32_16x16x32_bf16 v[140:143],a[244:247],a[20:23],v[140:143]
v_mfma_f32_16x16x32_bf16 v[144:147],a[244:247],a[24:27],v[144:147]
v_mfma_f32_16x16x32_bf16 v[148:151],a[244:247],a[28:31],v[148:151]
v_mfma_f32_16x16x32_bf16 v[152:155],a[244:247],a[32:35],v[152:155]
v_mfma_f32_16x16x32_bf16 v[156:159],a[244:247],a[36:39],v[156:159]
s_waitcnt lgkmcnt(0)
v_mfma_f32_16x16x32_bf16 v[160:163],a[200:203],a[0:3],v[160:163]
v_mfma_f32_16x16x32_bf16 v[164:167],a[200:203],a[4:7],v[164:167]
buffer_load_dword a250,v254,s[44:47],s74 offen
v_mfma_f32_16x16x32_bf16 v[168:171],a[200:203],a[8:11],v[168:171]
s_add_u32 m0,s59,20480
ds_read_b64_tr_b16 a[224:225],v245
ds_read_b64_tr_b16 a[226:227],v245 offset:1536
v_mfma_f32_16x16x32_bf16 v[172:175],a[200:203],a[12:15],v[172:175]
s_add_u32 s74,s74,786432
buffer_load_dwordx4 v243,s[20:23],s53 offen lds
v_mfma_f32_16x16x32_bf16 v[176:179],a[200:203],a[16:19],v[176:179]
s_add_u32 m0,s59,20480
ds_read_b64_tr_b16 a[228:229],v245 offset:8
ds_read_b64_tr_b16 a[230:231],v245 offset:1544
v_mfma_f32_16x16x32_bf16 v[180:183],a[204:207],a[0:3],v[180:183]
s_add_u32 s67,s53,9216
ds_read_b64_tr_b16 a[232:233],v244 offset:128
v_mfma_f32_16x16x32_bf16 v[184:187],a[204:207],a[4:7],v[184:187]
buffer_load_dwordx4 v243,s[20:23],s67 offen offset:1024 lds
v_mfma_f32_16x16x32_bf16 v[188:191],a[204:207],a[8:11],v[188:191]
s_add_u32 m0,s59,20480
ds_read_b64_tr_b16 a[234:235],v244 offset:1664
ds_read_b64_tr_b16 a[236:237],v244 offset:136
v_mfma_f32_16x16x32_bf16 v[192:195],a[204:207],a[12:15],v[192:195]
s_add_u32 s67,s53,18432
ds_read_b64_tr_b16 a[238:239],v244 offset:1672
v_mfma_f32_16x16x32_bf16 v[196:199],a[204:207],a[16:19],v[196:199]
buffer_load_dwordx4 v243,s[20:23],s67 offen offset:2048 lds
v_mfma_f32_16x16x32_bf16 v[200:203],a[208:211],a[0:3],v[200:203]
s_add_u32 m0,s59,20480
ds_read_b64_tr_b16 a[240:241],v245 offset:128
ds_read_b64_tr_b16 a[242:243],v245 offset:1664
v_mfma_f32_16x16x32_bf16 v[204:207],a[208:211],a[4:7],v[204:207]
s_add_u32 s67,s53,27648
ds_read_b64_tr_b16 a[244:245],v245 offset:136
v_mfma_f32_16x16x32_bf16 v[208:211],a[208:211],a[8:11],v[208:211]
buffer_load_dwordx4 v243,s[20:23],s67 offen offset:3072 lds
v_mfma_f32_16x16x32_bf16 v[212:215],a[208:211],a[12:15],v[212:215]
s_add_u32 m0,s59,24576
ds_read_b64_tr_b16 a[246:247],v245 offset:1672
v_mfma_f32_16x16x32_bf16 v[216:219],a[208:211],a[16:19],v[216:219]
s_add_u32 s67,s53,40960
v_mfma_f32_16x16x32_bf16 v[220:223],a[212:215],a[0:3],v[220:223]
buffer_load_dwordx4 v243,s[20:23],s67 offen lds
v_mfma_f32_16x16x32_bf16 v[224:227],a[212:215],a[4:7],v[224:227]
s_add_u32 s53,s53,128
v_mfma_f32_16x16x32_bf16 v[228:231],a[212:215],a[8:11],v[228:231]
v_mfma_f32_16x16x32_bf16 v[232:235],a[212:215],a[12:15],v[232:235]
v_mfma_f32_16x16x32_bf16 v[236:239],a[212:215],a[16:19],v[236:239]
v_mfma_f32_16x16x32_bf16 v[0:3],a[216:219],a[0:3],v[0:3]
v_mfma_f32_16x16x32_bf16 v[4:7],a[216:219],a[4:7],v[4:7]
v_mfma_f32_16x16x32_bf16 v[8:11],a[216:219],a[8:11],v[8:11]
v_mfma_f32_16x16x32_bf16 v[12:15],a[216:219],a[12:15],v[12:15]
v_mfma_f32_16x16x32_bf16 v[16:19],a[216:219],a[16:19],v[16:19]
v_mfma_f32_16x16x32_bf16 v[20:23],a[220:223],a[0:3],v[20:23]
v_mfma_f32_16x16x32_bf16 v[24:27],a[220:223],a[4:7],v[24:27]
v_mfma_f32_16x16x32_bf16 v[28:31],a[220:223],a[8:11],v[28:31]
v_mfma_f32_16x16x32_bf16 v[32:35],a[220:223],a[12:15],v[32:35]
v_mfma_f32_16x16x32_bf16 v[36:39],a[220:223],a[16:19],v[36:39]
s_waitcnt vmcnt(12) lgkmcnt(0)
s_barrier
v_mfma_f32_16x16x32_bf16 v[40:43],a[224:227],a[0:3],v[40:43]
ds_read_b128 a[20:23],v249 offset:40960
v_mfma_f32_16x16x32_bf16 v[44:47],a[224:227],a[4:7],v[44:47]
ds_read_b128 a[24:27],v249 offset:43008
v_mfma_f32_16x16x32_bf16 v[48:51],a[224:227],a[8:11],v[48:51]
ds_read_b128 a[28:31],v249 offset:45056
v_mfma_f32_16x16x32_bf16 v[52:55],a[224:227],a[12:15],v[52:55]
ds_read_b128 a[32:35],v249 offset:47104
v_mfma_f32_16x16x32_bf16 v[56:59],a[224:227],a[16:19],v[56:59]
ds_read_b128 a[36:39],v249 offset:49152
v_mfma_f32_16x16x32_bf16 v[60:63],a[228:231],a[0:3],v[60:63]
ds_read_b64_tr_b16 a[200:201],v244 offset:24840
v_mfma_f32_16x16x32_bf16 v[64:67],a[228:231],a[4:7],v[64:67]
ds_read_b64_tr_b16 a[202:203],v244 offset:26376
v_mfma_f32_16x16x32_bf16 v[68:71],a[228:231],a[8:11],v[68:71]
ds_read_b64_tr_b16 a[204:205],v244 offset:24848
v_mfma_f32_16x16x32_bf16 v[72:75],a[228:231],a[12:15],v[72:75]
ds_read_b64_tr_b16 a[206:207],v244 offset:26384
v_mfma_f32_16x16x32_bf16 v[76:79],a[228:231],a[16:19],v[76:79]
ds_read_b64_tr_b16 a[208:209],v245 offset:24840
v_mfma_f32_16x16x32_bf16 v[80:83],a[232:235],a[0:3],v[80:83]
ds_read_b64_tr_b16 a[210:211],v245 offset:26376
v_mfma_f32_16x16x32_bf16 v[84:87],a[232:235],a[4:7],v[84:87]
ds_read_b64_tr_b16 a[212:213],v245 offset:24848
v_mfma_f32_16x16x32_bf16 v[88:91],a[232:235],a[8:11],v[88:91]
ds_read_b64_tr_b16 a[214:215],v245 offset:26384
v_mfma_f32_16x16x32_bf16 v[92:95],a[232:235],a[12:15],v[92:95]
ds_read_b64_tr_b16 a[216:217],v244 offset:24584
v_mfma_f32_16x16x32_bf16 v[96:99],a[232:235],a[16:19],v[96:99]
ds_read_b64_tr_b16 a[218:219],v244 offset:26120
v_mfma_f32_16x16x32_bf16 v[100:103],a[236:239],a[0:3],v[100:103]
ds_read_b64_tr_b16 a[220:221],v244 offset:24592
v_mfma_f32_16x16x32_bf16 v[104:107],a[236:239],a[4:7],v[104:107]
ds_read_b64_tr_b16 a[222:223],v244 offset:26128
v_mfma_f32_16x16x32_bf16 v[108:111],a[236:239],a[8:11],v[108:111]
v_mfma_f32_16x16x32_bf16 v[112:115],a[236:239],a[12:15],v[112:115]
v_mfma_f32_16x16x32_bf16 v[116:119],a[236:239],a[16:19],v[116:119]
v_mfma_f32_16x16x32_bf16 v[120:123],a[240:243],a[0:3],v[120:123]
v_mfma_f32_16x16x32_bf16 v[124:127],a[240:243],a[4:7],v[124:127]
v_mfma_f32_16x16x32_bf16 v[128:131],a[240:243],a[8:11],v[128:131]
v_mfma_f32_16x16x32_bf16 v[132:135],a[240:243],a[12:15],v[132:135]
v_mfma_f32_16x16x32_bf16 v[136:139],a[240:243],a[16:19],v[136:139]
v_mfma_f32_16x16x32_bf16 v[140:143],a[244:247],a[0:3],v[140:143]
v_mfma_f32_16x16x32_bf16 v[144:147],a[244:247],a[4:7],v[144:147]
v_mfma_f32_16x16x32_bf16 v[148:151],a[244:247],a[8:11],v[148:151]
v_mfma_f32_16x16x32_bf16 v[152:155],a[244:247],a[12:15],v[152:155]
v_mfma_f32_16x16x32_bf16 v[156:159],a[244:247],a[16:19],v[156:159]
s_waitcnt lgkmcnt(0)
v_mfma_f32_16x16x32_bf16 v[160:163],a[200:203],a[20:23],v[160:163]
v_mfma_f32_16x16x32_bf16 v[164:167],a[200:203],a[24:27],v[164:167]
ds_read_b64_tr_b16 a[224:225],v245 offset:24584
v_mfma_f32_16x16x32_bf16 v[168:171],a[200:203],a[28:31],v[168:171]
ds_read_b64_tr_b16 a[226:227],v245 offset:26120
v_mfma_f32_16x16x32_bf16 v[172:175],a[200:203],a[32:35],v[172:175]
s_add_u32 m0,s58,73752
ds_read_b64_tr_b16 a[228:229],v245 offset:24592
v_mfma_f32_16x16x32_bf16 v[176:179],a[200:203],a[36:39],v[176:179]
buffer_load_dwordx4 v240,s[24:27],s52 offen lds
v_mfma_f32_16x16x32_bf16 v[180:183],a[204:207],a[20:23],v[180:183]
ds_read_b64_tr_b16 a[230:231],v245 offset:26128
ds_read_b64_tr_b16 a[232:233],v244 offset:24712
v_mfma_f32_16x16x32_bf16 v[184:187],a[204:207],a[24:27],v[184:187]
ds_read_b64_tr_b16 a[234:235],v244 offset:26248
v_mfma_f32_16x16x32_bf16 v[188:191],a[204:207],a[28:31],v[188:191]
s_add_u32 m0,s58,73752
ds_read_b64_tr_b16 a[236:237],v244 offset:24720
v_mfma_f32_16x16x32_bf16 v[192:195],a[204:207],a[32:35],v[192:195]
buffer_load_dwordx4 v241,s[24:27],s52 offen offset:1024 lds
v_mfma_f32_16x16x32_bf16 v[196:199],a[204:207],a[36:39],v[196:199]
ds_read_b64_tr_b16 a[238:239],v244 offset:26256
ds_read_b64_tr_b16 a[240:241],v245 offset:24712
v_mfma_f32_16x16x32_bf16 v[200:203],a[208:211],a[20:23],v[200:203]
ds_read_b64_tr_b16 a[242:243],v245 offset:26248
v_mfma_f32_16x16x32_bf16 v[204:207],a[208:211],a[24:27],v[204:207]
s_add_u32 m0,s58,73752
ds_read_b64_tr_b16 a[244:245],v245 offset:24720
v_mfma_f32_16x16x32_bf16 v[208:211],a[208:211],a[28:31],v[208:211]
buffer_load_dwordx4 v242,s[24:27],s52 offen offset:2048 lds
v_mfma_f32_16x16x32_bf16 v[212:215],a[208:211],a[32:35],v[212:215]
ds_read_b64_tr_b16 a[246:247],v245 offset:26256
v_mfma_f32_16x16x32_bf16 v[216:219],a[208:211],a[36:39],v[216:219]
s_add_u32 m0,s58,76824
v_mfma_f32_16x16x32_bf16 v[220:223],a[212:215],a[20:23],v[220:223]
s_add_u32 s66,s52,196608
v_mfma_f32_16x16x32_bf16 v[224:227],a[212:215],a[24:27],v[224:227]
buffer_load_dwordx4 v240,s[24:27],s66 offen lds
v_mfma_f32_16x16x32_bf16 v[228:231],a[212:215],a[28:31],v[228:231]
v_mfma_f32_16x16x32_bf16 v[232:235],a[212:215],a[32:35],v[232:235]
v_mfma_f32_16x16x32_bf16 v[236:239],a[212:215],a[36:39],v[236:239]
s_add_u32 m0,s58,76824
v_mfma_f32_16x16x32_bf16 v[0:3],a[216:219],a[20:23],v[0:3]
buffer_load_dwordx4 v241,s[24:27],s66 offen offset:1024 lds
v_mfma_f32_16x16x32_bf16 v[4:7],a[216:219],a[24:27],v[4:7]
v_mfma_f32_16x16x32_bf16 v[8:11],a[216:219],a[28:31],v[8:11]
v_mfma_f32_16x16x32_bf16 v[12:15],a[216:219],a[32:35],v[12:15]
s_add_u32 m0,s58,76824
v_mfma_f32_16x16x32_bf16 v[16:19],a[216:219],a[36:39],v[16:19]
buffer_load_dwordx4 v242,s[24:27],s66 offen offset:2048 lds
v_mfma_f32_16x16x32_bf16 v[20:23],a[220:223],a[20:23],v[20:23]
s_add_u32 s52,s52,393216
v_mfma_f32_16x16x32_bf16 v[24:27],a[220:223],a[24:27],v[24:27]
v_mfma_f32_16x16x32_bf16 v[28:31],a[220:223],a[28:31],v[28:31]
v_mfma_f32_16x16x32_bf16 v[32:35],a[220:223],a[32:35],v[32:35]
v_mfma_f32_16x16x32_bf16 v[36:39],a[220:223],a[36:39],v[36:39]
s_waitcnt vmcnt(12) lgkmcnt(0)
s_barrier
v_mfma_f32_16x16x32_bf16 v[40:43],a[224:227],a[20:23],v[40:43]
s_add_u32 m0,s58,0
ds_read_b128 a[0:3],v248
v_mfma_f32_16x16x32_bf16 v[44:47],a[224:227],a[24:27],v[44:47]
buffer_load_dwordx4 v240,s[24:27],s52 offen lds
v_mfma_f32_16x16x32_bf16 v[48:51],a[224:227],a[28:31],v[48:51]
ds_read_b128 a[4:7],v248 offset:2048
ds_read_b128 a[8:11],v248 offset:4096
v_mfma_f32_16x16x32_bf16 v[52:55],a[224:227],a[32:35],v[52:55]
ds_read_b128 a[12:15],v248 offset:6144
v_mfma_f32_16x16x32_bf16 v[56:59],a[224:227],a[36:39],v[56:59]
s_add_u32 m0,s58,0
ds_read_b128 a[16:19],v248 offset:8192
v_mfma_f32_16x16x32_bf16 v[60:63],a[228:231],a[20:23],v[60:63]
buffer_load_dwordx4 v241,s[24:27],s52 offen offset:1024 lds
v_mfma_f32_16x16x32_bf16 v[64:67],a[228:231],a[24:27],v[64:67]
ds_read_b64_tr_b16 a[200:201],v246 offset:256
ds_read_b64_tr_b16 a[202:203],v246 offset:1792
v_mfma_f32_16x16x32_bf16 v[68:71],a[228:231],a[28:31],v[68:71]
ds_read_b64_tr_b16 a[204:205],v246 offset:264
v_mfma_f32_16x16x32_bf16 v[72:75],a[228:231],a[32:35],v[72:75]
s_add_u32 m0,s58,0
ds_read_b64_tr_b16 a[206:207],v246 offset:1800
v_mfma_f32_16x16x32_bf16 v[76:79],a[228:231],a[36:39],v[76:79]
buffer_load_dwordx4 v242,s[24:27],s52 offen offset:2048 lds
v_mfma_f32_16x16x32_bf16 v[80:83],a[232:235],a[20:23],v[80:83]
ds_read_b64_tr_b16 a[208:209],v247 offset:256
ds_read_b64_tr_b16 a[210:211],v247 offset:1792
v_mfma_f32_16x16x32_bf16 v[84:87],a[232:235],a[24:27],v[84:87]
s_add_u32 m0,s58,3072
ds_read_b64_tr_b16 a[212:213],v247 offset:264
v_mfma_f32_16x16x32_bf16 v[88:91],a[232:235],a[28:31],v[88:91]
s_add_u32 s66,s52,196608
ds_read_b64_tr_b16 a[214:215],v247 offset:1800
v_mfma_f32_16x16x32_bf16 v[92:95],a[232:235],a[32:35],v[92:95]
buffer_load_dwordx4 v240,s[24:27],s66 offen lds
v_mfma_f32_16x16x32_bf16 v[96:99],a[232:235],a[36:39],v[96:99]
ds_read_b64_tr_b16 a[216:217],v246
ds_read_b64_tr_b16 a[218:219],v246 offset:1536
v_mfma_f32_16x16x32_bf16 v[100:103],a[236:239],a[20:23],v[100:103]
ds_read_b64_tr_b16 a[220:221],v246 offset:8
v_mfma_f32_16x16x32_bf16 v[104:107],a[236:239],a[24:27],v[104:107]
s_add_u32 m0,s58,3072
ds_read_b64_tr_b16 a[222:223],v246 offset:1544
v_mfma_f32_16x16x32_bf16 v[108:111],a[236:239],a[28:31],v[108:111]
buffer_load_dwordx4 v241,s[24:27],s66 offen offset:1024 lds
v_mfma_f32_16x16x32_bf16 v[112:115],a[236:239],a[32:35],v[112:115]
v_mfma_f32_16x16x32_bf16 v[116:119],a[236:239],a[36:39],v[116:119]
v_mfma_f32_16x16x32_bf16 v[120:123],a[240:243],a[20:23],v[120:123]
s_add_u32 m0,s58,3072
v_mfma_f32_16x16x32_bf16 v[124:127],a[240:243],a[24:27],v[124:127]
buffer_load_dwordx4 v242,s[24:27],s66 offen offset:2048 lds
v_mfma_f32_16x16x32_bf16 v[128:131],a[240:243],a[28:31],v[128:131]
s_add_u32 s52,s52,393216
v_mfma_f32_16x16x32_bf16 v[132:135],a[240:243],a[32:35],v[132:135]
v_mfma_f32_16x16x32_bf16 v[136:139],a[240:243],a[36:39],v[136:139]
v_mfma_f32_16x16x32_bf16 v[140:143],a[244:247],a[20:23],v[140:143]
v_mfma_f32_16x16x32_bf16 v[144:147],a[244:247],a[24:27],v[144:147]
v_mfma_f32_16x16x32_bf16 v[148:151],a[244:247],a[28:31],v[148:151]
v_mfma_f32_16x16x32_bf16 v[152:155],a[244:247],a[32:35],v[152:155]
v_mfma_f32_16x16x32_bf16 v[156:159],a[244:247],a[36:39],v[156:159]
s_waitcnt lgkmcnt(0)
v_mfma_f32_16x16x32_bf16 v[160:163],a[200:203],a[0:3],v[160:163]
v_mfma_f32_16x16x32_bf16 v[164:167],a[200:203],a[4:7],v[164:167]
ds_read_b64_tr_b16 a[224:225],v247
v_mfma_f32_16x16x32_bf16 v[168:171],a[200:203],a[8:11],v[168:171]
s_add_u32 m0,s59,40960
ds_read_b64_tr_b16 a[226:227],v247 offset:1536
v_mfma_f32_16x16x32_bf16 v[172:175],a[200:203],a[12:15],v[172:175]
buffer_load_dwordx4 v243,s[20:23],s53 offen lds
v_mfma_f32_16x16x32_bf16 v[176:179],a[200:203],a[16:19],v[176:179]
s_add_u32 m0,s59,40960
ds_read_b64_tr_b16 a[228:229],v247 offset:8
ds_read_b64_tr_b16 a[230:231],v247 offset:1544
v_mfma_f32_16x16x32_bf16 v[180:183],a[204:207],a[0:3],v[180:183]
s_add_u32 s67,s53,9216
ds_read_b64_tr_b16 a[232:233],v246 offset:128
v_mfma_f32_16x16x32_bf16 v[184:187],a[204:207],a[4:7],v[184:187]
buffer_load_dwordx4 v243,s[20:23],s67 offen offset:1024 lds
v_mfma_f32_16x16x32_bf16 v[188:191],a[204:207],a[8:11],v[188:191]
s_add_u32 m0,s59,40960
ds_read_b64_tr_b16 a[234:235],v246 offset:1664
ds_read_b64_tr_b16 a[236:237],v246 offset:136
v_mfma_f32_16x16x32_bf16 v[192:195],a[204:207],a[12:15],v[192:195]
s_add_u32 s67,s53,18432
ds_read_b64_tr_b16 a[238:239],v246 offset:1672
v_mfma_f32_16x16x32_bf16 v[196:199],a[204:207],a[16:19],v[196:199]
buffer_load_dwordx4 v243,s[20:23],s67 offen offset:2048 lds
v_mfma_f32_16x16x32_bf16 v[200:203],a[208:211],a[0:3],v[200:203]
s_add_u32 m0,s59,40960
ds_read_b64_tr_b16 a[240:241],v247 offset:128
ds_read_b64_tr_b16 a[242:243],v247 offset:1664
v_mfma_f32_16x16x32_bf16 v[204:207],a[208:211],a[4:7],v[204:207]
s_add_u32 s67,s53,27648
ds_read_b64_tr_b16 a[244:245],v247 offset:136
v_mfma_f32_16x16x32_bf16 v[208:211],a[208:211],a[8:11],v[208:211]
buffer_load_dwordx4 v243,s[20:23],s67 offen offset:3072 lds
v_mfma_f32_16x16x32_bf16 v[212:215],a[208:211],a[12:15],v[212:215]
s_add_u32 m0,s59,45056
ds_read_b64_tr_b16 a[246:247],v247 offset:1672
v_mfma_f32_16x16x32_bf16 v[216:219],a[208:211],a[16:19],v[216:219]
s_add_u32 s67,s53,40960
v_mfma_f32_16x16x32_bf16 v[220:223],a[212:215],a[0:3],v[220:223]
buffer_load_dwordx4 v243,s[20:23],s67 offen lds
v_mfma_f32_16x16x32_bf16 v[224:227],a[212:215],a[4:7],v[224:227]
s_add_u32 s53,s53,128
v_mfma_f32_16x16x32_bf16 v[228:231],a[212:215],a[8:11],v[228:231]
v_mfma_f32_16x16x32_bf16 v[232:235],a[212:215],a[12:15],v[232:235]
v_mfma_f32_16x16x32_bf16 v[236:239],a[212:215],a[16:19],v[236:239]
v_mfma_f32_16x16x32_bf16 v[0:3],a[216:219],a[0:3],v[0:3]
v_mfma_f32_16x16x32_bf16 v[4:7],a[216:219],a[4:7],v[4:7]
v_mfma_f32_16x16x32_bf16 v[8:11],a[216:219],a[8:11],v[8:11]
v_mfma_f32_16x16x32_bf16 v[12:15],a[216:219],a[12:15],v[12:15]
v_mfma_f32_16x16x32_bf16 v[16:19],a[216:219],a[16:19],v[16:19]
v_mfma_f32_16x16x32_bf16 v[20:23],a[220:223],a[0:3],v[20:23]
v_mfma_f32_16x16x32_bf16 v[24:27],a[220:223],a[4:7],v[24:27]
v_mfma_f32_16x16x32_bf16 v[28:31],a[220:223],a[8:11],v[28:31]
v_mfma_f32_16x16x32_bf16 v[32:35],a[220:223],a[12:15],v[32:35]
v_mfma_f32_16x16x32_bf16 v[36:39],a[220:223],a[16:19],v[36:39]
s_waitcnt vmcnt(11) lgkmcnt(0)
s_barrier
v_mfma_f32_16x16x32_bf16 v[40:43],a[224:227],a[0:3],v[40:43]
ds_read_b128 a[20:23],v249
v_mfma_f32_16x16x32_bf16 v[44:47],a[224:227],a[4:7],v[44:47]
ds_read_b128 a[24:27],v249 offset:2048
v_mfma_f32_16x16x32_bf16 v[48:51],a[224:227],a[8:11],v[48:51]
ds_read_b128 a[28:31],v249 offset:4096
v_mfma_f32_16x16x32_bf16 v[52:55],a[224:227],a[12:15],v[52:55]
ds_read_b128 a[32:35],v249 offset:6144
v_mfma_f32_16x16x32_bf16 v[56:59],a[224:227],a[16:19],v[56:59]
ds_read_b128 a[36:39],v249 offset:8192
v_mfma_f32_16x16x32_bf16 v[60:63],a[228:231],a[0:3],v[60:63]
ds_read_b64_tr_b16 a[200:201],v246 offset:24840
v_mfma_f32_16x16x32_bf16 v[64:67],a[228:231],a[4:7],v[64:67]
ds_read_b64_tr_b16 a[202:203],v246 offset:26376
v_mfma_f32_16x16x32_bf16 v[68:71],a[228:231],a[8:11],v[68:71]
ds_read_b64_tr_b16 a[204:205],v246 offset:24848
v_mfma_f32_16x16x32_bf16 v[72:75],a[228:231],a[12:15],v[72:75]
ds_read_b64_tr_b16 a[206:207],v246 offset:26384
v_mfma_f32_16x16x32_bf16 v[76:79],a[228:231],a[16:19],v[76:79]
ds_read_b64_tr_b16 a[208:209],v247 offset:24840
v_mfma_f32_16x16x32_bf16 v[80:83],a[232:235],a[0:3],v[80:83]
ds_read_b64_tr_b16 a[210:211],v247 offset:26376
v_mfma_f32_16x16x32_bf16 v[84:87],a[232:235],a[4:7],v[84:87]
ds_read_b64_tr_b16 a[212:213],v247 offset:24848
v_mfma_f32_16x16x32_bf16 v[88:91],a[232:235],a[8:11],v[88:91]
ds_read_b64_tr_b16 a[214:215],v247 offset:26384
v_mfma_f32_16x16x32_bf16 v[92:95],a[232:235],a[12:15],v[92:95]
ds_read_b64_tr_b16 a[216:217],v246 offset:24584
v_mfma_f32_16x16x32_bf16 v[96:99],a[232:235],a[16:19],v[96:99]
ds_read_b64_tr_b16 a[218:219],v246 offset:26120
v_mfma_f32_16x16x32_bf16 v[100:103],a[236:239],a[0:3],v[100:103]
ds_read_b64_tr_b16 a[220:221],v246 offset:24592
v_mfma_f32_16x16x32_bf16 v[104:107],a[236:239],a[4:7],v[104:107]
ds_read_b64_tr_b16 a[222:223],v246 offset:26128
v_mfma_f32_16x16x32_bf16 v[108:111],a[236:239],a[8:11],v[108:111]
v_mfma_f32_16x16x32_bf16 v[112:115],a[236:239],a[12:15],v[112:115]
v_mfma_f32_16x16x32_bf16 v[116:119],a[236:239],a[16:19],v[116:119]
v_mfma_f32_16x16x32_bf16 v[120:123],a[240:243],a[0:3],v[120:123]
v_mfma_f32_16x16x32_bf16 v[124:127],a[240:243],a[4:7],v[124:127]
v_mfma_f32_16x16x32_bf16 v[128:131],a[240:243],a[8:11],v[128:131]
v_mfma_f32_16x16x32_bf16 v[132:135],a[240:243],a[12:15],v[132:135]
v_mfma_f32_16x16x32_bf16 v[136:139],a[240:243],a[16:19],v[136:139]
v_mfma_f32_16x16x32_bf16 v[140:143],a[244:247],a[0:3],v[140:143]
v_mfma_f32_16x16x32_bf16 v[144:147],a[244:247],a[4:7],v[144:147]
v_mfma_f32_16x16x32_bf16 v[148:151],a[244:247],a[8:11],v[148:151]
v_mfma_f32_16x16x32_bf16 v[152:155],a[244:247],a[12:15],v[152:155]
v_mfma_f32_16x16x32_bf16 v[156:159],a[244:247],a[16:19],v[156:159]
s_waitcnt lgkmcnt(0)
v_mfma_f32_16x16x32_bf16 v[160:163],a[200:203],a[20:23],v[160:163]
v_mfma_f32_16x16x32_bf16 v[164:167],a[200:203],a[24:27],v[164:167]
ds_read_b64_tr_b16 a[224:225],v247 offset:24584
v_mfma_f32_16x16x32_bf16 v[168:171],a[200:203],a[28:31],v[168:171]
ds_read_b64_tr_b16 a[226:227],v247 offset:26120
v_mfma_f32_16x16x32_bf16 v[172:175],a[200:203],a[32:35],v[172:175]
s_add_u32 m0,s58,24584
ds_read_b64_tr_b16 a[228:229],v247 offset:24592
v_mfma_f32_16x16x32_bf16 v[176:179],a[200:203],a[36:39],v[176:179]
buffer_load_dwordx4 v240,s[24:27],s52 offen lds
v_mfma_f32_16x16x32_bf16 v[180:183],a[204:207],a[20:23],v[180:183]
ds_read_b64_tr_b16 a[230:231],v247 offset:26128
ds_read_b64_tr_b16 a[232:233],v246 offset:24712
v_mfma_f32_16x16x32_bf16 v[184:187],a[204:207],a[24:27],v[184:187]
ds_read_b64_tr_b16 a[234:235],v246 offset:26248
v_mfma_f32_16x16x32_bf16 v[188:191],a[204:207],a[28:31],v[188:191]
s_add_u32 m0,s58,24584
ds_read_b64_tr_b16 a[236:237],v246 offset:24720
v_mfma_f32_16x16x32_bf16 v[192:195],a[204:207],a[32:35],v[192:195]
buffer_load_dwordx4 v241,s[24:27],s52 offen offset:1024 lds
v_mfma_f32_16x16x32_bf16 v[196:199],a[204:207],a[36:39],v[196:199]
ds_read_b64_tr_b16 a[238:239],v246 offset:26256
ds_read_b64_tr_b16 a[240:241],v247 offset:24712
v_mfma_f32_16x16x32_bf16 v[200:203],a[208:211],a[20:23],v[200:203]
ds_read_b64_tr_b16 a[242:243],v247 offset:26248
v_mfma_f32_16x16x32_bf16 v[204:207],a[208:211],a[24:27],v[204:207]
s_add_u32 m0,s58,24584
ds_read_b64_tr_b16 a[244:245],v247 offset:24720
v_mfma_f32_16x16x32_bf16 v[208:211],a[208:211],a[28:31],v[208:211]
buffer_load_dwordx4 v242,s[24:27],s52 offen offset:2048 lds
v_mfma_f32_16x16x32_bf16 v[212:215],a[208:211],a[32:35],v[212:215]
ds_read_b64_tr_b16 a[246:247],v247 offset:26256
v_mfma_f32_16x16x32_bf16 v[216:219],a[208:211],a[36:39],v[216:219]
s_add_u32 m0,s58,27656
v_mfma_f32_16x16x32_bf16 v[220:223],a[212:215],a[20:23],v[220:223]
s_add_u32 s66,s52,196608
v_mfma_f32_16x16x32_bf16 v[224:227],a[212:215],a[24:27],v[224:227]
buffer_load_dwordx4 v240,s[24:27],s66 offen lds
v_mfma_f32_16x16x32_bf16 v[228:231],a[212:215],a[28:31],v[228:231]
v_mfma_f32_16x16x32_bf16 v[232:235],a[212:215],a[32:35],v[232:235]
v_mfma_f32_16x16x32_bf16 v[236:239],a[212:215],a[36:39],v[236:239]
s_add_u32 m0,s58,27656
v_mfma_f32_16x16x32_bf16 v[0:3],a[216:219],a[20:23],v[0:3]
buffer_load_dwordx4 v241,s[24:27],s66 offen offset:1024 lds
v_mfma_f32_16x16x32_bf16 v[4:7],a[216:219],a[24:27],v[4:7]
v_mfma_f32_16x16x32_bf16 v[8:11],a[216:219],a[28:31],v[8:11]
v_mfma_f32_16x16x32_bf16 v[12:15],a[216:219],a[32:35],v[12:15]
s_add_u32 m0,s58,27656
v_mfma_f32_16x16x32_bf16 v[16:19],a[216:219],a[36:39],v[16:19]
buffer_load_dwordx4 v242,s[24:27],s66 offen offset:2048 lds
v_mfma_f32_16x16x32_bf16 v[20:23],a[220:223],a[20:23],v[20:23]
s_add_u32 s52,s52,393216
v_mfma_f32_16x16x32_bf16 v[24:27],a[220:223],a[24:27],v[24:27]
v_mfma_f32_16x16x32_bf16 v[28:31],a[220:223],a[28:31],v[28:31]
v_mfma_f32_16x16x32_bf16 v[32:35],a[220:223],a[32:35],v[32:35]
v_mfma_f32_16x16x32_bf16 v[36:39],a[220:223],a[36:39],v[36:39]
s_waitcnt vmcnt(11) lgkmcnt(0)
s_barrier
v_mfma_f32_16x16x32_bf16 v[40:43],a[224:227],a[20:23],v[40:43]
s_add_u32 m0,s58,49168
ds_read_b128 a[0:3],v248 offset:20480
v_mfma_f32_16x16x32_bf16 v[44:47],a[224:227],a[24:27],v[44:47]
buffer_load_dwordx4 v240,s[24:27],s52 offen lds
v_mfma_f32_16x16x32_bf16 v[48:51],a[224:227],a[28:31],v[48:51]
ds_read_b128 a[4:7],v248 offset:22528
ds_read_b128 a[8:11],v248 offset:24576
v_mfma_f32_16x16x32_bf16 v[52:55],a[224:227],a[32:35],v[52:55]
ds_read_b128 a[12:15],v248 offset:26624
v_mfma_f32_16x16x32_bf16 v[56:59],a[224:227],a[36:39],v[56:59]
s_add_u32 m0,s58,49168
ds_read_b128 a[16:19],v248 offset:28672
v_mfma_f32_16x16x32_bf16 v[60:63],a[228:231],a[20:23],v[60:63]
buffer_load_dwordx4 v241,s[24:27],s52 offen offset:1024 lds
v_mfma_f32_16x16x32_bf16 v[64:67],a[228:231],a[24:27],v[64:67]
ds_read_b64_tr_b16 a[200:201],v244 offset:256
ds_read_b64_tr_b16 a[202:203],v244 offset:1792
v_mfma_f32_16x16x32_bf16 v[68:71],a[228:231],a[28:31],v[68:71]
ds_read_b64_tr_b16 a[204:205],v244 offset:264
v_mfma_f32_16x16x32_bf16 v[72:75],a[228:231],a[32:35],v[72:75]
s_add_u32 m0,s58,49168
ds_read_b64_tr_b16 a[206:207],v244 offset:1800
v_mfma_f32_16x16x32_bf16 v[76:79],a[228:231],a[36:39],v[76:79]
buffer_load_dwordx4 v242,s[24:27],s52 offen offset:2048 lds
v_mfma_f32_16x16x32_bf16 v[80:83],a[232:235],a[20:23],v[80:83]
ds_read_b64_tr_b16 a[208:209],v245 offset:256
ds_read_b64_tr_b16 a[210:211],v245 offset:1792
v_mfma_f32_16x16x32_bf16 v[84:87],a[232:235],a[24:27],v[84:87]
s_add_u32 m0,s58,52240
ds_read_b64_tr_b16 a[212:213],v245 offset:264
v_mfma_f32_16x16x32_bf16 v[88:91],a[232:235],a[28:31],v[88:91]
s_add_u32 s66,s52,196608
ds_read_b64_tr_b16 a[214:215],v245 offset:1800
v_mfma_f32_16x16x32_bf16 v[92:95],a[232:235],a[32:35],v[92:95]
buffer_load_dwordx4 v240,s[24:27],s66 offen lds
v_mfma_f32_16x16x32_bf16 v[96:99],a[232:235],a[36:39],v[96:99]
ds_read_b64_tr_b16 a[216:217],v244
ds_read_b64_tr_b16 a[218:219],v244 offset:1536
v_mfma_f32_16x16x32_bf16 v[100:103],a[236:239],a[20:23],v[100:103]
ds_read_b64_tr_b16 a[220:221],v244 offset:8
v_mfma_f32_16x16x32_bf16 v[104:107],a[236:239],a[24:27],v[104:107]
s_add_u32 m0,s58,52240
ds_read_b64_tr_b16 a[222:223],v244 offset:1544
v_mfma_f32_16x16x32_bf16 v[108:111],a[236:239],a[28:31],v[108:111]
buffer_load_dwordx4 v241,s[24:27],s66 offen offset:1024 lds
v_mfma_f32_16x16x32_bf16 v[112:115],a[236:239],a[32:35],v[112:115]
v_mfma_f32_16x16x32_bf16 v[116:119],a[236:239],a[36:39],v[116:119]
v_mfma_f32_16x16x32_bf16 v[120:123],a[240:243],a[20:23],v[120:123]
s_add_u32 m0,s58,52240
v_mfma_f32_16x16x32_bf16 v[124:127],a[240:243],a[24:27],v[124:127]
buffer_load_dwordx4 v242,s[24:27],s66 offen offset:2048 lds
v_mfma_f32_16x16x32_bf16 v[128:131],a[240:243],a[28:31],v[128:131]
s_add_u32 s52,s52,393216
v_mfma_f32_16x16x32_bf16 v[132:135],a[240:243],a[32:35],v[132:135]
v_mfma_f32_16x16x32_bf16 v[136:139],a[240:243],a[36:39],v[136:139]
v_mfma_f32_16x16x32_bf16 v[140:143],a[244:247],a[20:23],v[140:143]
v_mfma_f32_16x16x32_bf16 v[144:147],a[244:247],a[24:27],v[144:147]
v_mfma_f32_16x16x32_bf16 v[148:151],a[244:247],a[28:31],v[148:151]
v_mfma_f32_16x16x32_bf16 v[152:155],a[244:247],a[32:35],v[152:155]
v_mfma_f32_16x16x32_bf16 v[156:159],a[244:247],a[36:39],v[156:159]
s_waitcnt lgkmcnt(0)
v_mfma_f32_16x16x32_bf16 v[160:163],a[200:203],a[0:3],v[160:163]
v_mfma_f32_16x16x32_bf16 v[164:167],a[200:203],a[4:7],v[164:167]
ds_read_b64_tr_b16 a[224:225],v245
v_mfma_f32_16x16x32_bf16 v[168:171],a[200:203],a[8:11],v[168:171]
s_add_u32 m0,s58,73752
ds_read_b64_tr_b16 a[226:227],v245 offset:1536
v_mfma_f32_16x16x32_bf16 v[172:175],a[200:203],a[12:15],v[172:175]
buffer_load_dwordx4 v240,s[24:27],s52 offen lds
v_mfma_f32_16x16x32_bf16 v[176:179],a[200:203],a[16:19],v[176:179]
ds_read_b64_tr_b16 a[228:229],v245 offset:8
ds_read_b64_tr_b16 a[230:231],v245 offset:1544
v_mfma_f32_16x16x32_bf16 v[180:183],a[204:207],a[0:3],v[180:183]
ds_read_b64_tr_b16 a[232:233],v244 offset:128
v_mfma_f32_16x16x32_bf16 v[184:187],a[204:207],a[4:7],v[184:187]
s_add_u32 m0,s58,73752
ds_read_b64_tr_b16 a[234:235],v244 offset:1664
v_mfma_f32_16x16x32_bf16 v[188:191],a[204:207],a[8:11],v[188:191]
buffer_load_dwordx4 v241,s[24:27],s52 offen offset:1024 lds
v_mfma_f32_16x16x32_bf16 v[192:195],a[204:207],a[12:15],v[192:195]
ds_read_b64_tr_b16 a[236:237],v244 offset:136
ds_read_b64_tr_b16 a[238:239],v244 offset:1672
v_mfma_f32_16x16x32_bf16 v[196:199],a[204:207],a[16:19],v[196:199]
ds_read_b64_tr_b16 a[240:241],v245 offset:128
v_mfma_f32_16x16x32_bf16 v[200:203],a[208:211],a[0:3],v[200:203]
s_add_u32 m0,s58,73752
ds_read_b64_tr_b16 a[242:243],v245 offset:1664
v_mfma_f32_16x16x32_bf16 v[204:207],a[208:211],a[4:7],v[204:207]
buffer_load_dwordx4 v242,s[24:27],s52 offen offset:2048 lds
v_mfma_f32_16x16x32_bf16 v[208:211],a[208:211],a[8:11],v[208:211]
ds_read_b64_tr_b16 a[244:245],v245 offset:136
ds_read_b64_tr_b16 a[246:247],v245 offset:1672
v_mfma_f32_16x16x32_bf16 v[212:215],a[208:211],a[12:15],v[212:215]
s_add_u32 m0,s58,76824
v_mfma_f32_16x16x32_bf16 v[216:219],a[208:211],a[16:19],v[216:219]
s_add_u32 s66,s52,196608
v_mfma_f32_16x16x32_bf16 v[220:223],a[212:215],a[0:3],v[220:223]
buffer_load_dwordx4 v240,s[24:27],s66 offen lds
v_mfma_f32_16x16x32_bf16 v[224:227],a[212:215],a[4:7],v[224:227]
v_mfma_f32_16x16x32_bf16 v[228:231],a[212:215],a[8:11],v[228:231]
v_mfma_f32_16x16x32_bf16 v[232:235],a[212:215],a[12:15],v[232:235]
s_add_u32 m0,s58,76824
v_mfma_f32_16x16x32_bf16 v[236:239],a[212:215],a[16:19],v[236:239]
buffer_load_dwordx4 v241,s[24:27],s66 offen offset:1024 lds
v_mfma_f32_16x16x32_bf16 v[0:3],a[216:219],a[0:3],v[0:3]
v_mfma_f32_16x16x32_bf16 v[4:7],a[216:219],a[4:7],v[4:7]
v_mfma_f32_16x16x32_bf16 v[8:11],a[216:219],a[8:11],v[8:11]
s_add_u32 m0,s58,76824
v_mfma_f32_16x16x32_bf16 v[12:15],a[216:219],a[12:15],v[12:15]
buffer_load_dwordx4 v242,s[24:27],s66 offen offset:2048 lds
v_mfma_f32_16x16x32_bf16 v[16:19],a[216:219],a[16:19],v[16:19]
s_add_u32 s52,s52,393216
v_mfma_f32_16x16x32_bf16 v[20:23],a[220:223],a[0:3],v[20:23]
v_mfma_f32_16x16x32_bf16 v[24:27],a[220:223],a[4:7],v[24:27]
v_mfma_f32_16x16x32_bf16 v[28:31],a[220:223],a[8:11],v[28:31]
v_mfma_f32_16x16x32_bf16 v[32:35],a[220:223],a[12:15],v[32:35]
v_mfma_f32_16x16x32_bf16 v[36:39],a[220:223],a[16:19],v[36:39]
s_waitcnt vmcnt(12) lgkmcnt(0)
s_barrier
v_mfma_f32_16x16x32_bf16 v[40:43],a[224:227],a[0:3],v[40:43]
ds_read_b128 a[20:23],v249 offset:20480
v_mfma_f32_16x16x32_bf16 v[44:47],a[224:227],a[4:7],v[44:47]
ds_read_b128 a[24:27],v249 offset:22528
v_mfma_f32_16x16x32_bf16 v[48:51],a[224:227],a[8:11],v[48:51]
ds_read_b128 a[28:31],v249 offset:24576
v_mfma_f32_16x16x32_bf16 v[52:55],a[224:227],a[12:15],v[52:55]
s_add_u32 m0,s56,0
ds_read_b128 a[32:35],v249 offset:26624
v_mfma_f32_16x16x32_bf16 v[56:59],a[224:227],a[16:19],v[56:59]
buffer_load_dwordx4 v250,s[28:31],s54 offen nt lds
v_mfma_f32_16x16x32_bf16 v[60:63],a[228:231],a[0:3],v[60:63]
ds_read_b128 a[36:39],v249 offset:28672
ds_read_b64_tr_b16 a[200:201],v244 offset:24840
v_mfma_f32_16x16x32_bf16 v[64:67],a[228:231],a[4:7],v[64:67]
ds_read_b64_tr_b16 a[202:203],v244 offset:26376
v_mfma_f32_16x16x32_bf16 v[68:71],a[228:231],a[8:11],v[68:71]
ds_read_b64_tr_b16 a[204:205],v244 offset:24848
v_mfma_f32_16x16x32_bf16 v[72:75],a[228:231],a[12:15],v[72:75]
ds_read_b64_tr_b16 a[206:207],v244 offset:26384
v_mfma_f32_16x16x32_bf16 v[76:79],a[228:231],a[16:19],v[76:79]
ds_read_b64_tr_b16 a[208:209],v245 offset:24840
v_mfma_f32_16x16x32_bf16 v[80:83],a[232:235],a[0:3],v[80:83]
ds_read_b64_tr_b16 a[210:211],v245 offset:26376
v_mfma_f32_16x16x32_bf16 v[84:87],a[232:235],a[4:7],v[84:87]
ds_read_b64_tr_b16 a[212:213],v245 offset:24848
v_mfma_f32_16x16x32_bf16 v[88:91],a[232:235],a[8:11],v[88:91]
s_add_u32 m0,s56,0
ds_read_b64_tr_b16 a[214:215],v245 offset:26384
v_mfma_f32_16x16x32_bf16 v[92:95],a[232:235],a[12:15],v[92:95]
buffer_load_dwordx4 v251,s[28:31],s54 offen offset:1024 nt lds
v_mfma_f32_16x16x32_bf16 v[96:99],a[232:235],a[16:19],v[96:99]
ds_read_b64_tr_b16 a[216:217],v244 offset:24584
ds_read_b64_tr_b16 a[218:219],v244 offset:26120
v_mfma_f32_16x16x32_bf16 v[100:103],a[236:239],a[0:3],v[100:103]
ds_read_b64_tr_b16 a[220:221],v244 offset:24592
v_mfma_f32_16x16x32_bf16 v[104:107],a[236:239],a[4:7],v[104:107]
ds_read_b64_tr_b16 a[222:223],v244 offset:26128
v_mfma_f32_16x16x32_bf16 v[108:111],a[236:239],a[8:11],v[108:111]
v_mfma_f32_16x16x32_bf16 v[112:115],a[236:239],a[12:15],v[112:115]
v_mfma_f32_16x16x32_bf16 v[116:119],a[236:239],a[16:19],v[116:119]
v_mfma_f32_16x16x32_bf16 v[120:123],a[240:243],a[0:3],v[120:123]
v_mfma_f32_16x16x32_bf16 v[124:127],a[240:243],a[4:7],v[124:127]
s_add_u32 m0,s56,0
v_mfma_f32_16x16x32_bf16 v[128:131],a[240:243],a[8:11],v[128:131]
buffer_load_dwordx4 v252,s[28:31],s54 offen offset:2048 nt lds
v_mfma_f32_16x16x32_bf16 v[132:135],a[240:243],a[12:15],v[132:135]
v_mfma_f32_16x16x32_bf16 v[136:139],a[240:243],a[16:19],v[136:139]
v_mfma_f32_16x16x32_bf16 v[140:143],a[244:247],a[0:3],v[140:143]
v_mfma_f32_16x16x32_bf16 v[144:147],a[244:247],a[4:7],v[144:147]
v_mfma_f32_16x16x32_bf16 v[148:151],a[244:247],a[8:11],v[148:151]
v_mfma_f32_16x16x32_bf16 v[152:155],a[244:247],a[12:15],v[152:155]
v_mfma_f32_16x16x32_bf16 v[156:159],a[244:247],a[16:19],v[156:159]
s_waitcnt lgkmcnt(0)
v_mfma_f32_16x16x32_bf16 v[160:163],a[200:203],a[20:23],v[160:163]
s_add_u32 m0,s56,0
v_mfma_f32_16x16x32_bf16 v[164:167],a[200:203],a[24:27],v[164:167]
buffer_load_dwordx4 v250,s[28:31],s54 offen offset:3072 nt lds
v_mfma_f32_16x16x32_bf16 v[168:171],a[200:203],a[28:31],v[168:171]
ds_read_b64_tr_b16 a[224:225],v245 offset:24584
ds_read_b64_tr_b16 a[226:227],v245 offset:26120
v_mfma_f32_16x16x32_bf16 v[172:175],a[200:203],a[32:35],v[172:175]
ds_read_b64_tr_b16 a[228:229],v245 offset:24592
v_mfma_f32_16x16x32_bf16 v[176:179],a[200:203],a[36:39],v[176:179]
ds_read_b64_tr_b16 a[230:231],v245 offset:26128
v_mfma_f32_16x16x32_bf16 v[180:183],a[204:207],a[20:23],v[180:183]
ds_read_b64_tr_b16 a[232:233],v244 offset:24712
v_mfma_f32_16x16x32_bf16 v[184:187],a[204:207],a[24:27],v[184:187]
ds_read_b64_tr_b16 a[234:235],v244 offset:26248
v_mfma_f32_16x16x32_bf16 v[188:191],a[204:207],a[28:31],v[188:191]
ds_read_b64_tr_b16 a[236:237],v244 offset:24720
v_mfma_f32_16x16x32_bf16 v[192:195],a[204:207],a[32:35],v[192:195]
s_add_u32 m0,s56,4096
ds_read_b64_tr_b16 a[238:239],v244 offset:26256
v_mfma_f32_16x16x32_bf16 v[196:199],a[204:207],a[36:39],v[196:199]
s_add_u32 s68,s54,4096
ds_read_b64_tr_b16 a[240:241],v245 offset:24712
v_mfma_f32_16x16x32_bf16 v[200:203],a[208:211],a[20:23],v[200:203]
buffer_load_dwordx4 v251,s[28:31],s68 offen nt lds
v_mfma_f32_16x16x32_bf16 v[204:207],a[208:211],a[24:27],v[204:207]
ds_read_b64_tr_b16 a[242:243],v245 offset:26248
ds_read_b64_tr_b16 a[244:245],v245 offset:24720
v_mfma_f32_16x16x32_bf16 v[208:211],a[208:211],a[28:31],v[208:211]
ds_read_b64_tr_b16 a[246:247],v245 offset:26256
v_mfma_f32_16x16x32_bf16 v[212:215],a[208:211],a[32:35],v[212:215]
v_mfma_f32_16x16x32_bf16 v[216:219],a[208:211],a[36:39],v[216:219]
v_mfma_f32_16x16x32_bf16 v[220:223],a[212:215],a[20:23],v[220:223]
v_mfma_f32_16x16x32_bf16 v[224:227],a[212:215],a[24:27],v[224:227]
v_mfma_f32_16x16x32_bf16 v[228:231],a[212:215],a[28:31],v[228:231]
v_mfma_f32_16x16x32_bf16 v[232:235],a[212:215],a[32:35],v[232:235]
s_add_u32 m0,s56,4096
v_mfma_f32_16x16x32_bf16 v[236:239],a[212:215],a[36:39],v[236:239]
buffer_load_dwordx4 v252,s[28:31],s68 offen offset:1024 nt lds
v_mfma_f32_16x16x32_bf16 v[0:3],a[216:219],a[20:23],v[0:3]
s_add_u32 s54,s54,12288
v_mfma_f32_16x16x32_bf16 v[4:7],a[216:219],a[24:27],v[4:7]
v_mfma_f32_16x16x32_bf16 v[8:11],a[216:219],a[28:31],v[8:11]
v_mfma_f32_16x16x32_bf16 v[12:15],a[216:219],a[32:35],v[12:15]
v_mfma_f32_16x16x32_bf16 v[16:19],a[216:219],a[36:39],v[16:19]
v_mfma_f32_16x16x32_bf16 v[20:23],a[220:223],a[20:23],v[20:23]
v_mfma_f32_16x16x32_bf16 v[24:27],a[220:223],a[24:27],v[24:27]
v_mfma_f32_16x16x32_bf16 v[28:31],a[220:223],a[28:31],v[28:31]
v_mfma_f32_16x16x32_bf16 v[32:35],a[220:223],a[32:35],v[32:35]
v_mfma_f32_16x16x32_bf16 v[36:39],a[220:223],a[36:39],v[36:39]
s_waitcnt vmcnt(12) lgkmcnt(0)
s_barrier
v_mfma_f32_16x16x32_bf16 v[40:43],a[224:227],a[20:23],v[40:43]
ds_read_b128 a[0:3],v248 offset:40960
v_mfma_f32_16x16x32_bf16 v[44:47],a[224:227],a[24:27],v[44:47]
ds_read_b128 a[4:7],v248 offset:43008
v_mfma_f32_16x16x32_bf16 v[48:51],a[224:227],a[28:31],v[48:51]
ds_read_b128 a[8:11],v248 offset:45056
v_mfma_f32_16x16x32_bf16 v[52:55],a[224:227],a[32:35],v[52:55]
s_add_u32 m0,s56,24576
ds_read_b128 a[12:15],v248 offset:47104
v_mfma_f32_16x16x32_bf16 v[56:59],a[224:227],a[36:39],v[56:59]
buffer_load_dwordx4 v250,s[28:31],s54 offen nt lds
v_mfma_f32_16x16x32_bf16 v[60:63],a[228:231],a[20:23],v[60:63]
ds_read_b128 a[16:19],v248 offset:49152
ds_read_b64_tr_b16 a[200:201],v246 offset:256
v_mfma_f32_16x16x32_bf16 v[64:67],a[228:231],a[24:27],v[64:67]
ds_read_b64_tr_b16 a[202:203],v246 offset:1792
v_mfma_f32_16x16x32_bf16 v[68:71],a[228:231],a[28:31],v[68:71]
ds_read_b64_tr_b16 a[204:205],v246 offset:264
v_mfma_f32_16x16x32_bf16 v[72:75],a[228:231],a[32:35],v[72:75]
ds_read_b64_tr_b16 a[206:207],v246 offset:1800
v_mfma_f32_16x16x32_bf16 v[76:79],a[228:231],a[36:39],v[76:79]
ds_read_b64_tr_b16 a[208:209],v247 offset:256
v_mfma_f32_16x16x32_bf16 v[80:83],a[232:235],a[20:23],v[80:83]
ds_read_b64_tr_b16 a[210:211],v247 offset:1792
v_mfma_f32_16x16x32_bf16 v[84:87],a[232:235],a[24:27],v[84:87]
ds_read_b64_tr_b16 a[212:213],v247 offset:264
v_mfma_f32_16x16x32_bf16 v[88:91],a[232:235],a[28:31],v[88:91]
s_add_u32 m0,s56,24576
ds_read_b64_tr_b16 a[214:215],v247 offset:1800
v_mfma_f32_16x16x32_bf16 v[92:95],a[232:235],a[32:35],v[92:95]
buffer_load_dwordx4 v251,s[28:31],s54 offen offset:1024 nt lds
v_mfma_f32_16x16x32_bf16 v[96:99],a[232:235],a[36:39],v[96:99]
ds_read_b64_tr_b16 a[216:217],v246
ds_read_b64_tr_b16 a[218:219],v246 offset:1536
v_mfma_f32_16x16x32_bf16 v[100:103],a[236:239],a[20:23],v[100:103]
ds_read_b64_tr_b16 a[220:221],v246 offset:8
v_mfma_f32_16x16x32_bf16 v[104:107],a[236:239],a[24:27],v[104:107]
ds_read_b64_tr_b16 a[222:223],v246 offset:1544
v_mfma_f32_16x16x32_bf16 v[108:111],a[236:239],a[28:31],v[108:111]
v_mfma_f32_16x16x32_bf16 v[112:115],a[236:239],a[32:35],v[112:115]
v_mfma_f32_16x16x32_bf16 v[116:119],a[236:239],a[36:39],v[116:119]
v_mfma_f32_16x16x32_bf16 v[120:123],a[240:243],a[20:23],v[120:123]
v_mfma_f32_16x16x32_bf16 v[124:127],a[240:243],a[24:27],v[124:127]
s_add_u32 m0,s56,24576
v_mfma_f32_16x16x32_bf16 v[128:131],a[240:243],a[28:31],v[128:131]
buffer_load_dwordx4 v252,s[28:31],s54 offen offset:2048 nt lds
v_mfma_f32_16x16x32_bf16 v[132:135],a[240:243],a[32:35],v[132:135]
v_mfma_f32_16x16x32_bf16 v[136:139],a[240:243],a[36:39],v[136:139]
v_mfma_f32_16x16x32_bf16 v[140:143],a[244:247],a[20:23],v[140:143]
v_mfma_f32_16x16x32_bf16 v[144:147],a[244:247],a[24:27],v[144:147]
v_mfma_f32_16x16x32_bf16 v[148:151],a[244:247],a[28:31],v[148:151]
v_mfma_f32_16x16x32_bf16 v[152:155],a[244:247],a[32:35],v[152:155]
v_mfma_f32_16x16x32_bf16 v[156:159],a[244:247],a[36:39],v[156:159]
s_waitcnt lgkmcnt(0)
v_mfma_f32_16x16x32_bf16 v[160:163],a[200:203],a[0:3],v[160:163]
v_mfma_f32_16x16x32_bf16 v[164:167],a[200:203],a[4:7],v[164:167]
ds_read_b64_tr_b16 a[224:225],v247
v_mfma_f32_16x16x32_bf16 v[168:171],a[200:203],a[8:11],v[168:171]
ds_read_b64_tr_b16 a[226:227],v247 offset:1536
v_mfma_f32_16x16x32_bf16 v[172:175],a[200:203],a[12:15],v[172:175]
s_add_u32 m0,s56,24576
ds_read_b64_tr_b16 a[228:229],v247 offset:8
v_mfma_f32_16x16x32_bf16 v[176:179],a[200:203],a[16:19],v[176:179]
buffer_load_dwordx4 v250,s[28:31],s54 offen offset:3072 nt lds
v_mfma_f32_16x16x32_bf16 v[180:183],a[204:207],a[0:3],v[180:183]
ds_read_b64_tr_b16 a[230:231],v247 offset:1544
ds_read_b64_tr_b16 a[232:233],v246 offset:128
v_mfma_f32_16x16x32_bf16 v[184:187],a[204:207],a[4:7],v[184:187]
ds_read_b64_tr_b16 a[234:235],v246 offset:1664
v_mfma_f32_16x16x32_bf16 v[188:191],a[204:207],a[8:11],v[188:191]
ds_read_b64_tr_b16 a[236:237],v246 offset:136
v_mfma_f32_16x16x32_bf16 v[192:195],a[204:207],a[12:15],v[192:195]
ds_read_b64_tr_b16 a[238:239],v246 offset:1672
v_mfma_f32_16x16x32_bf16 v[196:199],a[204:207],a[16:19],v[196:199]
ds_read_b64_tr_b16 a[240:241],v247 offset:128
v_mfma_f32_16x16x32_bf16 v[200:203],a[208:211],a[0:3],v[200:203]
ds_read_b64_tr_b16 a[242:243],v247 offset:1664
v_mfma_f32_16x16x32_bf16 v[204:207],a[208:211],a[4:7],v[204:207]
s_add_u32 m0,s56,28672
ds_read_b64_tr_b16 a[244:245],v247 offset:136
v_mfma_f32_16x16x32_bf16 v[208:211],a[208:211],a[8:11],v[208:211]
s_add_u32 s68,s54,4096
ds_read_b64_tr_b16 a[246:247],v247 offset:1672
v_mfma_f32_16x16x32_bf16 v[212:215],a[208:211],a[12:15],v[212:215]
buffer_load_dwordx4 v251,s[28:31],s68 offen nt lds
v_mfma_f32_16x16x32_bf16 v[216:219],a[208:211],a[16:19],v[216:219]
v_mfma_f32_16x16x32_bf16 v[220:223],a[212:215],a[0:3],v[220:223]
v_mfma_f32_16x16x32_bf16 v[224:227],a[212:215],a[4:7],v[224:227]
v_mfma_f32_16x16x32_bf16 v[228:231],a[212:215],a[8:11],v[228:231]
v_mfma_f32_16x16x32_bf16 v[232:235],a[212:215],a[12:15],v[232:235]
v_mfma_f32_16x16x32_bf16 v[236:239],a[212:215],a[16:19],v[236:239]
v_mfma_f32_16x16x32_bf16 v[0:3],a[216:219],a[0:3],v[0:3]
s_waitcnt vmcnt(63)
v_mfma_f32_16x16x32_bf16 v[4:7],a[216:219],a[4:7],v[4:7]
s_add_u32 m0,s56,28672
v_mfma_f32_16x16x32_bf16 v[8:11],a[216:219],a[8:11],v[8:11]
buffer_load_dwordx4 v252,s[28:31],s68 offen offset:1024 nt lds
v_mfma_f32_16x16x32_bf16 v[12:15],a[216:219],a[12:15],v[12:15]
s_add_u32 s54,s54,12288
v_mfma_f32_16x16x32_bf16 v[16:19],a[216:219],a[16:19],v[16:19]
v_mfma_f32_16x16x32_bf16 v[20:23],a[220:223],a[0:3],v[20:23]
v_mfma_f32_16x16x32_bf16 v[24:27],a[220:223],a[4:7],v[24:27]
v_mfma_f32_16x16x32_bf16 v[28:31],a[220:223],a[8:11],v[28:31]
v_mfma_f32_16x16x32_bf16 v[32:35],a[220:223],a[12:15],v[32:35]
v_mfma_f32_16x16x32_bf16 v[36:39],a[220:223],a[16:19],v[36:39]
s_waitcnt vmcnt(12) lgkmcnt(0)
s_barrier
v_mfma_f32_16x16x32_bf16 v[40:43],a[224:227],a[0:3],v[40:43]
ds_read_b128 a[20:23],v249 offset:40960
v_mfma_f32_16x16x32_bf16 v[44:47],a[224:227],a[4:7],v[44:47]
buffer_store_dword v249,v253,s[8:11],0 offen sc1
v_mfma_f32_16x16x32_bf16 v[48:51],a[224:227],a[8:11],v[48:51]
s_mov_b32 s38,0
ds_read_b128 a[24:27],v249 offset:43008
ds_read_b128 a[28:31],v249 offset:45056
v_mfma_f32_16x16x32_bf16 v[52:55],a[224:227],a[12:15],v[52:55]
s_add_u32 m0,s56,49152
ds_read_b128 a[32:35],v249 offset:47104
v_mfma_f32_16x16x32_bf16 v[56:59],a[224:227],a[16:19],v[56:59]
s_mov_b32 s10,0
buffer_load_dwordx4 v250,s[28:31],s54 offen nt lds
v_mfma_f32_16x16x32_bf16 v[60:63],a[228:231],a[0:3],v[60:63]
ds_read_b128 a[36:39],v249 offset:49152
ds_read_b64_tr_b16 a[200:201],v246 offset:24840
v_mfma_f32_16x16x32_bf16 v[64:67],a[228:231],a[4:7],v[64:67]
ds_read_b64_tr_b16 a[202:203],v246 offset:26376
v_mfma_f32_16x16x32_bf16 v[68:71],a[228:231],a[8:11],v[68:71]
ds_read_b64_tr_b16 a[204:205],v246 offset:24848
v_mfma_f32_16x16x32_bf16 v[72:75],a[228:231],a[12:15],v[72:75]
ds_read_b64_tr_b16 a[206:207],v246 offset:26384
v_mfma_f32_16x16x32_bf16 v[76:79],a[228:231],a[16:19],v[76:79]
ds_read_b64_tr_b16 a[208:209],v247 offset:24840
v_mfma_f32_16x16x32_bf16 v[80:83],a[232:235],a[0:3],v[80:83]
ds_read_b64_tr_b16 a[210:211],v247 offset:26376
v_mfma_f32_16x16x32_bf16 v[84:87],a[232:235],a[4:7],v[84:87]
ds_read_b64_tr_b16 a[212:213],v247 offset:24848
v_mfma_f32_16x16x32_bf16 v[88:91],a[232:235],a[8:11],v[88:91]
s_add_u32 m0,s56,49152
ds_read_b64_tr_b16 a[214:215],v247 offset:26384
v_mfma_f32_16x16x32_bf16 v[92:95],a[232:235],a[12:15],v[92:95]
buffer_load_dwordx4 v251,s[28:31],s54 offen offset:1024 nt lds
v_mfma_f32_16x16x32_bf16 v[96:99],a[232:235],a[16:19],v[96:99]
ds_read_b64_tr_b16 a[216:217],v246 offset:24584
ds_read_b64_tr_b16 a[218:219],v246 offset:26120
v_mfma_f32_16x16x32_bf16 v[100:103],a[236:239],a[0:3],v[100:103]
ds_read_b64_tr_b16 a[220:221],v246 offset:24592
v_mfma_f32_16x16x32_bf16 v[104:107],a[236:239],a[4:7],v[104:107]
ds_read_b64_tr_b16 a[222:223],v246 offset:26128
v_mfma_f32_16x16x32_bf16 v[108:111],a[236:239],a[8:11],v[108:111]
v_mfma_f32_16x16x32_bf16 v[112:115],a[236:239],a[12:15],v[112:115]
v_mfma_f32_16x16x32_bf16 v[116:119],a[236:239],a[16:19],v[116:119]
v_mfma_f32_16x16x32_bf16 v[120:123],a[240:243],a[0:3],v[120:123]
v_mfma_f32_16x16x32_bf16 v[124:127],a[240:243],a[4:7],v[124:127]
s_add_u32 m0,s56,49152
v_mfma_f32_16x16x32_bf16 v[128:131],a[240:243],a[8:11],v[128:131]
buffer_load_dwordx4 v252,s[28:31],s54 offen offset:2048 nt lds
v_mfma_f32_16x16x32_bf16 v[132:135],a[240:243],a[12:15],v[132:135]
v_mfma_f32_16x16x32_bf16 v[136:139],a[240:243],a[16:19],v[136:139]
v_mfma_f32_16x16x32_bf16 v[140:143],a[244:247],a[0:3],v[140:143]
v_mfma_f32_16x16x32_bf16 v[144:147],a[244:247],a[4:7],v[144:147]
v_mfma_f32_16x16x32_bf16 v[148:151],a[244:247],a[8:11],v[148:151]
v_mfma_f32_16x16x32_bf16 v[152:155],a[244:247],a[12:15],v[152:155]
v_mfma_f32_16x16x32_bf16 v[156:159],a[244:247],a[16:19],v[156:159]
s_waitcnt lgkmcnt(0)
v_mfma_f32_16x16x32_bf16 v[160:163],a[200:203],a[20:23],v[160:163]
s_add_u32 m0,s56,49152
v_mfma_f32_16x16x32_bf16 v[164:167],a[200:203],a[24:27],v[164:167]
buffer_load_dwordx4 v250,s[28:31],s54 offen offset:3072 nt lds
v_mfma_f32_16x16x32_bf16 v[168:171],a[200:203],a[28:31],v[168:171]
ds_read_b64_tr_b16 a[224:225],v247 offset:24584
ds_read_b64_tr_b16 a[226:227],v247 offset:26120
v_mfma_f32_16x16x32_bf16 v[172:175],a[200:203],a[32:35],v[172:175]
ds_read_b64_tr_b16 a[228:229],v247 offset:24592
v_mfma_f32_16x16x32_bf16 v[176:179],a[200:203],a[36:39],v[176:179]
ds_read_b64_tr_b16 a[230:231],v247 offset:26128
v_mfma_f32_16x16x32_bf16 v[180:183],a[204:207],a[20:23],v[180:183]
ds_read_b64_tr_b16 a[232:233],v246 offset:24712
v_mfma_f32_16x16x32_bf16 v[184:187],a[204:207],a[24:27],v[184:187]
ds_read_b64_tr_b16 a[234:235],v246 offset:26248
v_mfma_f32_16x16x32_bf16 v[188:191],a[204:207],a[28:31],v[188:191]
ds_read_b64_tr_b16 a[236:237],v246 offset:24720
v_mfma_f32_16x16x32_bf16 v[192:195],a[204:207],a[32:35],v[192:195]
s_add_u32 m0,s56,53248
ds_read_b64_tr_b16 a[238:239],v246 offset:26256
v_mfma_f32_16x16x32_bf16 v[196:199],a[204:207],a[36:39],v[196:199]
s_add_u32 s68,s54,4096
ds_read_b64_tr_b16 a[240:241],v247 offset:24712
v_mfma_f32_16x16x32_bf16 v[200:203],a[208:211],a[20:23],v[200:203]
buffer_load_dwordx4 v251,s[28:31],s68 offen nt lds
v_mfma_f32_16x16x32_bf16 v[204:207],a[208:211],a[24:27],v[204:207]
ds_read_b64_tr_b16 a[242:243],v247 offset:26248
ds_read_b64_tr_b16 a[244:245],v247 offset:24720
v_mfma_f32_16x16x32_bf16 v[208:211],a[208:211],a[28:31],v[208:211]
ds_read_b64_tr_b16 a[246:247],v247 offset:26256
v_mfma_f32_16x16x32_bf16 v[212:215],a[208:211],a[32:35],v[212:215]
v_mfma_f32_16x16x32_bf16 v[216:219],a[208:211],a[36:39],v[216:219]
v_mfma_f32_16x16x32_bf16 v[220:223],a[212:215],a[20:23],v[220:223]
v_mfma_f32_16x16x32_bf16 v[224:227],a[212:215],a[24:27],v[224:227]
v_mfma_f32_16x16x32_bf16 v[228:231],a[212:215],a[28:31],v[228:231]
v_mfma_f32_16x16x32_bf16 v[232:235],a[212:215],a[32:35],v[232:235]
s_add_u32 m0,s56,53248
v_mfma_f32_16x16x32_bf16 v[236:239],a[212:215],a[36:39],v[236:239]
buffer_load_dwordx4 v252,s[28:31],s68 offen offset:1024 nt lds
v_mfma_f32_16x16x32_bf16 v[0:3],a[216:219],a[20:23],v[0:3]
s_add_u32 s54,s54,12288
v_mfma_f32_16x16x32_bf16 v[4:7],a[216:219],a[24:27],v[4:7]
v_mfma_f32_16x16x32_bf16 v[8:11],a[216:219],a[28:31],v[8:11]
v_mfma_f32_16x16x32_bf16 v[12:15],a[216:219],a[32:35],v[12:15]
v_mfma_f32_16x16x32_bf16 v[16:19],a[216:219],a[36:39],v[16:19]
v_mfma_f32_16x16x32_bf16 v[20:23],a[220:223],a[20:23],v[20:23]
v_mfma_f32_16x16x32_bf16 v[24:27],a[220:223],a[24:27],v[24:27]
v_mfma_f32_16x16x32_bf16 v[28:31],a[220:223],a[28:31],v[28:31]
v_mfma_f32_16x16x32_bf16 v[32:35],a[220:223],a[32:35],v[32:35]
v_mfma_f32_16x16x32_bf16 v[36:39],a[220:223],a[36:39],v[36:39]
s_waitcnt lgkmcnt(0)
s_barrier
v_mfma_f32_16x16x32_bf16 v[40:43],a[224:227],a[20:23],v[40:43]
v_mfma_f32_16x16x32_bf16 v[44:47],a[224:227],a[24:27],v[44:47]
v_mfma_f32_16x16x32_bf16 v[48:51],a[224:227],a[28:31],v[48:51]
v_mfma_f32_16x16x32_bf16 v[52:55],a[224:227],a[32:35],v[52:55]
s_add_u32 m0,s57,0
v_mfma_f32_16x16x32_bf16 v[56:59],a[224:227],a[36:39],v[56:59]
buffer_load_dwordx4 v250,s[40:43],s55 offen nt lds
v_mfma_f32_16x16x32_bf16 v[60:63],a[228:231],a[20:23],v[60:63]
v_mfma_f32_16x16x32_bf16 v[64:67],a[228:231],a[24:27],v[64:67]
v_mfma_f32_16x16x32_bf16 v[68:71],a[228:231],a[28:31],v[68:71]
v_mfma_f32_16x16x32_bf16 v[72:75],a[228:231],a[32:35],v[72:75]
v_mfma_f32_16x16x32_bf16 v[76:79],a[228:231],a[36:39],v[76:79]
v_mfma_f32_16x16x32_bf16 v[80:83],a[232:235],a[20:23],v[80:83]
v_mfma_f32_16x16x32_bf16 v[84:87],a[232:235],a[24:27],v[84:87]
v_mfma_f32_16x16x32_bf16 v[88:91],a[232:235],a[28:31],v[88:91]
s_add_u32 m0,s57,0
v_mfma_f32_16x16x32_bf16 v[92:95],a[232:235],a[32:35],v[92:95]
buffer_load_dwordx4 v251,s[40:43],s55 offen offset:1024 nt lds
v_mfma_f32_16x16x32_bf16 v[96:99],a[232:235],a[36:39],v[96:99]
v_mfma_f32_16x16x32_bf16 v[100:103],a[236:239],a[20:23],v[100:103]
v_mfma_f32_16x16x32_bf16 v[104:107],a[236:239],a[24:27],v[104:107]
v_mfma_f32_16x16x32_bf16 v[108:111],a[236:239],a[28:31],v[108:111]
v_mfma_f32_16x16x32_bf16 v[112:115],a[236:239],a[32:35],v[112:115]
v_mfma_f32_16x16x32_bf16 v[116:119],a[236:239],a[36:39],v[116:119]
v_mfma_f32_16x16x32_bf16 v[120:123],a[240:243],a[20:23],v[120:123]
v_mfma_f32_16x16x32_bf16 v[124:127],a[240:243],a[24:27],v[124:127]
s_add_u32 m0,s57,0
v_mfma_f32_16x16x32_bf16 v[128:131],a[240:243],a[28:31],v[128:131]
buffer_load_dwordx4 v252,s[40:43],s55 offen offset:2048 nt lds
v_mfma_f32_16x16x32_bf16 v[132:135],a[240:243],a[32:35],v[132:135]
v_mfma_f32_16x16x32_bf16 v[136:139],a[240:243],a[36:39],v[136:139]
v_mfma_f32_16x16x32_bf16 v[140:143],a[244:247],a[20:23],v[140:143]
v_mfma_f32_16x16x32_bf16 v[144:147],a[244:247],a[24:27],v[144:147]
v_mfma_f32_16x16x32_bf16 v[148:151],a[244:247],a[28:31],v[148:151]
v_mfma_f32_16x16x32_bf16 v[152:155],a[244:247],a[32:35],v[152:155]
v_mfma_f32_16x16x32_bf16 v[156:159],a[244:247],a[36:39],v[156:159]
v_mov_b32 v244,0x3dd53b95
v_mov_b32 v245,0x3dd53b95
s_nop 5
v_pk_mul_f32 v[160:161],v[160:161],v[244:245]
v_pk_mul_f32 v[162:163],v[162:163],v[244:245]
v_pk_mul_f32 v[180:181],v[180:181],v[244:245]
v_pk_mul_f32 v[182:183],v[182:183],v[244:245]
v_cvt_pk_bf16_f32 v160,v160,v161
v_cvt_pk_bf16_f32 v161,v162,v163
v_cvt_pk_bf16_f32 v162,v180,v181
v_cvt_pk_bf16_f32 v163,v182,v183
v_accvgpr_write_b32 a160,v160
v_accvgpr_write_b32 a161,v161
v_accvgpr_write_b32 a162,v162
v_accvgpr_write_b32 a163,v163
v_pk_mul_f32 v[164:165],v[164:165],v[244:245]
v_pk_mul_f32 v[166:167],v[166:167],v[244:245]
v_pk_mul_f32 v[184:185],v[184:185],v[244:245]
v_pk_mul_f32 v[186:187],v[186:187],v[244:245]
v_cvt_pk_bf16_f32 v164,v164,v165
v_cvt_pk_bf16_f32 v165,v166,v167
v_cvt_pk_bf16_f32 v166,v184,v185
v_cvt_pk_bf16_f32 v167,v186,v187
v_accvgpr_write_b32 a164,v164
v_accvgpr_write_b32 a165,v165
v_accvgpr_write_b32 a166,v166
v_accvgpr_write_b32 a167,v167
v_pk_mul_f32 v[168:169],v[168:169],v[244:245]
v_pk_mul_f32 v[170:171],v[170:171],v[244:245]
v_pk_mul_f32 v[188:189],v[188:189],v[244:245]
v_pk_mul_f32 v[190:191],v[190:191],v[244:245]
v_cvt_pk_bf16_f32 v168,v168,v169
v_cvt_pk_bf16_f32 v169,v170,v171
v_cvt_pk_bf16_f32 v170,v188,v189
v_cvt_pk_bf16_f32 v171,v190,v191
v_accvgpr_write_b32 a168,v168
v_accvgpr_write_b32 a169,v169
s_add_u32 m0,s57,0
s_nop 0
buffer_load_dwordx4 v250,s[40:43],s55 offen offset:3072 nt lds
v_accvgpr_write_b32 a170,v170
v_accvgpr_write_b32 a171,v171
v_pk_mul_f32 v[172:173],v[172:173],v[244:245]
v_pk_mul_f32 v[174:175],v[174:175],v[244:245]
v_pk_mul_f32 v[192:193],v[192:193],v[244:245]
v_pk_mul_f32 v[194:195],v[194:195],v[244:245]
v_cvt_pk_bf16_f32 v172,v172,v173
v_cvt_pk_bf16_f32 v173,v174,v175
v_cvt_pk_bf16_f32 v174,v192,v193
v_cvt_pk_bf16_f32 v175,v194,v195
v_accvgpr_write_b32 a172,v172
v_accvgpr_write_b32 a173,v173
v_accvgpr_write_b32 a174,v174
v_accvgpr_write_b32 a175,v175
v_pk_mul_f32 v[176:177],v[176:177],v[244:245]
v_pk_mul_f32 v[178:179],v[178:179],v[244:245]
v_pk_mul_f32 v[196:197],v[196:197],v[244:245]
v_pk_mul_f32 v[198:199],v[198:199],v[244:245]
v_cvt_pk_bf16_f32 v176,v176,v177
v_cvt_pk_bf16_f32 v177,v178,v179
v_cvt_pk_bf16_f32 v178,v196,v197
v_cvt_pk_bf16_f32 v179,v198,v199
v_accvgpr_write_b32 a176,v176
v_accvgpr_write_b32 a177,v177
v_accvgpr_write_b32 a178,v178
v_accvgpr_write_b32 a179,v179
v_pk_mul_f32 v[200:201],v[200:201],v[244:245]
v_pk_mul_f32 v[202:203],v[202:203],v[244:245]
v_pk_mul_f32 v[220:221],v[220:221],v[244:245]
v_pk_mul_f32 v[222:223],v[222:223],v[244:245]
v_cvt_pk_bf16_f32 v200,v200,v201
v_cvt_pk_bf16_f32 v201,v202,v203
v_cvt_pk_bf16_f32 v202,v220,v221
v_cvt_pk_bf16_f32 v203,v222,v223
v_accvgpr_write_b32 a180,v200
v_accvgpr_write_b32 a181,v201
s_add_u32 m0,s57,4096
s_add_u32 s68,s55,4096
buffer_load_dwordx4 v251,s[40:43],s68 offen nt lds
v_accvgpr_write_b32 a182,v202
v_accvgpr_write_b32 a183,v203
v_pk_mul_f32 v[204:205],v[204:205],v[244:245]
v_pk_mul_f32 v[206:207],v[206:207],v[244:245]
v_pk_mul_f32 v[224:225],v[224:225],v[244:245]
v_pk_mul_f32 v[226:227],v[226:227],v[244:245]
v_cvt_pk_bf16_f32 v204,v204,v205
v_cvt_pk_bf16_f32 v205,v206,v207
v_cvt_pk_bf16_f32 v206,v224,v225
v_cvt_pk_bf16_f32 v207,v226,v227
v_accvgpr_write_b32 a184,v204
v_accvgpr_write_b32 a185,v205
v_accvgpr_write_b32 a186,v206
v_accvgpr_write_b32 a187,v207
v_pk_mul_f32 v[208:209],v[208:209],v[244:245]
v_pk_mul_f32 v[210:211],v[210:211],v[244:245]
v_pk_mul_f32 v[228:229],v[228:229],v[244:245]
v_pk_mul_f32 v[230:231],v[230:231],v[244:245]
v_cvt_pk_bf16_f32 v208,v208,v209
v_cvt_pk_bf16_f32 v209,v210,v211
v_cvt_pk_bf16_f32 v210,v228,v229
v_cvt_pk_bf16_f32 v211,v230,v231
v_accvgpr_write_b32 a188,v208
v_accvgpr_write_b32 a189,v209
v_accvgpr_write_b32 a190,v210
v_accvgpr_write_b32 a191,v211
v_pk_mul_f32 v[212:213],v[212:213],v[244:245]
v_pk_mul_f32 v[214:215],v[214:215],v[244:245]
v_pk_mul_f32 v[232:233],v[232:233],v[244:245]
v_pk_mul_f32 v[234:235],v[234:235],v[244:245]
v_cvt_pk_bf16_f32 v212,v212,v213
v_cvt_pk_bf16_f32 v213,v214,v215
v_cvt_pk_bf16_f32 v214,v232,v233
v_cvt_pk_bf16_f32 v215,v234,v235
v_accvgpr_write_b32 a192,v212
v_accvgpr_write_b32 a193,v213
s_add_u32 m0,s57,4096
s_nop 0
buffer_load_dwordx4 v252,s[40:43],s68 offen offset:1024 nt lds
s_add_u32 s55,s55,12288
v_accvgpr_write_b32 a194,v214
v_accvgpr_write_b32 a195,v215
v_pk_mul_f32 v[216:217],v[216:217],v[244:245]
v_pk_mul_f32 v[218:219],v[218:219],v[244:245]
v_pk_mul_f32 v[236:237],v[236:237],v[244:245]
v_pk_mul_f32 v[238:239],v[238:239],v[244:245]
v_cvt_pk_bf16_f32 v216,v216,v217
v_cvt_pk_bf16_f32 v217,v218,v219
v_cvt_pk_bf16_f32 v218,v236,v237
v_cvt_pk_bf16_f32 v219,v238,v239
v_accvgpr_write_b32 a196,v216
v_accvgpr_write_b32 a197,v217
v_accvgpr_write_b32 a198,v218
v_accvgpr_write_b32 a199,v219
v_pk_mul_f32 v[0:1],v[0:1],v[244:245]
v_pk_mul_f32 v[2:3],v[2:3],v[244:245]
v_pk_mul_f32 v[20:21],v[20:21],v[244:245]
v_pk_mul_f32 v[22:23],v[22:23],v[244:245]
v_cvt_pk_bf16_f32 v0,v0,v1
v_cvt_pk_bf16_f32 v1,v2,v3
v_cvt_pk_bf16_f32 v2,v20,v21
v_cvt_pk_bf16_f32 v3,v22,v23
v_accvgpr_write_b32 a80,v0
v_accvgpr_write_b32 a81,v1
v_accvgpr_write_b32 a82,v2
v_accvgpr_write_b32 a83,v3
v_pk_mul_f32 v[4:5],v[4:5],v[244:245]
v_pk_mul_f32 v[6:7],v[6:7],v[244:245]
v_pk_mul_f32 v[24:25],v[24:25],v[244:245]
v_pk_mul_f32 v[26:27],v[26:27],v[244:245]
v_cvt_pk_bf16_f32 v4,v4,v5
v_cvt_pk_bf16_f32 v5,v6,v7
v_cvt_pk_bf16_f32 v6,v24,v25
v_cvt_pk_bf16_f32 v7,v26,v27
v_accvgpr_write_b32 a84,v4
v_accvgpr_write_b32 a85,v5
v_accvgpr_write_b32 a86,v6
s_add_u32 m0,s57,24576
s_nop 0
buffer_load_dwordx4 v250,s[40:43],s55 offen nt lds
v_accvgpr_write_b32 a87,v7
v_pk_mul_f32 v[8:9],v[8:9],v[244:245]
v_pk_mul_f32 v[10:11],v[10:11],v[244:245]
v_pk_mul_f32 v[28:29],v[28:29],v[244:245]
v_pk_mul_f32 v[30:31],v[30:31],v[244:245]
v_cvt_pk_bf16_f32 v8,v8,v9
v_cvt_pk_bf16_f32 v9,v10,v11
v_cvt_pk_bf16_f32 v10,v28,v29
v_cvt_pk_bf16_f32 v11,v30,v31
v_accvgpr_write_b32 a88,v8
v_accvgpr_write_b32 a89,v9
v_accvgpr_write_b32 a90,v10
v_accvgpr_write_b32 a91,v11
v_pk_mul_f32 v[12:13],v[12:13],v[244:245]
v_pk_mul_f32 v[14:15],v[14:15],v[244:245]
v_pk_mul_f32 v[32:33],v[32:33],v[244:245]
v_pk_mul_f32 v[34:35],v[34:35],v[244:245]
v_cvt_pk_bf16_f32 v12,v12,v13
v_cvt_pk_bf16_f32 v13,v14,v15
v_cvt_pk_bf16_f32 v14,v32,v33
v_cvt_pk_bf16_f32 v15,v34,v35
v_accvgpr_write_b32 a92,v12
v_accvgpr_write_b32 a93,v13
v_accvgpr_write_b32 a94,v14
v_accvgpr_write_b32 a95,v15
v_pk_mul_f32 v[16:17],v[16:17],v[244:245]
v_pk_mul_f32 v[18:19],v[18:19],v[244:245]
v_pk_mul_f32 v[36:37],v[36:37],v[244:245]
v_pk_mul_f32 v[38:39],v[38:39],v[244:245]
v_cvt_pk_bf16_f32 v16,v16,v17
v_cvt_pk_bf16_f32 v17,v18,v19
v_cvt_pk_bf16_f32 v18,v36,v37
v_cvt_pk_bf16_f32 v19,v38,v39
v_accvgpr_write_b32 a96,v16
v_accvgpr_write_b32 a97,v17
v_accvgpr_write_b32 a98,v18
s_add_u32 m0,s57,24576
s_nop 0
buffer_load_dwordx4 v251,s[40:43],s55 offen offset:1024 nt lds
v_accvgpr_write_b32 a99,v19
v_pk_mul_f32 v[40:41],v[40:41],v[244:245]
v_pk_mul_f32 v[42:43],v[42:43],v[244:245]
v_pk_mul_f32 v[60:61],v[60:61],v[244:245]
v_pk_mul_f32 v[62:63],v[62:63],v[244:245]
v_cvt_pk_bf16_f32 v40,v40,v41
v_cvt_pk_bf16_f32 v41,v42,v43
v_cvt_pk_bf16_f32 v42,v60,v61
v_cvt_pk_bf16_f32 v43,v62,v63
v_accvgpr_write_b32 a100,v40
v_accvgpr_write_b32 a101,v41
v_accvgpr_write_b32 a102,v42
v_accvgpr_write_b32 a103,v43
v_pk_mul_f32 v[44:45],v[44:45],v[244:245]
v_pk_mul_f32 v[46:47],v[46:47],v[244:245]
v_pk_mul_f32 v[64:65],v[64:65],v[244:245]
v_pk_mul_f32 v[66:67],v[66:67],v[244:245]
v_cvt_pk_bf16_f32 v44,v44,v45
v_cvt_pk_bf16_f32 v45,v46,v47
v_cvt_pk_bf16_f32 v46,v64,v65
v_cvt_pk_bf16_f32 v47,v66,v67
v_accvgpr_write_b32 a104,v44
v_accvgpr_write_b32 a105,v45
v_accvgpr_write_b32 a106,v46
v_accvgpr_write_b32 a107,v47
v_pk_mul_f32 v[48:49],v[48:49],v[244:245]
v_pk_mul_f32 v[50:51],v[50:51],v[244:245]
v_pk_mul_f32 v[68:69],v[68:69],v[244:245]
v_pk_mul_f32 v[70:71],v[70:71],v[244:245]
v_cvt_pk_bf16_f32 v48,v48,v49
v_cvt_pk_bf16_f32 v49,v50,v51
v_cvt_pk_bf16_f32 v50,v68,v69
v_cvt_pk_bf16_f32 v51,v70,v71
v_accvgpr_write_b32 a108,v48
v_accvgpr_write_b32 a109,v49
v_accvgpr_write_b32 a110,v50
s_add_u32 m0,s57,24576
s_nop 0
buffer_load_dwordx4 v252,s[40:43],s55 offen offset:2048 nt lds
v_accvgpr_write_b32 a111,v51
v_pk_mul_f32 v[52:53],v[52:53],v[244:245]
v_pk_mul_f32 v[54:55],v[54:55],v[244:245]
v_pk_mul_f32 v[72:73],v[72:73],v[244:245]
v_pk_mul_f32 v[74:75],v[74:75],v[244:245]
v_cvt_pk_bf16_f32 v52,v52,v53
v_cvt_pk_bf16_f32 v53,v54,v55
v_cvt_pk_bf16_f32 v54,v72,v73
v_cvt_pk_bf16_f32 v55,v74,v75
v_accvgpr_write_b32 a112,v52
v_accvgpr_write_b32 a113,v53
v_accvgpr_write_b32 a114,v54
v_accvgpr_write_b32 a115,v55
v_pk_mul_f32 v[56:57],v[56:57],v[244:245]
v_pk_mul_f32 v[58:59],v[58:59],v[244:245]
v_pk_mul_f32 v[76:77],v[76:77],v[244:245]
v_pk_mul_f32 v[78:79],v[78:79],v[244:245]
v_cvt_pk_bf16_f32 v56,v56,v57
v_cvt_pk_bf16_f32 v57,v58,v59
v_cvt_pk_bf16_f32 v58,v76,v77
v_cvt_pk_bf16_f32 v59,v78,v79
v_accvgpr_write_b32 a116,v56
v_accvgpr_write_b32 a117,v57
v_accvgpr_write_b32 a118,v58
v_accvgpr_write_b32 a119,v59
v_pk_mul_f32 v[80:81],v[80:81],v[244:245]
v_pk_mul_f32 v[82:83],v[82:83],v[244:245]
v_pk_mul_f32 v[100:101],v[100:101],v[244:245]
v_pk_mul_f32 v[102:103],v[102:103],v[244:245]
v_cvt_pk_bf16_f32 v80,v80,v81
v_cvt_pk_bf16_f32 v81,v82,v83
v_cvt_pk_bf16_f32 v82,v100,v101
v_cvt_pk_bf16_f32 v83,v102,v103
v_accvgpr_write_b32 a120,v80
v_accvgpr_write_b32 a121,v81
v_accvgpr_write_b32 a122,v82
v_accvgpr_write_b32 a123,v83
s_add_u32 m0,s57,24576
s_nop 0
buffer_load_dwordx4 v250,s[40:43],s55 offen offset:3072 nt lds
v_pk_mul_f32 v[84:85],v[84:85],v[244:245]
v_pk_mul_f32 v[86:87],v[86:87],v[244:245]
v_pk_mul_f32 v[104:105],v[104:105],v[244:245]
v_pk_mul_f32 v[106:107],v[106:107],v[244:245]
v_cvt_pk_bf16_f32 v84,v84,v85
v_cvt_pk_bf16_f32 v85,v86,v87
v_cvt_pk_bf16_f32 v86,v104,v105
v_cvt_pk_bf16_f32 v87,v106,v107
v_accvgpr_write_b32 a124,v84
v_accvgpr_write_b32 a125,v85
v_accvgpr_write_b32 a126,v86
v_accvgpr_write_b32 a127,v87
v_pk_mul_f32 v[88:89],v[88:89],v[244:245]
v_pk_mul_f32 v[90:91],v[90:91],v[244:245]
v_pk_mul_f32 v[108:109],v[108:109],v[244:245]
v_pk_mul_f32 v[110:111],v[110:111],v[244:245]
v_cvt_pk_bf16_f32 v88,v88,v89
v_cvt_pk_bf16_f32 v89,v90,v91
v_cvt_pk_bf16_f32 v90,v108,v109
v_cvt_pk_bf16_f32 v91,v110,v111
v_accvgpr_write_b32 a128,v88
v_accvgpr_write_b32 a129,v89
v_accvgpr_write_b32 a130,v90
v_accvgpr_write_b32 a131,v91
v_pk_mul_f32 v[92:93],v[92:93],v[244:245]
v_pk_mul_f32 v[94:95],v[94:95],v[244:245]
v_pk_mul_f32 v[112:113],v[112:113],v[244:245]
v_pk_mul_f32 v[114:115],v[114:115],v[244:245]
v_cvt_pk_bf16_f32 v92,v92,v93
v_cvt_pk_bf16_f32 v93,v94,v95
v_cvt_pk_bf16_f32 v94,v112,v113
v_cvt_pk_bf16_f32 v95,v114,v115
v_accvgpr_write_b32 a132,v92
v_accvgpr_write_b32 a133,v93
v_accvgpr_write_b32 a134,v94
v_accvgpr_write_b32 a135,v95
s_add_u32 m0,s57,28672
s_add_u32 s68,s55,4096
buffer_load_dwordx4 v251,s[40:43],s68 offen nt lds
v_pk_mul_f32 v[96:97],v[96:97],v[244:245]
v_pk_mul_f32 v[98:99],v[98:99],v[244:245]
v_pk_mul_f32 v[116:117],v[116:117],v[244:245]
v_pk_mul_f32 v[118:119],v[118:119],v[244:245]
v_cvt_pk_bf16_f32 v96,v96,v97
v_cvt_pk_bf16_f32 v97,v98,v99
v_cvt_pk_bf16_f32 v98,v116,v117
v_cvt_pk_bf16_f32 v99,v118,v119
v_accvgpr_write_b32 a136,v96
v_accvgpr_write_b32 a137,v97
v_accvgpr_write_b32 a138,v98
v_accvgpr_write_b32 a139,v99
v_pk_mul_f32 v[120:121],v[120:121],v[244:245]
v_pk_mul_f32 v[122:123],v[122:123],v[244:245]
v_pk_mul_f32 v[140:141],v[140:141],v[244:245]
v_pk_mul_f32 v[142:143],v[142:143],v[244:245]
v_cvt_pk_bf16_f32 v120,v120,v121
v_cvt_pk_bf16_f32 v121,v122,v123
v_cvt_pk_bf16_f32 v122,v140,v141
v_cvt_pk_bf16_f32 v123,v142,v143
v_accvgpr_write_b32 a140,v120
v_accvgpr_write_b32 a141,v121
v_accvgpr_write_b32 a142,v122
v_accvgpr_write_b32 a143,v123
v_pk_mul_f32 v[124:125],v[124:125],v[244:245]
v_pk_mul_f32 v[126:127],v[126:127],v[244:245]
v_pk_mul_f32 v[144:145],v[144:145],v[244:245]
v_pk_mul_f32 v[146:147],v[146:147],v[244:245]
v_cvt_pk_bf16_f32 v124,v124,v125
v_cvt_pk_bf16_f32 v125,v126,v127
v_cvt_pk_bf16_f32 v126,v144,v145
v_cvt_pk_bf16_f32 v127,v146,v147
v_accvgpr_write_b32 a144,v124
v_accvgpr_write_b32 a145,v125
v_accvgpr_write_b32 a146,v126
v_accvgpr_write_b32 a147,v127
s_add_u32 m0,s57,28672
s_nop 0
buffer_load_dwordx4 v252,s[40:43],s68 offen offset:1024 nt lds
s_add_u32 s55,s55,12288
v_pk_mul_f32 v[128:129],v[128:129],v[244:245]
v_pk_mul_f32 v[130:131],v[130:131],v[244:245]
v_pk_mul_f32 v[148:149],v[148:149],v[244:245]
v_pk_mul_f32 v[150:151],v[150:151],v[244:245]
v_cvt_pk_bf16_f32 v128,v128,v129
v_cvt_pk_bf16_f32 v129,v130,v131
v_cvt_pk_bf16_f32 v130,v148,v149
v_cvt_pk_bf16_f32 v131,v150,v151
v_accvgpr_write_b32 a148,v128
v_accvgpr_write_b32 a149,v129
v_accvgpr_write_b32 a150,v130
v_accvgpr_write_b32 a151,v131
v_pk_mul_f32 v[132:133],v[132:133],v[244:245]
v_pk_mul_f32 v[134:135],v[134:135],v[244:245]
v_pk_mul_f32 v[152:153],v[152:153],v[244:245]
v_pk_mul_f32 v[154:155],v[154:155],v[244:245]
v_cvt_pk_bf16_f32 v132,v132,v133
v_cvt_pk_bf16_f32 v133,v134,v135
v_cvt_pk_bf16_f32 v134,v152,v153
v_cvt_pk_bf16_f32 v135,v154,v155
v_accvgpr_write_b32 a152,v132
v_accvgpr_write_b32 a153,v133
v_accvgpr_write_b32 a154,v134
v_accvgpr_write_b32 a155,v135
v_pk_mul_f32 v[136:137],v[136:137],v[244:245]
v_pk_mul_f32 v[138:139],v[138:139],v[244:245]
v_pk_mul_f32 v[156:157],v[156:157],v[244:245]
v_pk_mul_f32 v[158:159],v[158:159],v[244:245]
v_cvt_pk_bf16_f32 v136,v136,v137
v_cvt_pk_bf16_f32 v137,v138,v139
v_cvt_pk_bf16_f32 v138,v156,v157
v_cvt_pk_bf16_f32 v139,v158,v159
v_accvgpr_write_b32 a156,v136
v_accvgpr_write_b32 a157,v137
v_accvgpr_write_b32 a158,v138
v_accvgpr_write_b32 a159,v139
LAPRO_%=:
v_mbcnt_lo_u32_b32 v254,-1,0
v_mbcnt_hi_u32_b32 v254,-1,v254
v_lshrrev_b32 v240,4,v254
v_add_u32 v240,0,v240
v_and_b32 v255,6,v254
v_xor_b32 v240,v240,v255
v_lshlrev_b32 v240,4,v240
v_and_b32 v255,15,v254
v_mul_u32_u24 v255,384,v255
v_add_u32 v240,v240,v255
s_mul_i32 s64,s18,12288
v_add_u32 v240,s64,v240
v_lshrrev_b32 v241,4,v254
v_add_u32 v241,4,v241
v_and_b32 v255,6,v254
v_xor_b32 v241,v241,v255
v_lshlrev_b32 v241,4,v241
v_and_b32 v255,15,v254
v_mul_u32_u24 v255,384,v255
v_add_u32 v241,v241,v255
s_mul_i32 s64,s18,12288
v_add_u32 v241,s64,v241
v_lshrrev_b32 v242,4,v254
v_mul_u32_u24 v242,4,v242
v_bfe_u32 v255,v254,2,2
v_add_u32 v242,v242,v255
v_mul_u32_u24 v242,384,v242
v_bfe_u32 v255,v254,1,1
v_lshlrev_b32 v255,4,v255
v_add_u32 v242,v242,v255
v_and_b32 v255,1,v254
v_lshlrev_b32 v255,3,v255
v_add_u32 v242,v242,v255
s_mul_i32 s64,s18,12288
s_add_u32 s64,s64,73728
v_add_u32 v242,s64,v242
v_lshrrev_b32 v255,2,v254
v_and_b32 v255,6,v255
v_xor_b32 v245,6,v255
v_lshlrev_b32 v245,4,v245
v_add_u32 v245,v245,v242
v_xor_b32 v244,4,v255
v_lshlrev_b32 v244,4,v244
v_add_u32 v244,v244,v242
v_xor_b32 v243,2,v255
v_lshlrev_b32 v243,4,v243
v_add_u32 v243,v243,v242
v_lshlrev_b32 v255,4,v255
v_add_u32 v242,v242,v255
v_mov_b32 v246,0
v_mov_b32 v247,0
v_mov_b32 v248,0
v_mov_b32 v249,0
v_mov_b32 v253,0
v_mov_b32 v254,163824
ds_write_b32 v254,v246
s_cmp_lg_u32 s60,0
s_cbranch_scc1 LKEEPM_%=
v_mov_b32 v220,0
v_mov_b32 v221,0
v_mov_b32 v222,0
v_mov_b32 v223,0
v_mov_b32 v224,0
v_mov_b32 v225,0
v_mov_b32 v226,0
v_mov_b32 v227,0
v_mov_b32 v228,0
v_mov_b32 v229,0
v_mov_b32 v230,0
v_mov_b32 v231,0
v_mov_b32 v232,0
v_mov_b32 v233,0
v_mov_b32 v234,0
v_mov_b32 v235,0
v_mov_b32 v236,0
v_mov_b32 v237,0
v_mov_b32 v238,0
v_mov_b32 v239,0
LKEEPM_%=:
s_waitcnt vmcnt(18) lgkmcnt(0)
s_barrier
ds_read_b128 a[200:203],v240
ds_read_b128 a[204:207],v241
ds_read_b128 a[208:211],v240 offset:128
ds_read_b128 a[212:215],v241 offset:128
ds_read_b128 a[216:219],v240 offset:256
ds_read_b128 a[220:223],v241 offset:256
ds_read_b128 a[224:227],v240 offset:6144
ds_read_b128 a[228:231],v241 offset:6144
ds_read_b128 a[232:235],v240 offset:6272
ds_read_b128 a[236:239],v241 offset:6272
ds_read_b128 a[240:243],v240 offset:6400
ds_read_b128 a[244:247],v241 offset:6400
s_waitcnt lgkmcnt(6)
v_mfma_f32_16x16x32_bf16 v[160:163],a[200:203],a[80:83],v[220:223]
v_mfma_f32_16x16x32_bf16 v[164:167],a[200:203],a[84:87],v[224:227]
v_mfma_f32_16x16x32_bf16 v[168:171],a[200:203],a[88:91],v[228:231]
v_mfma_f32_16x16x32_bf16 v[172:175],a[200:203],a[92:95],v[232:235]
v_mfma_f32_16x16x32_bf16 v[176:179],a[200:203],a[96:99],v[236:239]
v_mfma_f32_16x16x32_bf16 v[160:163],a[204:207],a[100:103],v[160:163]
v_mfma_f32_16x16x32_bf16 v[164:167],a[204:207],a[104:107],v[164:167]
v_mfma_f32_16x16x32_bf16 v[168:171],a[204:207],a[108:111],v[168:171]
v_mfma_f32_16x16x32_bf16 v[172:175],a[204:207],a[112:115],v[172:175]
v_mfma_f32_16x16x32_bf16 v[176:179],a[204:207],a[116:119],v[176:179]
v_mfma_f32_16x16x32_bf16 v[160:163],a[208:211],a[120:123],v[160:163]
v_mfma_f32_16x16x32_bf16 v[164:167],a[208:211],a[124:127],v[164:167]
v_mfma_f32_16x16x32_bf16 v[168:171],a[208:211],a[128:131],v[168:171]
v_mfma_f32_16x16x32_bf16 v[172:175],a[208:211],a[132:135],v[172:175]
v_mfma_f32_16x16x32_bf16 v[176:179],a[208:211],a[136:139],v[176:179]
v_mfma_f32_16x16x32_bf16 v[160:163],a[212:215],a[140:143],v[160:163]
v_mfma_f32_16x16x32_bf16 v[164:167],a[212:215],a[144:147],v[164:167]
v_mfma_f32_16x16x32_bf16 v[168:171],a[212:215],a[148:151],v[168:171]
v_mfma_f32_16x16x32_bf16 v[172:175],a[212:215],a[152:155],v[172:175]
v_mfma_f32_16x16x32_bf16 v[176:179],a[212:215],a[156:159],v[176:179]
v_mfma_f32_16x16x32_bf16 v[160:163],a[216:219],a[160:163],v[160:163]
s_waitcnt lgkmcnt(0)
v_mfma_f32_16x16x32_bf16 v[164:167],a[216:219],a[164:167],v[164:167]
v_mfma_f32_16x16x32_bf16 v[168:171],a[216:219],a[168:171],v[168:171]
v_mfma_f32_16x16x32_bf16 v[172:175],a[216:219],a[172:175],v[172:175]
v_mfma_f32_16x16x32_bf16 v[176:179],a[216:219],a[176:179],v[176:179]
v_mfma_f32_16x16x32_bf16 v[160:163],a[220:223],a[180:183],v[160:163]
v_mfma_f32_16x16x32_bf16 v[164:167],a[220:223],a[184:187],v[164:167]
v_mfma_f32_16x16x32_bf16 v[168:171],a[220:223],a[188:191],v[168:171]
v_mfma_f32_16x16x32_bf16 v[172:175],a[220:223],a[192:195],v[172:175]
v_mfma_f32_16x16x32_bf16 v[176:179],a[220:223],a[196:199],v[176:179]
v_mfma_f32_16x16x32_bf16 v[180:183],a[224:227],a[80:83],v[220:223]
v_mfma_f32_16x16x32_bf16 v[184:187],a[224:227],a[84:87],v[224:227]
v_mfma_f32_16x16x32_bf16 v[188:191],a[224:227],a[88:91],v[228:231]
v_mfma_f32_16x16x32_bf16 v[192:195],a[224:227],a[92:95],v[232:235]
v_mfma_f32_16x16x32_bf16 v[196:199],a[224:227],a[96:99],v[236:239]
v_exp_f32 v160,v160
v_exp_f32 v161,v161
v_mfma_f32_16x16x32_bf16 v[180:183],a[228:231],a[100:103],v[180:183]
v_exp_f32 v162,v162
v_exp_f32 v163,v163
v_mfma_f32_16x16x32_bf16 v[184:187],a[228:231],a[104:107],v[184:187]
v_exp_f32 v164,v164
v_exp_f32 v165,v165
v_mfma_f32_16x16x32_bf16 v[188:191],a[228:231],a[108:111],v[188:191]
v_exp_f32 v166,v166
v_exp_f32 v167,v167
v_mfma_f32_16x16x32_bf16 v[192:195],a[228:231],a[112:115],v[192:195]
v_exp_f32 v168,v168
v_exp_f32 v169,v169
v_mfma_f32_16x16x32_bf16 v[196:199],a[228:231],a[116:119],v[196:199]
v_exp_f32 v170,v170
v_exp_f32 v171,v171
v_mfma_f32_16x16x32_bf16 v[180:183],a[232:235],a[120:123],v[180:183]
v_exp_f32 v172,v172
v_exp_f32 v173,v173
v_mfma_f32_16x16x32_bf16 v[184:187],a[232:235],a[124:127],v[184:187]
v_exp_f32 v174,v174
v_exp_f32 v175,v175
v_mfma_f32_16x16x32_bf16 v[188:191],a[232:235],a[128:131],v[188:191]
v_exp_f32 v176,v176
v_exp_f32 v177,v177
v_mfma_f32_16x16x32_bf16 v[192:195],a[232:235],a[132:135],v[192:195]
v_exp_f32 v178,v178
v_exp_f32 v179,v179
v_mfma_f32_16x16x32_bf16 v[196:199],a[232:235],a[136:139],v[196:199]
v_mfma_f32_16x16x32_bf16 v[180:183],a[236:239],a[140:143],v[180:183]
v_mfma_f32_16x16x32_bf16 v[184:187],a[236:239],a[144:147],v[184:187]
v_mfma_f32_16x16x32_bf16 v[188:191],a[236:239],a[148:151],v[188:191]
v_mfma_f32_16x16x32_bf16 v[192:195],a[236:239],a[152:155],v[192:195]
v_mfma_f32_16x16x32_bf16 v[196:199],a[236:239],a[156:159],v[196:199]
v_mfma_f32_16x16x32_bf16 v[180:183],a[240:243],a[160:163],v[180:183]
v_mfma_f32_16x16x32_bf16 v[184:187],a[240:243],a[164:167],v[184:187]
v_mfma_f32_16x16x32_bf16 v[188:191],a[240:243],a[168:171],v[188:191]
v_mfma_f32_16x16x32_bf16 v[192:195],a[240:243],a[172:175],v[192:195]
v_mfma_f32_16x16x32_bf16 v[196:199],a[240:243],a[176:179],v[196:199]
v_mfma_f32_16x16x32_bf16 v[180:183],a[244:247],a[180:183],v[180:183]
v_mfma_f32_16x16x32_bf16 v[184:187],a[244:247],a[184:187],v[184:187]
v_mfma_f32_16x16x32_bf16 v[188:191],a[244:247],a[188:191],v[188:191]
v_mfma_f32_16x16x32_bf16 v[192:195],a[244:247],a[192:195],v[192:195]
v_mfma_f32_16x16x32_bf16 v[196:199],a[244:247],a[196:199],v[196:199]
ds_read_b128 a[200:203],v240 offset:24576
ds_read_b128 a[204:207],v241 offset:24576
ds_read_b128 a[208:211],v240 offset:24704
ds_read_b128 a[212:215],v241 offset:24704
ds_read_b128 a[216:219],v240 offset:24832
ds_read_b128 a[220:223],v241 offset:24832
v_add_f32 v246,v246,v160
v_add_f32 v246,v246,v161
v_add_f32 v246,v246,v162
v_add_f32 v246,v246,v163
v_add_f32 v247,v247,v164
v_add_f32 v247,v247,v165
v_add_f32 v247,v247,v166
v_add_f32 v247,v247,v167
v_add_f32 v248,v248,v168
v_add_f32 v248,v248,v169
v_add_f32 v248,v248,v170
v_add_f32 v248,v248,v171
v_add_f32 v249,v249,v172
v_add_f32 v249,v249,v173
v_add_f32 v249,v249,v174
v_add_f32 v249,v249,v175
v_add_f32 v253,v253,v176
v_add_f32 v253,v253,v177
v_add_f32 v253,v253,v178
v_add_f32 v253,v253,v179
v_exp_f32 v180,v180
v_exp_f32 v181,v181
v_exp_f32 v182,v182
v_exp_f32 v183,v183
v_exp_f32 v184,v184
v_exp_f32 v185,v185
v_exp_f32 v186,v186
v_exp_f32 v187,v187
v_exp_f32 v188,v188
v_exp_f32 v189,v189
v_exp_f32 v190,v190
v_exp_f32 v191,v191
v_exp_f32 v192,v192
v_exp_f32 v193,v193
v_exp_f32 v194,v194
v_exp_f32 v195,v195
v_cvt_pk_bf16_f32 v200,v160,v161
v_cvt_pk_bf16_f32 v201,v162,v163
v_cvt_pk_bf16_f32 v204,v164,v165
v_cvt_pk_bf16_f32 v205,v166,v167
v_cvt_pk_bf16_f32 v208,v168,v169
v_cvt_pk_bf16_f32 v209,v170,v171
v_cvt_pk_bf16_f32 v212,v172,v173
v_cvt_pk_bf16_f32 v213,v174,v175
v_cvt_pk_bf16_f32 v216,v176,v177
v_cvt_pk_bf16_f32 v217,v178,v179
s_waitcnt vmcnt(6) lgkmcnt(0)
s_barrier
s_add_u32 m0,s56,0
s_nop 0
buffer_load_dwordx4 v250,s[28:31],s54 offen nt lds
buffer_load_dwordx4 v251,s[28:31],s54 offen offset:1024 nt lds
buffer_load_dwordx4 v252,s[28:31],s54 offen offset:2048 nt lds
s_getpc_b64 s[44:45]
LPC1_%=:
s_sub_u32 s44,s44,LPC1_%=-LSTART_%=
s_subb_u32 s45,s45,0
s_mov_b32 s46,LCODEEND_%=-LSTART_%=
s_lshr_b32 s64,s17,3
s_lshl_b32 s64,s64,2
s_add_u32 s64,s64,s16
s_mul_i32 s64,s64,768
v_mbcnt_lo_u32_b32 v254,-1,0
v_mbcnt_hi_u32_b32 v254,-1,v254
v_min_u32 v254,5,v254
v_lshlrev_b32 v254,7,v254
buffer_load_dword a250,v254,s[44:47],s64 offen
s_cmp_eq_u32 s61,0
s_cbranch_scc0 LTSO_%=
s_lshl_b32 s64,s62,8
s_lshl_b32 s65,s48,4
s_add_u32 s64,s64,s65
s_add_u32 s64,s64,s49
s_lshl_b32 s64,s64,6
s_add_u32 s8,s98,s64
s_addc_u32 s9,s99,0
s_cmp_eq_u32 s16,0
s_cselect_b32 s10,4,0
s_mov_b32 s10,0
s_xor_b32 s70,s62,1
s_and_b32 s64,s48,7
s_lshl_b32 s64,s64,1
s_add_u32 s64,s64,s34
s_lshl_b32 s64,s64,2
s_add_u32 s64,s64,s16
s_add_u32 s64,s64,32
s_mul_i32 s74,s64,12288
s_lshl_b32 s75,s34,2
s_add_u32 s75,s75,s16
s_mul_i32 s75,s75,25600
s_cmp_eq_u32 s70,0
s_cselect_b64 s[12:13],s[76:77],s[86:87]
s_mul_i32 s65,s48,204800
s_add_u32 s12,s12,s65
s_addc_u32 s13,s13,0
s_cmp_eq_u32 s70,0
s_cselect_b64 s[44:45],s[82:83],s[92:93]
s_mul_i32 s65,s33,3072
s_mul_i32 s66,s50,1536
s_add_u32 s65,s65,s66
s_add_u32 s44,s44,s65
s_addc_u32 s45,s45,0
s_mov_b32 s7,204800
s_mov_b32 s6,7853568
v_mbcnt_lo_u32_b32 v255,-1,0
v_mbcnt_hi_u32_b32 v255,-1,v255
v_min_u32 v254,11,v255
v_lshlrev_b32 v254,7,v254
v_min_u32 v255,19,v255
v_mul_u32_u24 v255,1280,v255
s_mov_b32 s73,786432
s_add_u32 s75,s75,256
s_mov_b32 s22,128
s_mov_b32 s20,s7
s_branch LTSX_%=
LTSO_%=:
s_cmp_eq_u32 s50,0
s_cselect_b64 s[44:45],s[84:85],s[94:95]
s_mul_i32 s65,s33,1966080
s_add_u32 s44,s44,s65
s_addc_u32 s45,s45,0
s_and_b32 s64,s48,7
s_lshl_b32 s64,s64,1
s_add_u32 s64,s64,s34
s_lshl_b32 s64,s64,2
s_add_u32 s64,s64,s16
s_mul_i32 s74,s64,1280
s_mul_i32 s65,s50,983040
s_add_u32 s74,s74,s65
s_add_u32 s74,s74,40960
s_mov_b32 s6,1966080
s_mov_b32 s73,81920
v_mbcnt_lo_u32_b32 v255,-1,0
v_mbcnt_hi_u32_b32 v255,-1,v255
v_min_u32 v254,9,v255
v_lshlrev_b32 v254,7,v254
v_min_u32 v255,19,v255
v_mul_u32_u24 v255,128,v255
s_lshl_b32 s64,s50,8
s_lshl_b32 s65,s48,4
s_add_u32 s64,s64,s65
s_lshl_b32 s65,s33,2
s_add_u32 s64,s64,s65
s_mul_i32 s65,s50,2
s_add_u32 s64,s64,s65
s_lshl_b32 s64,s64,6
s_add_u32 s12,s98,s64
s_addc_u32 s13,s99,0
s_mov_b32 s14,196
s_mov_b64 s[8:9],s[12:13]
s_lshl_b32 s64,s50,9
s_lshl_b32 s65,s48,5
s_add_u32 s64,s64,s65
s_lshl_b32 s65,s33,3
s_add_u32 s64,s64,s65
s_lshl_b32 s65,s50,2
s_add_u32 s64,s64,s65
s_mul_i32 s64,s64,61440
s_add_u32 s64,s64,65536
s_add_u32 s12,s98,s64
s_addc_u32 s13,s99,0
s_lshl_b32 s75,s34,2
s_add_u32 s75,s75,s16
s_mul_i32 s75,s75,2560
s_add_u32 s75,s75,40960
s_mov_b32 s22,20480
s_mov_b32 s7,122880
s_mov_b32 s20,0
LTSX_%=:
s_mov_b32 s14,0
s_mov_b32 s46,0
s_mul_i32 s64,s73,21
s_sub_u32 s74,s74,s64
s_mul_i32 s64,s22,11
s_sub_u32 s75,s75,s64
v_mfma_f32_16x16x32_bf16 v[160:163],a[200:203],a[80:83],v[220:223]
ds_read_b128 a[224:227],v240 offset:30720
v_exp_f32 v196,v196
v_mfma_f32_16x16x32_bf16 v[164:167],a[200:203],a[84:87],v[224:227]
v_exp_f32 v197,v197
v_exp_f32 v198,v198
v_mfma_f32_16x16x32_bf16 v[168:171],a[200:203],a[88:91],v[228:231]
s_add_u32 m0,s56,0
ds_read_b128 a[228:231],v241 offset:30720
v_exp_f32 v199,v199
v_mfma_f32_16x16x32_bf16 v[172:175],a[200:203],a[92:95],v[232:235]
buffer_load_dwordx4 v250,s[28:31],s54 offen offset:3072 nt lds
v_mfma_f32_16x16x32_bf16 v[176:179],a[200:203],a[96:99],v[236:239]
s_add_u32 m0,s56,4096
ds_read_b128 a[232:235],v240 offset:30848
v_cvt_pk_bf16_f32 v202,v180,v181
v_mfma_f32_16x16x32_bf16 v[160:163],a[204:207],a[100:103],v[160:163]
s_add_u32 s68,s54,4096
v_cvt_pk_bf16_f32 v203,v182,v183
v_cvt_pk_bf16_f32 v206,v184,v185
v_mfma_f32_16x16x32_bf16 v[164:167],a[204:207],a[104:107],v[164:167]
ds_read_b128 a[236:239],v241 offset:30848
v_cvt_pk_bf16_f32 v207,v186,v187
v_mfma_f32_16x16x32_bf16 v[168:171],a[204:207],a[108:111],v[168:171]
v_cvt_pk_bf16_f32 v210,v188,v189
v_cvt_pk_bf16_f32 v211,v190,v191
v_mfma_f32_16x16x32_bf16 v[172:175],a[204:207],a[112:115],v[172:175]
ds_read_b128 a[240:243],v240 offset:30976
v_cvt_pk_bf16_f32 v214,v192,v193
v_mfma_f32_16x16x32_bf16 v[176:179],a[204:207],a[116:119],v[176:179]
v_cvt_pk_bf16_f32 v215,v194,v195
v_cvt_pk_bf16_f32 v218,v196,v197
v_mfma_f32_16x16x32_bf16 v[160:163],a[208:211],a[120:123],v[160:163]
ds_read_b128 a[244:247],v241 offset:30976
v_cvt_pk_bf16_f32 v219,v198,v199
v_mfma_f32_16x16x32_bf16 v[164:167],a[208:211],a[124:127],v[164:167]
v_add_f32 v246,v246,v180
v_add_f32 v246,v246,v181
v_mfma_f32_16x16x32_bf16 v[168:171],a[208:211],a[128:131],v[168:171]
v_add_f32 v246,v246,v182
v_add_f32 v246,v246,v183
v_mfma_f32_16x16x32_bf16 v[172:175],a[208:211],a[132:135],v[172:175]
buffer_load_dwordx4 v251,s[28:31],s68 offen nt lds
v_mfma_f32_16x16x32_bf16 v[176:179],a[208:211],a[136:139],v[176:179]
v_add_f32 v247,v247,v184
v_add_f32 v247,v247,v185
v_mfma_f32_16x16x32_bf16 v[160:163],a[212:215],a[140:143],v[160:163]
v_add_f32 v247,v247,v186
v_add_f32 v247,v247,v187
v_mfma_f32_16x16x32_bf16 v[164:167],a[212:215],a[144:147],v[164:167]
v_add_f32 v248,v248,v188
v_add_f32 v248,v248,v189
v_mfma_f32_16x16x32_bf16 v[168:171],a[212:215],a[148:151],v[168:171]
v_add_f32 v248,v248,v190
v_add_f32 v248,v248,v191
v_mfma_f32_16x16x32_bf16 v[172:175],a[212:215],a[152:155],v[172:175]
v_add_f32 v249,v249,v192
v_add_f32 v249,v249,v193
v_mfma_f32_16x16x32_bf16 v[176:179],a[212:215],a[156:159],v[176:179]
v_add_f32 v249,v249,v194
v_add_f32 v249,v249,v195
v_mfma_f32_16x16x32_bf16 v[160:163],a[216:219],a[160:163],v[160:163]
v_add_f32 v253,v253,v196
v_add_f32 v253,v253,v197
v_mfma_f32_16x16x32_bf16 v[164:167],a[216:219],a[164:167],v[164:167]
v_add_f32 v253,v253,v198
v_add_f32 v253,v253,v199
v_mfma_f32_16x16x32_bf16 v[168:171],a[216:219],a[168:171],v[168:171]
v_mfma_f32_16x16x32_bf16 v[172:175],a[216:219],a[172:175],v[172:175]
buffer_load_dwordx4 v252,s[28:31],s68 offen offset:1024 nt lds
v_mfma_f32_16x16x32_bf16 v[176:179],a[216:219],a[176:179],v[176:179]
s_add_u32 s54,s54,12288
v_mfma_f32_16x16x32_bf16 v[160:163],a[220:223],a[180:183],v[160:163]
v_mfma_f32_16x16x32_bf16 v[164:167],a[220:223],a[184:187],v[164:167]
v_mfma_f32_16x16x32_bf16 v[168:171],a[220:223],a[188:191],v[168:171]
v_mfma_f32_16x16x32_bf16 v[172:175],a[220:223],a[192:195],v[172:175]
v_mfma_f32_16x16x32_bf16 v[176:179],a[220:223],a[196:199],v[176:179]
s_waitcnt lgkmcnt(0)
v_mfma_f32_16x16x32_bf16 v[180:183],a[224:227],a[80:83],v[220:223]
ds_read_b64_tr_b16 a[200:201],v242
v_mfma_f32_16x16x32_bf16 v[184:187],a[224:227],a[84:87],v[224:227]
ds_read_b64_tr_b16 a[202:203],v242 offset:6144
v_mfma_f32_16x16x32_bf16 v[188:191],a[224:227],a[88:91],v[228:231]
s_add_u32 m0,s57,49152
ds_read_b64_tr_b16 a[204:205],v243
v_mfma_f32_16x16x32_bf16 v[192:195],a[224:227],a[92:95],v[232:235]
buffer_load_dwordx4 v250,s[40:43],s55 offen nt lds
v_mfma_f32_16x16x32_bf16 v[196:199],a[224:227],a[96:99],v[236:239]
ds_read_b64_tr_b16 a[206:207],v243 offset:6144
ds_read_b64_tr_b16 a[208:209],v244
v_mfma_f32_16x16x32_bf16 v[180:183],a[228:231],a[100:103],v[180:183]
v_exp_f32 v160,v160
v_exp_f32 v161,v161
v_mfma_f32_16x16x32_bf16 v[184:187],a[228:231],a[104:107],v[184:187]
ds_read_b64_tr_b16 a[210:211],v244 offset:6144
v_exp_f32 v162,v162
v_mfma_f32_16x16x32_bf16 v[188:191],a[228:231],a[108:111],v[188:191]
v_exp_f32 v163,v163
v_exp_f32 v164,v164
v_mfma_f32_16x16x32_bf16 v[192:195],a[228:231],a[112:115],v[192:195]
ds_read_b64_tr_b16 a[212:213],v245
v_exp_f32 v165,v165
v_mfma_f32_16x16x32_bf16 v[196:199],a[228:231],a[116:119],v[196:199]
v_exp_f32 v166,v166
v_exp_f32 v167,v167
v_mfma_f32_16x16x32_bf16 v[180:183],a[232:235],a[120:123],v[180:183]
ds_read_b64_tr_b16 a[214:215],v245 offset:6144
v_exp_f32 v168,v168
v_mfma_f32_16x16x32_bf16 v[184:187],a[232:235],a[124:127],v[184:187]
v_exp_f32 v169,v169
v_exp_f32 v170,v170
v_mfma_f32_16x16x32_bf16 v[188:191],a[232:235],a[128:131],v[188:191]
ds_read_b64_tr_b16 a[216:217],v242 offset:128
v_exp_f32 v171,v171
v_mfma_f32_16x16x32_bf16 v[192:195],a[232:235],a[132:135],v[192:195]
buffer_load_dwordx4 v251,s[40:43],s55 offen offset:1024 nt lds
v_mfma_f32_16x16x32_bf16 v[196:199],a[232:235],a[136:139],v[196:199]
ds_read_b64_tr_b16 a[218:219],v242 offset:6272
v_exp_f32 v172,v172
v_mfma_f32_16x16x32_bf16 v[180:183],a[236:239],a[140:143],v[180:183]
v_exp_f32 v173,v173
v_exp_f32 v174,v174
v_mfma_f32_16x16x32_bf16 v[184:187],a[236:239],a[144:147],v[184:187]
ds_read_b64_tr_b16 a[220:221],v243 offset:128
v_exp_f32 v175,v175
v_mfma_f32_16x16x32_bf16 v[188:191],a[236:239],a[148:151],v[188:191]
v_exp_f32 v176,v176
v_exp_f32 v177,v177
v_mfma_f32_16x16x32_bf16 v[192:195],a[236:239],a[152:155],v[192:195]
ds_read_b64_tr_b16 a[222:223],v243 offset:6272
v_exp_f32 v178,v178
v_mfma_f32_16x16x32_bf16 v[196:199],a[236:239],a[156:159],v[196:199]
v_exp_f32 v179,v179
v_add_f32 v246,v246,v160
v_mfma_f32_16x16x32_bf16 v[180:183],a[240:243],a[160:163],v[180:183]
v_add_f32 v246,v246,v161
v_add_f32 v246,v246,v162
v_mfma_f32_16x16x32_bf16 v[184:187],a[240:243],a[164:167],v[184:187]
v_add_f32 v246,v246,v163
v_add_f32 v247,v247,v164
v_mfma_f32_16x16x32_bf16 v[188:191],a[240:243],a[168:171],v[188:191]
v_add_f32 v247,v247,v165
v_add_f32 v247,v247,v166
v_mfma_f32_16x16x32_bf16 v[192:195],a[240:243],a[172:175],v[192:195]
buffer_load_dwordx4 v252,s[40:43],s55 offen offset:2048 nt lds
v_mfma_f32_16x16x32_bf16 v[196:199],a[240:243],a[176:179],v[196:199]
v_add_f32 v247,v247,v167
v_add_f32 v248,v248,v168
v_mfma_f32_16x16x32_bf16 v[180:183],a[244:247],a[180:183],v[180:183]
v_add_f32 v248,v248,v169
v_add_f32 v248,v248,v170
v_mfma_f32_16x16x32_bf16 v[184:187],a[244:247],a[184:187],v[184:187]
v_add_f32 v248,v248,v171
v_add_f32 v249,v249,v172
v_mfma_f32_16x16x32_bf16 v[188:191],a[244:247],a[188:191],v[188:191]
v_add_f32 v249,v249,v173
v_add_f32 v249,v249,v174
v_mfma_f32_16x16x32_bf16 v[192:195],a[244:247],a[192:195],v[192:195]
v_add_f32 v249,v249,v175
v_add_f32 v253,v253,v176
v_mfma_f32_16x16x32_bf16 v[196:199],a[244:247],a[196:199],v[196:199]
s_waitcnt lgkmcnt(0)
v_add_f32 v253,v253,v177
v_add_f32 v253,v253,v178
v_mfma_f32_16x16x32_bf16 v[0:3],a[200:203],v[200:203],0
ds_read_b64_tr_b16 a[224:225],v244 offset:128
v_add_f32 v253,v253,v179
v_mfma_f32_16x16x32_bf16 v[4:7],a[200:203],v[204:207],0
ds_read_b64_tr_b16 a[226:227],v244 offset:6272
v_mfma_f32_16x16x32_bf16 v[8:11],a[200:203],v[208:211],0
ds_read_b64_tr_b16 a[228:229],v245 offset:128
v_mfma_f32_16x16x32_bf16 v[12:15],a[200:203],v[212:215],0
buffer_load_dwordx4 v250,s[40:43],s55 offen offset:3072 nt lds
v_mfma_f32_16x16x32_bf16 v[16:19],a[200:203],v[216:219],0
s_add_u32 m0,s57,53248
ds_read_b64_tr_b16 a[230:231],v245 offset:6272
ds_read_b64_tr_b16 a[232:233],v242 offset:256
v_mfma_f32_16x16x32_bf16 v[20:23],a[204:207],v[200:203],0
s_add_u32 s68,s55,4096
v_exp_f32 v180,v180
v_exp_f32 v181,v181
v_mfma_f32_16x16x32_bf16 v[24:27],a[204:207],v[204:207],0
ds_read_b64_tr_b16 a[234:235],v242 offset:6400
v_exp_f32 v182,v182
v_mfma_f32_16x16x32_bf16 v[28:31],a[204:207],v[208:211],0
v_exp_f32 v183,v183
v_exp_f32 v184,v184
v_mfma_f32_16x16x32_bf16 v[32:35],a[204:207],v[212:215],0
ds_read_b64_tr_b16 a[236:237],v243 offset:256
v_exp_f32 v185,v185
v_mfma_f32_16x16x32_bf16 v[36:39],a[204:207],v[216:219],0
v_exp_f32 v186,v186
v_exp_f32 v187,v187
v_mfma_f32_16x16x32_bf16 v[40:43],a[208:211],v[200:203],0
ds_read_b64_tr_b16 a[238:239],v243 offset:6400
v_exp_f32 v188,v188
v_mfma_f32_16x16x32_bf16 v[44:47],a[208:211],v[204:207],0
v_exp_f32 v189,v189
v_exp_f32 v190,v190
v_mfma_f32_16x16x32_bf16 v[48:51],a[208:211],v[208:211],0
ds_read_b64_tr_b16 a[240:241],v244 offset:256
v_exp_f32 v191,v191
v_mfma_f32_16x16x32_bf16 v[52:55],a[208:211],v[212:215],0
buffer_load_dwordx4 v251,s[40:43],s68 offen nt lds
v_mfma_f32_16x16x32_bf16 v[56:59],a[208:211],v[216:219],0
ds_read_b64_tr_b16 a[242:243],v244 offset:6400
v_exp_f32 v192,v192
v_mfma_f32_16x16x32_bf16 v[60:63],a[212:215],v[200:203],0
v_exp_f32 v193,v193
v_exp_f32 v194,v194
v_mfma_f32_16x16x32_bf16 v[64:67],a[212:215],v[204:207],0
ds_read_b64_tr_b16 a[244:245],v245 offset:256
v_exp_f32 v195,v195
v_mfma_f32_16x16x32_bf16 v[68:71],a[212:215],v[208:211],0
v_mfma_f32_16x16x32_bf16 v[72:75],a[212:215],v[212:215],0
ds_read_b64_tr_b16 a[246:247],v245 offset:6400
v_mfma_f32_16x16x32_bf16 v[76:79],a[212:215],v[216:219],0
v_mfma_f32_16x16x32_bf16 v[80:83],a[216:219],v[200:203],0
v_mfma_f32_16x16x32_bf16 v[84:87],a[216:219],v[204:207],0
v_mfma_f32_16x16x32_bf16 v[88:91],a[216:219],v[208:211],0
v_mfma_f32_16x16x32_bf16 v[92:95],a[216:219],v[212:215],0
buffer_load_dwordx4 v252,s[40:43],s68 offen offset:1024 nt lds
v_mfma_f32_16x16x32_bf16 v[96:99],a[216:219],v[216:219],0
s_add_u32 s55,s55,12288
v_mfma_f32_16x16x32_bf16 v[100:103],a[220:223],v[200:203],0
v_mfma_f32_16x16x32_bf16 v[104:107],a[220:223],v[204:207],0
v_mfma_f32_16x16x32_bf16 v[108:111],a[220:223],v[208:211],0
v_mfma_f32_16x16x32_bf16 v[112:115],a[220:223],v[212:215],0
v_mfma_f32_16x16x32_bf16 v[116:119],a[220:223],v[216:219],0
s_waitcnt vmcnt(12) lgkmcnt(0)
s_barrier
v_mfma_f32_16x16x32_bf16 v[120:123],a[224:227],v[200:203],0
ds_read_b128 a[200:203],v240 offset:49152
v_mfma_f32_16x16x32_bf16 v[124:127],a[224:227],v[204:207],0
s_add_u32 m0,s56,24576
v_mfma_f32_16x16x32_bf16 v[128:131],a[224:227],v[208:211],0
buffer_load_dwordx4 v250,s[28:31],s54 offen nt lds
v_mfma_f32_16x16x32_bf16 v[132:135],a[224:227],v[212:215],0
ds_read_b128 a[204:207],v241 offset:49152
v_mfma_f32_16x16x32_bf16 v[136:139],a[224:227],v[216:219],0
v_mfma_f32_16x16x32_bf16 v[140:143],a[228:231],v[200:203],0
v_mfma_f32_16x16x32_bf16 v[144:147],a[228:231],v[204:207],0
ds_read_b128 a[208:211],v240 offset:49280
v_mfma_f32_16x16x32_bf16 v[148:151],a[228:231],v[208:211],0
v_mfma_f32_16x16x32_bf16 v[152:155],a[228:231],v[212:215],0
v_mfma_f32_16x16x32_bf16 v[156:159],a[228:231],v[216:219],0
ds_read_b128 a[212:215],v241 offset:49280
v_mfma_f32_16x16x32_bf16 a[0:3],a[232:235],v[200:203],0
v_mfma_f32_16x16x32_bf16 a[4:7],a[232:235],v[204:207],0
v_mfma_f32_16x16x32_bf16 a[8:11],a[232:235],v[208:211],0
buffer_load_dwordx4 v251,s[28:31],s54 offen offset:1024 nt lds
v_mfma_f32_16x16x32_bf16 a[12:15],a[232:235],v[212:215],0
ds_read_b128 a[216:219],v240 offset:49408
v_mfma_f32_16x16x32_bf16 a[16:19],a[232:235],v[216:219],0
v_mfma_f32_16x16x32_bf16 a[20:23],a[236:239],v[200:203],0
ds_read_b128 a[220:223],v241 offset:49408
v_mfma_f32_16x16x32_bf16 a[24:27],a[236:239],v[204:207],0
v_mfma_f32_16x16x32_bf16 a[28:31],a[236:239],v[208:211],0
v_mfma_f32_16x16x32_bf16 a[32:35],a[236:239],v[212:215],0
v_mfma_f32_16x16x32_bf16 a[36:39],a[236:239],v[216:219],0
v_mfma_f32_16x16x32_bf16 a[40:43],a[240:243],v[200:203],0
v_mfma_f32_16x16x32_bf16 a[44:47],a[240:243],v[204:207],0
v_mfma_f32_16x16x32_bf16 a[48:51],a[240:243],v[208:211],0
buffer_load_dwordx4 v252,s[28:31],s54 offen offset:2048 nt lds
v_mfma_f32_16x16x32_bf16 a[52:55],a[240:243],v[212:215],0
v_mfma_f32_16x16x32_bf16 a[56:59],a[240:243],v[216:219],0
v_mfma_f32_16x16x32_bf16 a[60:63],a[244:247],v[200:203],0
v_cvt_pk_bf16_f32 v200,v160,v161
v_cvt_pk_bf16_f32 v201,v162,v163
v_mfma_f32_16x16x32_bf16 a[64:67],a[244:247],v[204:207],0
v_cvt_pk_bf16_f32 v204,v164,v165
v_cvt_pk_bf16_f32 v205,v166,v167
v_mfma_f32_16x16x32_bf16 a[68:71],a[244:247],v[208:211],0
v_cvt_pk_bf16_f32 v208,v168,v169
v_cvt_pk_bf16_f32 v209,v170,v171
v_mfma_f32_16x16x32_bf16 a[72:75],a[244:247],v[212:215],0
v_cvt_pk_bf16_f32 v212,v172,v173
v_cvt_pk_bf16_f32 v213,v174,v175
v_mfma_f32_16x16x32_bf16 a[76:79],a[244:247],v[216:219],0
s_waitcnt lgkmcnt(0)
v_cvt_pk_bf16_f32 v216,v176,v177
v_cvt_pk_bf16_f32 v217,v178,v179
s_mov_b32 s51,5
LA_%=:
v_mfma_f32_16x16x32_bf16 v[160:163],a[200:203],a[80:83],v[220:223]
ds_read_b128 a[224:227],v240 offset:55296
v_exp_f32 v196,v196
v_mfma_f32_16x16x32_bf16 v[164:167],a[200:203],a[84:87],v[224:227]
v_exp_f32 v197,v197
v_exp_f32 v198,v198
v_mfma_f32_16x16x32_bf16 v[168:171],a[200:203],a[88:91],v[228:231]
s_add_u32 m0,s56,24576
ds_read_b128 a[228:231],v241 offset:55296
v_exp_f32 v199,v199
v_mfma_f32_16x16x32_bf16 v[172:175],a[200:203],a[92:95],v[232:235]
buffer_load_dwordx4 v250,s[28:31],s54 offen offset:3072 nt lds
v_mfma_f32_16x16x32_bf16 v[176:179],a[200:203],a[96:99],v[236:239]
s_add_u32 m0,s56,28672
ds_read_b128 a[232:235],v240 offset:55424
v_cvt_pk_bf16_f32 v202,v180,v181
v_mfma_f32_16x16x32_bf16 v[160:163],a[204:207],a[100:103],v[160:163]
s_add_u32 s68,s54,4096
v_cvt_pk_bf16_f32 v203,v182,v183
v_cvt_pk_bf16_f32 v206,v184,v185
v_mfma_f32_16x16x32_bf16 v[164:167],a[204:207],a[104:107],v[164:167]
ds_read_b128 a[236:239],v241 offset:55424
v_cvt_pk_bf16_f32 v207,v186,v187
v_mfma_f32_16x16x32_bf16 v[168:171],a[204:207],a[108:111],v[168:171]
v_cvt_pk_bf16_f32 v210,v188,v189
v_cvt_pk_bf16_f32 v211,v190,v191
v_mfma_f32_16x16x32_bf16 v[172:175],a[204:207],a[112:115],v[172:175]
buffer_load_dword a250,v254,s[44:47],s74 offen
v_mfma_f32_16x16x32_bf16 v[176:179],a[204:207],a[116:119],v[176:179]
s_add_u32 s74,s74,s73
ds_read_b128 a[240:243],v240 offset:55552
v_cvt_pk_bf16_f32 v214,v192,v193
v_mfma_f32_16x16x32_bf16 v[160:163],a[208:211],a[120:123],v[160:163]
ds_read_b128 a[244:247],v241 offset:55552
v_cvt_pk_bf16_f32 v215,v194,v195
v_mfma_f32_16x16x32_bf16 v[164:167],a[208:211],a[124:127],v[164:167]
v_cvt_pk_bf16_f32 v218,v196,v197
v_cvt_pk_bf16_f32 v219,v198,v199
v_mfma_f32_16x16x32_bf16 v[168:171],a[208:211],a[128:131],v[168:171]
v_add_f32 v246,v246,v180
v_add_f32 v246,v246,v181
v_mfma_f32_16x16x32_bf16 v[172:175],a[208:211],a[132:135],v[172:175]
buffer_load_dwordx4 v251,s[28:31],s68 offen nt lds
v_mfma_f32_16x16x32_bf16 v[176:179],a[208:211],a[136:139],v[176:179]
v_add_f32 v246,v246,v182
v_add_f32 v246,v246,v183
v_mfma_f32_16x16x32_bf16 v[160:163],a[212:215],a[140:143],v[160:163]
v_add_f32 v247,v247,v184
v_add_f32 v247,v247,v185
v_mfma_f32_16x16x32_bf16 v[164:167],a[212:215],a[144:147],v[164:167]
v_add_f32 v247,v247,v186
v_add_f32 v247,v247,v187
v_mfma_f32_16x16x32_bf16 v[168:171],a[212:215],a[148:151],v[168:171]
buffer_load_dword a250,v255,s[12:15],s75 offen sc1
v_mfma_f32_16x16x32_bf16 v[172:175],a[212:215],a[152:155],v[172:175]
s_add_u32 s75,s75,s22
v_add_f32 v248,v248,v188
v_add_f32 v248,v248,v189
v_mfma_f32_16x16x32_bf16 v[176:179],a[212:215],a[156:159],v[176:179]
v_add_f32 v248,v248,v190
v_add_f32 v248,v248,v191
v_mfma_f32_16x16x32_bf16 v[160:163],a[216:219],a[160:163],v[160:163]
v_add_f32 v249,v249,v192
v_add_f32 v249,v249,v193
v_mfma_f32_16x16x32_bf16 v[164:167],a[216:219],a[164:167],v[164:167]
v_add_f32 v249,v249,v194
v_add_f32 v249,v249,v195
v_mfma_f32_16x16x32_bf16 v[168:171],a[216:219],a[168:171],v[168:171]
v_add_f32 v253,v253,v196
v_add_f32 v253,v253,v197
v_mfma_f32_16x16x32_bf16 v[172:175],a[216:219],a[172:175],v[172:175]
buffer_load_dwordx4 v252,s[28:31],s68 offen offset:1024 nt lds
v_mfma_f32_16x16x32_bf16 v[176:179],a[216:219],a[176:179],v[176:179]
s_add_u32 s54,s54,12288
v_add_f32 v253,v253,v198
v_add_f32 v253,v253,v199
v_mfma_f32_16x16x32_bf16 v[160:163],a[220:223],a[180:183],v[160:163]
v_mfma_f32_16x16x32_bf16 v[164:167],a[220:223],a[184:187],v[164:167]
v_mfma_f32_16x16x32_bf16 v[168:171],a[220:223],a[188:191],v[168:171]
v_mfma_f32_16x16x32_bf16 v[172:175],a[220:223],a[192:195],v[172:175]
v_mfma_f32_16x16x32_bf16 v[176:179],a[220:223],a[196:199],v[176:179]
s_waitcnt lgkmcnt(0)
v_mfma_f32_16x16x32_bf16 v[180:183],a[224:227],a[80:83],v[220:223]
ds_read_b64_tr_b16 a[200:201],v242 offset:24576
v_mfma_f32_16x16x32_bf16 v[184:187],a[224:227],a[84:87],v[224:227]
ds_read_b64_tr_b16 a[202:203],v242 offset:30720
v_mfma_f32_16x16x32_bf16 v[188:191],a[224:227],a[88:91],v[228:231]
s_add_u32 m0,s57,0
ds_read_b64_tr_b16 a[204:205],v243 offset:24576
v_mfma_f32_16x16x32_bf16 v[192:195],a[224:227],a[92:95],v[232:235]
buffer_load_dwordx4 v250,s[40:43],s55 offen nt lds
v_mfma_f32_16x16x32_bf16 v[196:199],a[224:227],a[96:99],v[236:239]
ds_read_b64_tr_b16 a[206:207],v243 offset:30720
ds_read_b64_tr_b16 a[208:209],v244 offset:24576
v_mfma_f32_16x16x32_bf16 v[180:183],a[228:231],a[100:103],v[180:183]
v_exp_f32 v160,v160
v_exp_f32 v161,v161
v_mfma_f32_16x16x32_bf16 v[184:187],a[228:231],a[104:107],v[184:187]
ds_read_b64_tr_b16 a[210:211],v244 offset:30720
v_exp_f32 v162,v162
v_mfma_f32_16x16x32_bf16 v[188:191],a[228:231],a[108:111],v[188:191]
v_exp_f32 v163,v163
v_exp_f32 v164,v164
v_mfma_f32_16x16x32_bf16 v[192:195],a[228:231],a[112:115],v[192:195]
ds_read_b64_tr_b16 a[212:213],v245 offset:24576
v_exp_f32 v165,v165
v_mfma_f32_16x16x32_bf16 v[196:199],a[228:231],a[116:119],v[196:199]
v_exp_f32 v166,v166
v_exp_f32 v167,v167
v_mfma_f32_16x16x32_bf16 v[180:183],a[232:235],a[120:123],v[180:183]
ds_read_b64_tr_b16 a[214:215],v245 offset:30720
v_exp_f32 v168,v168
v_mfma_f32_16x16x32_bf16 v[184:187],a[232:235],a[124:127],v[184:187]
v_exp_f32 v169,v169
v_exp_f32 v170,v170
v_mfma_f32_16x16x32_bf16 v[188:191],a[232:235],a[128:131],v[188:191]
ds_read_b64_tr_b16 a[216:217],v242 offset:24704
v_exp_f32 v171,v171
v_mfma_f32_16x16x32_bf16 v[192:195],a[232:235],a[132:135],v[192:195]
buffer_load_dwordx4 v251,s[40:43],s55 offen offset:1024 nt lds
v_mfma_f32_16x16x32_bf16 v[196:199],a[232:235],a[136:139],v[196:199]
ds_read_b64_tr_b16 a[218:219],v242 offset:30848
v_exp_f32 v172,v172
v_mfma_f32_16x16x32_bf16 v[180:183],a[236:239],a[140:143],v[180:183]
v_exp_f32 v173,v173
v_exp_f32 v174,v174
v_mfma_f32_16x16x32_bf16 v[184:187],a[236:239],a[144:147],v[184:187]
ds_read_b64_tr_b16 a[220:221],v243 offset:24704
v_exp_f32 v175,v175
v_mfma_f32_16x16x32_bf16 v[188:191],a[236:239],a[148:151],v[188:191]
v_exp_f32 v176,v176
v_exp_f32 v177,v177
v_mfma_f32_16x16x32_bf16 v[192:195],a[236:239],a[152:155],v[192:195]
ds_read_b64_tr_b16 a[222:223],v243 offset:30848
v_exp_f32 v178,v178
v_mfma_f32_16x16x32_bf16 v[196:199],a[236:239],a[156:159],v[196:199]
v_exp_f32 v179,v179
v_add_f32 v246,v246,v160
v_mfma_f32_16x16x32_bf16 v[180:183],a[240:243],a[160:163],v[180:183]
v_add_f32 v246,v246,v161
v_add_f32 v246,v246,v162
v_mfma_f32_16x16x32_bf16 v[184:187],a[240:243],a[164:167],v[184:187]
v_add_f32 v246,v246,v163
v_add_f32 v247,v247,v164
v_mfma_f32_16x16x32_bf16 v[188:191],a[240:243],a[168:171],v[188:191]
v_add_f32 v247,v247,v165
v_add_f32 v247,v247,v166
v_mfma_f32_16x16x32_bf16 v[192:195],a[240:243],a[172:175],v[192:195]
buffer_load_dwordx4 v252,s[40:43],s55 offen offset:2048 nt lds
v_mfma_f32_16x16x32_bf16 v[196:199],a[240:243],a[176:179],v[196:199]
v_add_f32 v247,v247,v167
v_add_f32 v248,v248,v168
v_mfma_f32_16x16x32_bf16 v[180:183],a[244:247],a[180:183],v[180:183]
v_add_f32 v248,v248,v169
v_add_f32 v248,v248,v170
v_mfma_f32_16x16x32_bf16 v[184:187],a[244:247],a[184:187],v[184:187]
v_add_f32 v248,v248,v171
v_add_f32 v249,v249,v172
v_mfma_f32_16x16x32_bf16 v[188:191],a[244:247],a[188:191],v[188:191]
v_add_f32 v249,v249,v173
v_add_f32 v249,v249,v174
v_mfma_f32_16x16x32_bf16 v[192:195],a[244:247],a[192:195],v[192:195]
v_add_f32 v249,v249,v175
v_add_f32 v253,v253,v176
v_mfma_f32_16x16x32_bf16 v[196:199],a[244:247],a[196:199],v[196:199]
s_waitcnt lgkmcnt(0)
v_add_f32 v253,v253,v177
v_add_f32 v253,v253,v178
v_mfma_f32_16x16x32_bf16 v[0:3],a[200:203],v[200:203],v[0:3]
ds_read_b64_tr_b16 a[224:225],v244 offset:24704
v_add_f32 v253,v253,v179
v_mfma_f32_16x16x32_bf16 v[4:7],a[200:203],v[204:207],v[4:7]
ds_read_b64_tr_b16 a[226:227],v244 offset:30848
v_mfma_f32_16x16x32_bf16 v[8:11],a[200:203],v[208:211],v[8:11]
ds_read_b64_tr_b16 a[228:229],v245 offset:24704
v_mfma_f32_16x16x32_bf16 v[12:15],a[200:203],v[212:215],v[12:15]
buffer_load_dwordx4 v250,s[40:43],s55 offen offset:3072 nt lds
v_mfma_f32_16x16x32_bf16 v[16:19],a[200:203],v[216:219],v[16:19]
s_add_u32 m0,s57,4096
ds_read_b64_tr_b16 a[230:231],v245 offset:30848
ds_read_b64_tr_b16 a[232:233],v242 offset:24832
v_mfma_f32_16x16x32_bf16 v[20:23],a[204:207],v[200:203],v[20:23]
s_add_u32 s68,s55,4096
v_exp_f32 v180,v180
v_exp_f32 v181,v181
v_mfma_f32_16x16x32_bf16 v[24:27],a[204:207],v[204:207],v[24:27]
ds_read_b64_tr_b16 a[234:235],v242 offset:30976
v_exp_f32 v182,v182
v_mfma_f32_16x16x32_bf16 v[28:31],a[204:207],v[208:211],v[28:31]
v_exp_f32 v183,v183
v_exp_f32 v184,v184
v_mfma_f32_16x16x32_bf16 v[32:35],a[204:207],v[212:215],v[32:35]
ds_read_b64_tr_b16 a[236:237],v243 offset:24832
v_exp_f32 v185,v185
v_mfma_f32_16x16x32_bf16 v[36:39],a[204:207],v[216:219],v[36:39]
v_exp_f32 v186,v186
v_exp_f32 v187,v187
v_mfma_f32_16x16x32_bf16 v[40:43],a[208:211],v[200:203],v[40:43]
ds_read_b64_tr_b16 a[238:239],v243 offset:30976
v_exp_f32 v188,v188
v_mfma_f32_16x16x32_bf16 v[44:47],a[208:211],v[204:207],v[44:47]
v_exp_f32 v189,v189
v_exp_f32 v190,v190
v_mfma_f32_16x16x32_bf16 v[48:51],a[208:211],v[208:211],v[48:51]
ds_read_b64_tr_b16 a[240:241],v244 offset:24832
v_exp_f32 v191,v191
v_mfma_f32_16x16x32_bf16 v[52:55],a[208:211],v[212:215],v[52:55]
buffer_load_dwordx4 v251,s[40:43],s68 offen nt lds
v_mfma_f32_16x16x32_bf16 v[56:59],a[208:211],v[216:219],v[56:59]
ds_read_b64_tr_b16 a[242:243],v244 offset:30976
v_exp_f32 v192,v192
v_mfma_f32_16x16x32_bf16 v[60:63],a[212:215],v[200:203],v[60:63]
v_exp_f32 v193,v193
v_exp_f32 v194,v194
v_mfma_f32_16x16x32_bf16 v[64:67],a[212:215],v[204:207],v[64:67]
ds_read_b64_tr_b16 a[244:245],v245 offset:24832
v_exp_f32 v195,v195
v_mfma_f32_16x16x32_bf16 v[68:71],a[212:215],v[208:211],v[68:71]
v_mfma_f32_16x16x32_bf16 v[72:75],a[212:215],v[212:215],v[72:75]
ds_read_b64_tr_b16 a[246:247],v245 offset:30976
v_mfma_f32_16x16x32_bf16 v[76:79],a[212:215],v[216:219],v[76:79]
v_mfma_f32_16x16x32_bf16 v[80:83],a[216:219],v[200:203],v[80:83]
v_mfma_f32_16x16x32_bf16 v[84:87],a[216:219],v[204:207],v[84:87]
v_mfma_f32_16x16x32_bf16 v[88:91],a[216:219],v[208:211],v[88:91]
v_mfma_f32_16x16x32_bf16 v[92:95],a[216:219],v[212:215],v[92:95]
buffer_load_dwordx4 v252,s[40:43],s68 offen offset:1024 nt lds
v_mfma_f32_16x16x32_bf16 v[96:99],a[216:219],v[216:219],v[96:99]
s_add_u32 s55,s55,12288
v_mfma_f32_16x16x32_bf16 v[100:103],a[220:223],v[200:203],v[100:103]
v_mfma_f32_16x16x32_bf16 v[104:107],a[220:223],v[204:207],v[104:107]
v_mfma_f32_16x16x32_bf16 v[108:111],a[220:223],v[208:211],v[108:111]
v_mfma_f32_16x16x32_bf16 v[112:115],a[220:223],v[212:215],v[112:115]
v_mfma_f32_16x16x32_bf16 v[116:119],a[220:223],v[216:219],v[116:119]
s_waitcnt vmcnt(14) lgkmcnt(0)
s_barrier
v_mfma_f32_16x16x32_bf16 v[120:123],a[224:227],v[200:203],v[120:123]
ds_read_b128 a[200:203],v240
v_mfma_f32_16x16x32_bf16 v[124:127],a[224:227],v[204:207],v[124:127]
s_add_u32 m0,s56,49152
v_mfma_f32_16x16x32_bf16 v[128:131],a[224:227],v[208:211],v[128:131]
buffer_load_dwordx4 v250,s[28:31],s54 offen nt lds
v_mfma_f32_16x16x32_bf16 v[132:135],a[224:227],v[212:215],v[132:135]
ds_read_b128 a[204:207],v241
v_mfma_f32_16x16x32_bf16 v[136:139],a[224:227],v[216:219],v[136:139]
v_mfma_f32_16x16x32_bf16 v[140:143],a[228:231],v[200:203],v[140:143]
v_mfma_f32_16x16x32_bf16 v[144:147],a[228:231],v[204:207],v[144:147]
ds_read_b128 a[208:211],v240 offset:128
v_mfma_f32_16x16x32_bf16 v[148:151],a[228:231],v[208:211],v[148:151]
buffer_load_dword a250,v254,s[44:47],s74 offen
v_mfma_f32_16x16x32_bf16 v[152:155],a[228:231],v[212:215],v[152:155]
s_add_u32 s74,s74,s73
v_mfma_f32_16x16x32_bf16 v[156:159],a[228:231],v[216:219],v[156:159]
ds_read_b128 a[212:215],v241 offset:128
v_mfma_f32_16x16x32_bf16 a[0:3],a[232:235],v[200:203],a[0:3]
v_mfma_f32_16x16x32_bf16 a[4:7],a[232:235],v[204:207],a[4:7]
v_mfma_f32_16x16x32_bf16 a[8:11],a[232:235],v[208:211],a[8:11]
buffer_load_dwordx4 v251,s[28:31],s54 offen offset:1024 nt lds
v_mfma_f32_16x16x32_bf16 a[12:15],a[232:235],v[212:215],a[12:15]
ds_read_b128 a[216:219],v240 offset:256
v_mfma_f32_16x16x32_bf16 a[16:19],a[232:235],v[216:219],a[16:19]
v_mfma_f32_16x16x32_bf16 a[20:23],a[236:239],v[200:203],a[20:23]
ds_read_b128 a[220:223],v241 offset:256
v_mfma_f32_16x16x32_bf16 a[24:27],a[236:239],v[204:207],a[24:27]
v_mfma_f32_16x16x32_bf16 a[28:31],a[236:239],v[208:211],a[28:31]
v_mfma_f32_16x16x32_bf16 a[32:35],a[236:239],v[212:215],a[32:35]
v_mfma_f32_16x16x32_bf16 a[36:39],a[236:239],v[216:219],a[36:39]
v_mfma_f32_16x16x32_bf16 a[40:43],a[240:243],v[200:203],a[40:43]
v_mfma_f32_16x16x32_bf16 a[44:47],a[240:243],v[204:207],a[44:47]
v_mfma_f32_16x16x32_bf16 a[48:51],a[240:243],v[208:211],a[48:51]
buffer_load_dwordx4 v252,s[28:31],s54 offen offset:2048 nt lds
v_mfma_f32_16x16x32_bf16 a[52:55],a[240:243],v[212:215],a[52:55]
v_mfma_f32_16x16x32_bf16 a[56:59],a[240:243],v[216:219],a[56:59]
v_mfma_f32_16x16x32_bf16 a[60:63],a[244:247],v[200:203],a[60:63]
v_cvt_pk_bf16_f32 v200,v160,v161
v_cvt_pk_bf16_f32 v201,v162,v163
v_mfma_f32_16x16x32_bf16 a[64:67],a[244:247],v[204:207],a[64:67]
v_cvt_pk_bf16_f32 v204,v164,v165
v_cvt_pk_bf16_f32 v205,v166,v167
v_mfma_f32_16x16x32_bf16 a[68:71],a[244:247],v[208:211],a[68:71]
v_cvt_pk_bf16_f32 v208,v168,v169
v_cvt_pk_bf16_f32 v209,v170,v171
v_mfma_f32_16x16x32_bf16 a[72:75],a[244:247],v[212:215],a[72:75]
v_cvt_pk_bf16_f32 v212,v172,v173
v_cvt_pk_bf16_f32 v213,v174,v175
v_mfma_f32_16x16x32_bf16 a[76:79],a[244:247],v[216:219],a[76:79]
s_waitcnt lgkmcnt(0)
v_cvt_pk_bf16_f32 v216,v176,v177
v_cvt_pk_bf16_f32 v217,v178,v179
v_mfma_f32_16x16x32_bf16 v[160:163],a[200:203],a[80:83],v[220:223]
ds_read_b128 a[224:227],v240 offset:6144
v_exp_f32 v196,v196
v_mfma_f32_16x16x32_bf16 v[164:167],a[200:203],a[84:87],v[224:227]
v_exp_f32 v197,v197
v_exp_f32 v198,v198
v_mfma_f32_16x16x32_bf16 v[168:171],a[200:203],a[88:91],v[228:231]
s_add_u32 m0,s56,49152
ds_read_b128 a[228:231],v241 offset:6144
v_exp_f32 v199,v199
v_mfma_f32_16x16x32_bf16 v[172:175],a[200:203],a[92:95],v[232:235]
buffer_load_dwordx4 v250,s[28:31],s54 offen offset:3072 nt lds
v_mfma_f32_16x16x32_bf16 v[176:179],a[200:203],a[96:99],v[236:239]
s_add_u32 m0,s56,53248
ds_read_b128 a[232:235],v240 offset:6272
v_cvt_pk_bf16_f32 v202,v180,v181
v_mfma_f32_16x16x32_bf16 v[160:163],a[204:207],a[100:103],v[160:163]
s_add_u32 s68,s54,4096
v_cvt_pk_bf16_f32 v203,v182,v183
v_cvt_pk_bf16_f32 v206,v184,v185
v_mfma_f32_16x16x32_bf16 v[164:167],a[204:207],a[104:107],v[164:167]
ds_read_b128 a[236:239],v241 offset:6272
v_cvt_pk_bf16_f32 v207,v186,v187
v_mfma_f32_16x16x32_bf16 v[168:171],a[204:207],a[108:111],v[168:171]
v_cvt_pk_bf16_f32 v210,v188,v189
v_cvt_pk_bf16_f32 v211,v190,v191
v_mfma_f32_16x16x32_bf16 v[172:175],a[204:207],a[112:115],v[172:175]
buffer_load_dword a250,v254,s[44:47],s74 offen
v_mfma_f32_16x16x32_bf16 v[176:179],a[204:207],a[116:119],v[176:179]
s_add_u32 s74,s74,s73
ds_read_b128 a[240:243],v240 offset:6400
v_cvt_pk_bf16_f32 v214,v192,v193
v_mfma_f32_16x16x32_bf16 v[160:163],a[208:211],a[120:123],v[160:163]
ds_read_b128 a[244:247],v241 offset:6400
v_cvt_pk_bf16_f32 v215,v194,v195
v_mfma_f32_16x16x32_bf16 v[164:167],a[208:211],a[124:127],v[164:167]
v_cvt_pk_bf16_f32 v218,v196,v197
v_cvt_pk_bf16_f32 v219,v198,v199
v_mfma_f32_16x16x32_bf16 v[168:171],a[208:211],a[128:131],v[168:171]
v_add_f32 v246,v246,v180
v_add_f32 v246,v246,v181
v_mfma_f32_16x16x32_bf16 v[172:175],a[208:211],a[132:135],v[172:175]
buffer_load_dwordx4 v251,s[28:31],s68 offen nt lds
v_mfma_f32_16x16x32_bf16 v[176:179],a[208:211],a[136:139],v[176:179]
v_add_f32 v246,v246,v182
v_add_f32 v246,v246,v183
v_mfma_f32_16x16x32_bf16 v[160:163],a[212:215],a[140:143],v[160:163]
v_add_f32 v247,v247,v184
v_add_f32 v247,v247,v185
v_mfma_f32_16x16x32_bf16 v[164:167],a[212:215],a[144:147],v[164:167]
v_add_f32 v247,v247,v186
v_add_f32 v247,v247,v187
v_mfma_f32_16x16x32_bf16 v[168:171],a[212:215],a[148:151],v[168:171]
buffer_load_dword a250,v255,s[12:15],s75 offen sc1
v_mfma_f32_16x16x32_bf16 v[172:175],a[212:215],a[152:155],v[172:175]
s_add_u32 s75,s75,s22
v_add_f32 v248,v248,v188
v_add_f32 v248,v248,v189
v_mfma_f32_16x16x32_bf16 v[176:179],a[212:215],a[156:159],v[176:179]
v_add_f32 v248,v248,v190
v_add_f32 v248,v248,v191
v_mfma_f32_16x16x32_bf16 v[160:163],a[216:219],a[160:163],v[160:163]
v_add_f32 v249,v249,v192
v_add_f32 v249,v249,v193
v_mfma_f32_16x16x32_bf16 v[164:167],a[216:219],a[164:167],v[164:167]
v_add_f32 v249,v249,v194
v_add_f32 v249,v249,v195
v_mfma_f32_16x16x32_bf16 v[168:171],a[216:219],a[168:171],v[168:171]
v_add_f32 v253,v253,v196
v_add_f32 v253,v253,v197
v_mfma_f32_16x16x32_bf16 v[172:175],a[216:219],a[172:175],v[172:175]
buffer_load_dwordx4 v252,s[28:31],s68 offen offset:1024 nt lds
v_mfma_f32_16x16x32_bf16 v[176:179],a[216:219],a[176:179],v[176:179]
s_add_u32 s54,s54,12288
v_add_f32 v253,v253,v198
v_add_f32 v253,v253,v199
v_mfma_f32_16x16x32_bf16 v[160:163],a[220:223],a[180:183],v[160:163]
v_mfma_f32_16x16x32_bf16 v[164:167],a[220:223],a[184:187],v[164:167]
v_mfma_f32_16x16x32_bf16 v[168:171],a[220:223],a[188:191],v[168:171]
v_mfma_f32_16x16x32_bf16 v[172:175],a[220:223],a[192:195],v[172:175]
v_mfma_f32_16x16x32_bf16 v[176:179],a[220:223],a[196:199],v[176:179]
s_waitcnt lgkmcnt(0)
v_mfma_f32_16x16x32_bf16 v[180:183],a[224:227],a[80:83],v[220:223]
ds_read_b64_tr_b16 a[200:201],v242 offset:49152
v_mfma_f32_16x16x32_bf16 v[184:187],a[224:227],a[84:87],v[224:227]
ds_read_b64_tr_b16 a[202:203],v242 offset:55296
v_mfma_f32_16x16x32_bf16 v[188:191],a[224:227],a[88:91],v[228:231]
s_add_u32 m0,s57,24576
ds_read_b64_tr_b16 a[204:205],v243 offset:49152
v_mfma_f32_16x16x32_bf16 v[192:195],a[224:227],a[92:95],v[232:235]
buffer_load_dwordx4 v250,s[40:43],s55 offen nt lds
v_mfma_f32_16x16x32_bf16 v[196:199],a[224:227],a[96:99],v[236:239]
ds_read_b64_tr_b16 a[206:207],v243 offset:55296
ds_read_b64_tr_b16 a[208:209],v244 offset:49152
v_mfma_f32_16x16x32_bf16 v[180:183],a[228:231],a[100:103],v[180:183]
v_exp_f32 v160,v160
v_exp_f32 v161,v161
v_mfma_f32_16x16x32_bf16 v[184:187],a[228:231],a[104:107],v[184:187]
ds_read_b64_tr_b16 a[210:211],v244 offset:55296
v_exp_f32 v162,v162
v_mfma_f32_16x16x32_bf16 v[188:191],a[228:231],a[108:111],v[188:191]
v_exp_f32 v163,v163
v_exp_f32 v164,v164
v_mfma_f32_16x16x32_bf16 v[192:195],a[228:231],a[112:115],v[192:195]
ds_read_b64_tr_b16 a[212:213],v245 offset:49152
v_exp_f32 v165,v165
v_mfma_f32_16x16x32_bf16 v[196:199],a[228:231],a[116:119],v[196:199]
v_exp_f32 v166,v166
v_exp_f32 v167,v167
v_mfma_f32_16x16x32_bf16 v[180:183],a[232:235],a[120:123],v[180:183]
ds_read_b64_tr_b16 a[214:215],v245 offset:55296
v_exp_f32 v168,v168
v_mfma_f32_16x16x32_bf16 v[184:187],a[232:235],a[124:127],v[184:187]
v_exp_f32 v169,v169
v_exp_f32 v170,v170
v_mfma_f32_16x16x32_bf16 v[188:191],a[232:235],a[128:131],v[188:191]
ds_read_b64_tr_b16 a[216:217],v242 offset:49280
v_exp_f32 v171,v171
v_mfma_f32_16x16x32_bf16 v[192:195],a[232:235],a[132:135],v[192:195]
buffer_load_dwordx4 v251,s[40:43],s55 offen offset:1024 nt lds
v_mfma_f32_16x16x32_bf16 v[196:199],a[232:235],a[136:139],v[196:199]
ds_read_b64_tr_b16 a[218:219],v242 offset:55424
v_exp_f32 v172,v172
v_mfma_f32_16x16x32_bf16 v[180:183],a[236:239],a[140:143],v[180:183]
v_exp_f32 v173,v173
v_exp_f32 v174,v174
v_mfma_f32_16x16x32_bf16 v[184:187],a[236:239],a[144:147],v[184:187]
ds_read_b64_tr_b16 a[220:221],v243 offset:49280
v_exp_f32 v175,v175
v_mfma_f32_16x16x32_bf16 v[188:191],a[236:239],a[148:151],v[188:191]
v_exp_f32 v176,v176
v_exp_f32 v177,v177
v_mfma_f32_16x16x32_bf16 v[192:195],a[236:239],a[152:155],v[192:195]
ds_read_b64_tr_b16 a[222:223],v243 offset:55424
v_exp_f32 v178,v178
v_mfma_f32_16x16x32_bf16 v[196:199],a[236:239],a[156:159],v[196:199]
v_exp_f32 v179,v179
v_add_f32 v246,v246,v160
v_mfma_f32_16x16x32_bf16 v[180:183],a[240:243],a[160:163],v[180:183]
v_add_f32 v246,v246,v161
v_add_f32 v246,v246,v162
v_mfma_f32_16x16x32_bf16 v[184:187],a[240:243],a[164:167],v[184:187]
v_add_f32 v246,v246,v163
v_add_f32 v247,v247,v164
v_mfma_f32_16x16x32_bf16 v[188:191],a[240:243],a[168:171],v[188:191]
v_add_f32 v247,v247,v165
v_add_f32 v247,v247,v166
v_mfma_f32_16x16x32_bf16 v[192:195],a[240:243],a[172:175],v[192:195]
buffer_load_dwordx4 v252,s[40:43],s55 offen offset:2048 nt lds
v_mfma_f32_16x16x32_bf16 v[196:199],a[240:243],a[176:179],v[196:199]
v_add_f32 v247,v247,v167
v_add_f32 v248,v248,v168
v_mfma_f32_16x16x32_bf16 v[180:183],a[244:247],a[180:183],v[180:183]
v_add_f32 v248,v248,v169
v_add_f32 v248,v248,v170
v_mfma_f32_16x16x32_bf16 v[184:187],a[244:247],a[184:187],v[184:187]
v_add_f32 v248,v248,v171
v_add_f32 v249,v249,v172
v_mfma_f32_16x16x32_bf16 v[188:191],a[244:247],a[188:191],v[188:191]
v_add_f32 v249,v249,v173
v_add_f32 v249,v249,v174
v_mfma_f32_16x16x32_bf16 v[192:195],a[244:247],a[192:195],v[192:195]
v_add_f32 v249,v249,v175
v_add_f32 v253,v253,v176
v_mfma_f32_16x16x32_bf16 v[196:199],a[244:247],a[196:199],v[196:199]
s_waitcnt lgkmcnt(0)
v_add_f32 v253,v253,v177
v_add_f32 v253,v253,v178
v_mfma_f32_16x16x32_bf16 v[0:3],a[200:203],v[200:203],v[0:3]
ds_read_b64_tr_b16 a[224:225],v244 offset:49280
v_add_f32 v253,v253,v179
v_mfma_f32_16x16x32_bf16 v[4:7],a[200:203],v[204:207],v[4:7]
ds_read_b64_tr_b16 a[226:227],v244 offset:55424
v_mfma_f32_16x16x32_bf16 v[8:11],a[200:203],v[208:211],v[8:11]
ds_read_b64_tr_b16 a[228:229],v245 offset:49280
v_mfma_f32_16x16x32_bf16 v[12:15],a[200:203],v[212:215],v[12:15]
buffer_load_dwordx4 v250,s[40:43],s55 offen offset:3072 nt lds
v_mfma_f32_16x16x32_bf16 v[16:19],a[200:203],v[216:219],v[16:19]
s_add_u32 m0,s57,28672
ds_read_b64_tr_b16 a[230:231],v245 offset:55424
ds_read_b64_tr_b16 a[232:233],v242 offset:49408
v_mfma_f32_16x16x32_bf16 v[20:23],a[204:207],v[200:203],v[20:23]
s_add_u32 s68,s55,4096
v_exp_f32 v180,v180
v_exp_f32 v181,v181
v_mfma_f32_16x16x32_bf16 v[24:27],a[204:207],v[204:207],v[24:27]
ds_read_b64_tr_b16 a[234:235],v242 offset:55552
v_exp_f32 v182,v182
v_mfma_f32_16x16x32_bf16 v[28:31],a[204:207],v[208:211],v[28:31]
v_exp_f32 v183,v183
v_exp_f32 v184,v184
v_mfma_f32_16x16x32_bf16 v[32:35],a[204:207],v[212:215],v[32:35]
ds_read_b64_tr_b16 a[236:237],v243 offset:49408
v_exp_f32 v185,v185
v_mfma_f32_16x16x32_bf16 v[36:39],a[204:207],v[216:219],v[36:39]
v_exp_f32 v186,v186
v_exp_f32 v187,v187
v_mfma_f32_16x16x32_bf16 v[40:43],a[208:211],v[200:203],v[40:43]
ds_read_b64_tr_b16 a[238:239],v243 offset:55552
v_exp_f32 v188,v188
v_mfma_f32_16x16x32_bf16 v[44:47],a[208:211],v[204:207],v[44:47]
v_exp_f32 v189,v189
v_exp_f32 v190,v190
v_mfma_f32_16x16x32_bf16 v[48:51],a[208:211],v[208:211],v[48:51]
ds_read_b64_tr_b16 a[240:241],v244 offset:49408
v_exp_f32 v191,v191
v_mfma_f32_16x16x32_bf16 v[52:55],a[208:211],v[212:215],v[52:55]
buffer_load_dwordx4 v251,s[40:43],s68 offen nt lds
v_mfma_f32_16x16x32_bf16 v[56:59],a[208:211],v[216:219],v[56:59]
ds_read_b64_tr_b16 a[242:243],v244 offset:55552
v_exp_f32 v192,v192
v_mfma_f32_16x16x32_bf16 v[60:63],a[212:215],v[200:203],v[60:63]
v_exp_f32 v193,v193
v_exp_f32 v194,v194
v_mfma_f32_16x16x32_bf16 v[64:67],a[212:215],v[204:207],v[64:67]
ds_read_b64_tr_b16 a[244:245],v245 offset:49408
v_exp_f32 v195,v195
v_mfma_f32_16x16x32_bf16 v[68:71],a[212:215],v[208:211],v[68:71]
v_mfma_f32_16x16x32_bf16 v[72:75],a[212:215],v[212:215],v[72:75]
ds_read_b64_tr_b16 a[246:247],v245 offset:55552
v_mfma_f32_16x16x32_bf16 v[76:79],a[212:215],v[216:219],v[76:79]
v_mfma_f32_16x16x32_bf16 v[80:83],a[216:219],v[200:203],v[80:83]
v_mfma_f32_16x16x32_bf16 v[84:87],a[216:219],v[204:207],v[84:87]
v_mfma_f32_16x16x32_bf16 v[88:91],a[216:219],v[208:211],v[88:91]
v_mfma_f32_16x16x32_bf16 v[92:95],a[216:219],v[212:215],v[92:95]
buffer_load_dwordx4 v252,s[40:43],s68 offen offset:1024 nt lds
v_mfma_f32_16x16x32_bf16 v[96:99],a[216:219],v[216:219],v[96:99]
s_add_u32 s55,s55,12288
v_mfma_f32_16x16x32_bf16 v[100:103],a[220:223],v[200:203],v[100:103]
v_mfma_f32_16x16x32_bf16 v[104:107],a[220:223],v[204:207],v[104:107]
v_mfma_f32_16x16x32_bf16 v[108:111],a[220:223],v[208:211],v[108:111]
v_mfma_f32_16x16x32_bf16 v[112:115],a[220:223],v[212:215],v[112:115]
v_mfma_f32_16x16x32_bf16 v[116:119],a[220:223],v[216:219],v[116:119]
s_waitcnt vmcnt(15) lgkmcnt(0)
s_barrier
v_mfma_f32_16x16x32_bf16 v[120:123],a[224:227],v[200:203],v[120:123]
ds_read_b128 a[200:203],v240 offset:24576
v_mfma_f32_16x16x32_bf16 v[124:127],a[224:227],v[204:207],v[124:127]
s_add_u32 m0,s56,0
v_mfma_f32_16x16x32_bf16 v[128:131],a[224:227],v[208:211],v[128:131]
buffer_load_dwordx4 v250,s[28:31],s54 offen nt lds
v_mfma_f32_16x16x32_bf16 v[132:135],a[224:227],v[212:215],v[132:135]
ds_read_b128 a[204:207],v241 offset:24576
v_mfma_f32_16x16x32_bf16 v[136:139],a[224:227],v[216:219],v[136:139]
v_mfma_f32_16x16x32_bf16 v[140:143],a[228:231],v[200:203],v[140:143]
v_mfma_f32_16x16x32_bf16 v[144:147],a[228:231],v[204:207],v[144:147]
ds_read_b128 a[208:211],v240 offset:24704
v_mfma_f32_16x16x32_bf16 v[148:151],a[228:231],v[208:211],v[148:151]
buffer_load_dword a250,v254,s[44:47],s74 offen
v_mfma_f32_16x16x32_bf16 v[152:155],a[228:231],v[212:215],v[152:155]
s_add_u32 s74,s74,s73
v_mfma_f32_16x16x32_bf16 v[156:159],a[228:231],v[216:219],v[156:159]
ds_read_b128 a[212:215],v241 offset:24704
v_mfma_f32_16x16x32_bf16 a[0:3],a[232:235],v[200:203],a[0:3]
s_sub_u32 s51,s51,1
v_mfma_f32_16x16x32_bf16 a[4:7],a[232:235],v[204:207],a[4:7]
s_load_dword s26,s[8:9],0 glc
v_mfma_f32_16x16x32_bf16 a[8:11],a[232:235],v[208:211],a[8:11]
buffer_load_dwordx4 v251,s[28:31],s54 offen offset:1024 nt lds
v_mfma_f32_16x16x32_bf16 a[12:15],a[232:235],v[212:215],a[12:15]
ds_read_b128 a[216:219],v240 offset:24832
v_mfma_f32_16x16x32_bf16 a[16:19],a[232:235],v[216:219],a[16:19]
v_mfma_f32_16x16x32_bf16 a[20:23],a[236:239],v[200:203],a[20:23]
ds_read_b128 a[220:223],v241 offset:24832
v_mfma_f32_16x16x32_bf16 a[24:27],a[236:239],v[204:207],a[24:27]
v_mfma_f32_16x16x32_bf16 a[28:31],a[236:239],v[208:211],a[28:31]
v_mfma_f32_16x16x32_bf16 a[32:35],a[236:239],v[212:215],a[32:35]
v_mfma_f32_16x16x32_bf16 a[36:39],a[236:239],v[216:219],a[36:39]
v_mfma_f32_16x16x32_bf16 a[40:43],a[240:243],v[200:203],a[40:43]
v_mfma_f32_16x16x32_bf16 a[44:47],a[240:243],v[204:207],a[44:47]
v_mfma_f32_16x16x32_bf16 a[48:51],a[240:243],v[208:211],a[48:51]
buffer_load_dwordx4 v252,s[28:31],s54 offen offset:2048 nt lds
v_mfma_f32_16x16x32_bf16 a[52:55],a[240:243],v[212:215],a[52:55]
v_mfma_f32_16x16x32_bf16 a[56:59],a[240:243],v[216:219],a[56:59]
v_mfma_f32_16x16x32_bf16 a[60:63],a[244:247],v[200:203],a[60:63]
v_cvt_pk_bf16_f32 v200,v160,v161
v_cvt_pk_bf16_f32 v201,v162,v163
v_mfma_f32_16x16x32_bf16 a[64:67],a[244:247],v[204:207],a[64:67]
v_cvt_pk_bf16_f32 v204,v164,v165
v_cvt_pk_bf16_f32 v205,v166,v167
v_mfma_f32_16x16x32_bf16 a[68:71],a[244:247],v[208:211],a[68:71]
v_cvt_pk_bf16_f32 v208,v168,v169
v_cvt_pk_bf16_f32 v209,v170,v171
v_mfma_f32_16x16x32_bf16 a[72:75],a[244:247],v[212:215],a[72:75]
v_cvt_pk_bf16_f32 v212,v172,v173
v_cvt_pk_bf16_f32 v213,v174,v175
v_mfma_f32_16x16x32_bf16 a[76:79],a[244:247],v[216:219],a[76:79]
s_waitcnt lgkmcnt(0)
v_cvt_pk_bf16_f32 v216,v176,v177
v_cvt_pk_bf16_f32 v217,v178,v179
s_cmp_lg_u32 s26,0
s_cselect_b32 s64,s7,s20
s_cmp_eq_u32 s51,1
s_cselect_b32 s14,s64,0
s_cselect_b32 s46,s6,0
s_cmp_eq_u32 s51,0
s_cbranch_scc1 LAX_%=
v_mfma_f32_16x16x32_bf16 v[160:163],a[200:203],a[80:83],v[220:223]
ds_read_b128 a[224:227],v240 offset:30720
v_exp_f32 v196,v196
v_mfma_f32_16x16x32_bf16 v[164:167],a[200:203],a[84:87],v[224:227]
v_exp_f32 v197,v197
v_exp_f32 v198,v198
v_mfma_f32_16x16x32_bf16 v[168:171],a[200:203],a[88:91],v[228:231]
s_add_u32 m0,s56,0
ds_read_b128 a[228:231],v241 offset:30720
v_exp_f32 v199,v199
v_mfma_f32_16x16x32_bf16 v[172:175],a[200:203],a[92:95],v[232:235]
buffer_load_dwordx4 v250,s[28:31],s54 offen offset:3072 nt lds
v_mfma_f32_16x16x32_bf16 v[176:179],a[200:203],a[96:99],v[236:239]
s_add_u32 m0,s56,4096
ds_read_b128 a[232:235],v240 offset:30848
v_cvt_pk_bf16_f32 v202,v180,v181
v_mfma_f32_16x16x32_bf16 v[160:163],a[204:207],a[100:103],v[160:163]
s_add_u32 s68,s54,4096
v_cvt_pk_bf16_f32 v203,v182,v183
v_cvt_pk_bf16_f32 v206,v184,v185
v_mfma_f32_16x16x32_bf16 v[164:167],a[204:207],a[104:107],v[164:167]
ds_read_b128 a[236:239],v241 offset:30848
v_cvt_pk_bf16_f32 v207,v186,v187
v_mfma_f32_16x16x32_bf16 v[168:171],a[204:207],a[108:111],v[168:171]
v_cvt_pk_bf16_f32 v210,v188,v189
v_cvt_pk_bf16_f32 v211,v190,v191
v_mfma_f32_16x16x32_bf16 v[172:175],a[204:207],a[112:115],v[172:175]
buffer_load_dword a250,v254,s[44:47],s74 offen
v_mfma_f32_16x16x32_bf16 v[176:179],a[204:207],a[116:119],v[176:179]
s_add_u32 s74,s74,s73
ds_read_b128 a[240:243],v240 offset:30976
v_cvt_pk_bf16_f32 v214,v192,v193
v_mfma_f32_16x16x32_bf16 v[160:163],a[208:211],a[120:123],v[160:163]
ds_read_b128 a[244:247],v241 offset:30976
v_cvt_pk_bf16_f32 v215,v194,v195
v_mfma_f32_16x16x32_bf16 v[164:167],a[208:211],a[124:127],v[164:167]
v_cvt_pk_bf16_f32 v218,v196,v197
v_cvt_pk_bf16_f32 v219,v198,v199
v_mfma_f32_16x16x32_bf16 v[168:171],a[208:211],a[128:131],v[168:171]
v_add_f32 v246,v246,v180
v_add_f32 v246,v246,v181
v_mfma_f32_16x16x32_bf16 v[172:175],a[208:211],a[132:135],v[172:175]
buffer_load_dwordx4 v251,s[28:31],s68 offen nt lds
v_mfma_f32_16x16x32_bf16 v[176:179],a[208:211],a[136:139],v[176:179]
v_add_f32 v246,v246,v182
v_add_f32 v246,v246,v183
v_mfma_f32_16x16x32_bf16 v[160:163],a[212:215],a[140:143],v[160:163]
v_add_f32 v247,v247,v184
v_add_f32 v247,v247,v185
v_mfma_f32_16x16x32_bf16 v[164:167],a[212:215],a[144:147],v[164:167]
v_add_f32 v247,v247,v186
v_add_f32 v247,v247,v187
v_mfma_f32_16x16x32_bf16 v[168:171],a[212:215],a[148:151],v[168:171]
buffer_load_dword a250,v255,s[12:15],s75 offen sc1
v_mfma_f32_16x16x32_bf16 v[172:175],a[212:215],a[152:155],v[172:175]
s_add_u32 s75,s75,s22
v_add_f32 v248,v248,v188
v_add_f32 v248,v248,v189
v_mfma_f32_16x16x32_bf16 v[176:179],a[212:215],a[156:159],v[176:179]
v_add_f32 v248,v248,v190
v_add_f32 v248,v248,v191
v_mfma_f32_16x16x32_bf16 v[160:163],a[216:219],a[160:163],v[160:163]
v_add_f32 v249,v249,v192
v_add_f32 v249,v249,v193
v_mfma_f32_16x16x32_bf16 v[164:167],a[216:219],a[164:167],v[164:167]
v_add_f32 v249,v249,v194
v_add_f32 v249,v249,v195
v_mfma_f32_16x16x32_bf16 v[168:171],a[216:219],a[168:171],v[168:171]
v_add_f32 v253,v253,v196
v_add_f32 v253,v253,v197
v_mfma_f32_16x16x32_bf16 v[172:175],a[216:219],a[172:175],v[172:175]
buffer_load_dwordx4 v252,s[28:31],s68 offen offset:1024 nt lds
v_mfma_f32_16x16x32_bf16 v[176:179],a[216:219],a[176:179],v[176:179]
s_add_u32 s54,s54,12288
v_add_f32 v253,v253,v198
v_add_f32 v253,v253,v199
v_mfma_f32_16x16x32_bf16 v[160:163],a[220:223],a[180:183],v[160:163]
v_mfma_f32_16x16x32_bf16 v[164:167],a[220:223],a[184:187],v[164:167]
v_mfma_f32_16x16x32_bf16 v[168:171],a[220:223],a[188:191],v[168:171]
v_mfma_f32_16x16x32_bf16 v[172:175],a[220:223],a[192:195],v[172:175]
v_mfma_f32_16x16x32_bf16 v[176:179],a[220:223],a[196:199],v[176:179]
s_waitcnt lgkmcnt(0)
v_mfma_f32_16x16x32_bf16 v[180:183],a[224:227],a[80:83],v[220:223]
ds_read_b64_tr_b16 a[200:201],v242
v_mfma_f32_16x16x32_bf16 v[184:187],a[224:227],a[84:87],v[224:227]
ds_read_b64_tr_b16 a[202:203],v242 offset:6144
v_mfma_f32_16x16x32_bf16 v[188:191],a[224:227],a[88:91],v[228:231]
s_add_u32 m0,s57,49152
ds_read_b64_tr_b16 a[204:205],v243
v_mfma_f32_16x16x32_bf16 v[192:195],a[224:227],a[92:95],v[232:235]
buffer_load_dwordx4 v250,s[40:43],s55 offen nt lds
v_mfma_f32_16x16x32_bf16 v[196:199],a[224:227],a[96:99],v[236:239]
ds_read_b64_tr_b16 a[206:207],v243 offset:6144
ds_read_b64_tr_b16 a[208:209],v244
v_mfma_f32_16x16x32_bf16 v[180:183],a[228:231],a[100:103],v[180:183]
v_exp_f32 v160,v160
v_exp_f32 v161,v161
v_mfma_f32_16x16x32_bf16 v[184:187],a[228:231],a[104:107],v[184:187]
ds_read_b64_tr_b16 a[210:211],v244 offset:6144
v_exp_f32 v162,v162
v_mfma_f32_16x16x32_bf16 v[188:191],a[228:231],a[108:111],v[188:191]
v_exp_f32 v163,v163
v_exp_f32 v164,v164
v_mfma_f32_16x16x32_bf16 v[192:195],a[228:231],a[112:115],v[192:195]
ds_read_b64_tr_b16 a[212:213],v245
v_exp_f32 v165,v165
v_mfma_f32_16x16x32_bf16 v[196:199],a[228:231],a[116:119],v[196:199]
v_exp_f32 v166,v166
v_exp_f32 v167,v167
v_mfma_f32_16x16x32_bf16 v[180:183],a[232:235],a[120:123],v[180:183]
ds_read_b64_tr_b16 a[214:215],v245 offset:6144
v_exp_f32 v168,v168
v_mfma_f32_16x16x32_bf16 v[184:187],a[232:235],a[124:127],v[184:187]
v_exp_f32 v169,v169
v_exp_f32 v170,v170
v_mfma_f32_16x16x32_bf16 v[188:191],a[232:235],a[128:131],v[188:191]
ds_read_b64_tr_b16 a[216:217],v242 offset:128
v_exp_f32 v171,v171
v_mfma_f32_16x16x32_bf16 v[192:195],a[232:235],a[132:135],v[192:195]
buffer_load_dwordx4 v251,s[40:43],s55 offen offset:1024 nt lds
v_mfma_f32_16x16x32_bf16 v[196:199],a[232:235],a[136:139],v[196:199]
ds_read_b64_tr_b16 a[218:219],v242 offset:6272
v_exp_f32 v172,v172
v_mfma_f32_16x16x32_bf16 v[180:183],a[236:239],a[140:143],v[180:183]
v_exp_f32 v173,v173
v_exp_f32 v174,v174
v_mfma_f32_16x16x32_bf16 v[184:187],a[236:239],a[144:147],v[184:187]
ds_read_b64_tr_b16 a[220:221],v243 offset:128
v_exp_f32 v175,v175
v_mfma_f32_16x16x32_bf16 v[188:191],a[236:239],a[148:151],v[188:191]
v_exp_f32 v176,v176
v_exp_f32 v177,v177
v_mfma_f32_16x16x32_bf16 v[192:195],a[236:239],a[152:155],v[192:195]
ds_read_b64_tr_b16 a[222:223],v243 offset:6272
v_exp_f32 v178,v178
v_mfma_f32_16x16x32_bf16 v[196:199],a[236:239],a[156:159],v[196:199]
v_exp_f32 v179,v179
v_add_f32 v246,v246,v160
v_mfma_f32_16x16x32_bf16 v[180:183],a[240:243],a[160:163],v[180:183]
v_add_f32 v246,v246,v161
v_add_f32 v246,v246,v162
v_mfma_f32_16x16x32_bf16 v[184:187],a[240:243],a[164:167],v[184:187]
v_add_f32 v246,v246,v163
v_add_f32 v247,v247,v164
v_mfma_f32_16x16x32_bf16 v[188:191],a[240:243],a[168:171],v[188:191]
v_add_f32 v247,v247,v165
v_add_f32 v247,v247,v166
v_mfma_f32_16x16x32_bf16 v[192:195],a[240:243],a[172:175],v[192:195]
buffer_load_dwordx4 v252,s[40:43],s55 offen offset:2048 nt lds
v_mfma_f32_16x16x32_bf16 v[196:199],a[240:243],a[176:179],v[196:199]
v_add_f32 v247,v247,v167
v_add_f32 v248,v248,v168
v_mfma_f32_16x16x32_bf16 v[180:183],a[244:247],a[180:183],v[180:183]
v_add_f32 v248,v248,v169
v_add_f32 v248,v248,v170
v_mfma_f32_16x16x32_bf16 v[184:187],a[244:247],a[184:187],v[184:187]
v_add_f32 v248,v248,v171
v_add_f32 v249,v249,v172
v_mfma_f32_16x16x32_bf16 v[188:191],a[244:247],a[188:191],v[188:191]
v_add_f32 v249,v249,v173
v_add_f32 v249,v249,v174
v_mfma_f32_16x16x32_bf16 v[192:195],a[244:247],a[192:195],v[192:195]
v_add_f32 v249,v249,v175
v_add_f32 v253,v253,v176
v_mfma_f32_16x16x32_bf16 v[196:199],a[244:247],a[196:199],v[196:199]
s_waitcnt lgkmcnt(0)
v_add_f32 v253,v253,v177
v_add_f32 v253,v253,v178
v_mfma_f32_16x16x32_bf16 v[0:3],a[200:203],v[200:203],v[0:3]
ds_read_b64_tr_b16 a[224:225],v244 offset:128
v_add_f32 v253,v253,v179
v_mfma_f32_16x16x32_bf16 v[4:7],a[200:203],v[204:207],v[4:7]
ds_read_b64_tr_b16 a[226:227],v244 offset:6272
v_mfma_f32_16x16x32_bf16 v[8:11],a[200:203],v[208:211],v[8:11]
ds_read_b64_tr_b16 a[228:229],v245 offset:128
v_mfma_f32_16x16x32_bf16 v[12:15],a[200:203],v[212:215],v[12:15]
buffer_load_dwordx4 v250,s[40:43],s55 offen offset:3072 nt lds
v_mfma_f32_16x16x32_bf16 v[16:19],a[200:203],v[216:219],v[16:19]
s_add_u32 m0,s57,53248
ds_read_b64_tr_b16 a[230:231],v245 offset:6272
ds_read_b64_tr_b16 a[232:233],v242 offset:256
v_mfma_f32_16x16x32_bf16 v[20:23],a[204:207],v[200:203],v[20:23]
s_add_u32 s68,s55,4096
v_exp_f32 v180,v180
v_exp_f32 v181,v181
v_mfma_f32_16x16x32_bf16 v[24:27],a[204:207],v[204:207],v[24:27]
ds_read_b64_tr_b16 a[234:235],v242 offset:6400
v_exp_f32 v182,v182
v_mfma_f32_16x16x32_bf16 v[28:31],a[204:207],v[208:211],v[28:31]
v_exp_f32 v183,v183
v_exp_f32 v184,v184
v_mfma_f32_16x16x32_bf16 v[32:35],a[204:207],v[212:215],v[32:35]
ds_read_b64_tr_b16 a[236:237],v243 offset:256
v_exp_f32 v185,v185
v_mfma_f32_16x16x32_bf16 v[36:39],a[204:207],v[216:219],v[36:39]
v_exp_f32 v186,v186
v_exp_f32 v187,v187
v_mfma_f32_16x16x32_bf16 v[40:43],a[208:211],v[200:203],v[40:43]
ds_read_b64_tr_b16 a[238:239],v243 offset:6400
v_exp_f32 v188,v188
v_mfma_f32_16x16x32_bf16 v[44:47],a[208:211],v[204:207],v[44:47]
v_exp_f32 v189,v189
v_exp_f32 v190,v190
v_mfma_f32_16x16x32_bf16 v[48:51],a[208:211],v[208:211],v[48:51]
ds_read_b64_tr_b16 a[240:241],v244 offset:256
v_exp_f32 v191,v191
v_mfma_f32_16x16x32_bf16 v[52:55],a[208:211],v[212:215],v[52:55]
buffer_load_dwordx4 v251,s[40:43],s68 offen nt lds
v_mfma_f32_16x16x32_bf16 v[56:59],a[208:211],v[216:219],v[56:59]
ds_read_b64_tr_b16 a[242:243],v244 offset:6400
v_exp_f32 v192,v192
v_mfma_f32_16x16x32_bf16 v[60:63],a[212:215],v[200:203],v[60:63]
v_exp_f32 v193,v193
v_exp_f32 v194,v194
v_mfma_f32_16x16x32_bf16 v[64:67],a[212:215],v[204:207],v[64:67]
ds_read_b64_tr_b16 a[244:245],v245 offset:256
v_exp_f32 v195,v195
v_mfma_f32_16x16x32_bf16 v[68:71],a[212:215],v[208:211],v[68:71]
v_mfma_f32_16x16x32_bf16 v[72:75],a[212:215],v[212:215],v[72:75]
ds_read_b64_tr_b16 a[246:247],v245 offset:6400
v_mfma_f32_16x16x32_bf16 v[76:79],a[212:215],v[216:219],v[76:79]
v_mfma_f32_16x16x32_bf16 v[80:83],a[216:219],v[200:203],v[80:83]
v_mfma_f32_16x16x32_bf16 v[84:87],a[216:219],v[204:207],v[84:87]
v_mfma_f32_16x16x32_bf16 v[88:91],a[216:219],v[208:211],v[88:91]
v_mfma_f32_16x16x32_bf16 v[92:95],a[216:219],v[212:215],v[92:95]
buffer_load_dwordx4 v252,s[40:43],s68 offen offset:1024 nt lds
v_mfma_f32_16x16x32_bf16 v[96:99],a[216:219],v[216:219],v[96:99]
s_add_u32 s55,s55,12288
v_mfma_f32_16x16x32_bf16 v[100:103],a[220:223],v[200:203],v[100:103]
v_mfma_f32_16x16x32_bf16 v[104:107],a[220:223],v[204:207],v[104:107]
v_mfma_f32_16x16x32_bf16 v[108:111],a[220:223],v[208:211],v[108:111]
v_mfma_f32_16x16x32_bf16 v[112:115],a[220:223],v[212:215],v[112:115]
v_mfma_f32_16x16x32_bf16 v[116:119],a[220:223],v[216:219],v[116:119]
s_waitcnt vmcnt(15) lgkmcnt(0)
s_barrier
v_mfma_f32_16x16x32_bf16 v[120:123],a[224:227],v[200:203],v[120:123]
ds_read_b128 a[200:203],v240 offset:49152
v_mfma_f32_16x16x32_bf16 v[124:127],a[224:227],v[204:207],v[124:127]
s_add_u32 m0,s56,24576
v_mfma_f32_16x16x32_bf16 v[128:131],a[224:227],v[208:211],v[128:131]
buffer_load_dwordx4 v250,s[28:31],s54 offen nt lds
v_mfma_f32_16x16x32_bf16 v[132:135],a[224:227],v[212:215],v[132:135]
ds_read_b128 a[204:207],v241 offset:49152
v_mfma_f32_16x16x32_bf16 v[136:139],a[224:227],v[216:219],v[136:139]
v_mfma_f32_16x16x32_bf16 v[140:143],a[228:231],v[200:203],v[140:143]
v_mfma_f32_16x16x32_bf16 v[144:147],a[228:231],v[204:207],v[144:147]
ds_read_b128 a[208:211],v240 offset:49280
v_mfma_f32_16x16x32_bf16 v[148:151],a[228:231],v[208:211],v[148:151]
buffer_load_dword a250,v254,s[44:47],s74 offen
v_mfma_f32_16x16x32_bf16 v[152:155],a[228:231],v[212:215],v[152:155]
s_add_u32 s74,s74,s73
v_mfma_f32_16x16x32_bf16 v[156:159],a[228:231],v[216:219],v[156:159]
ds_read_b128 a[212:215],v241 offset:49280
v_mfma_f32_16x16x32_bf16 a[0:3],a[232:235],v[200:203],a[0:3]
v_mfma_f32_16x16x32_bf16 a[4:7],a[232:235],v[204:207],a[4:7]
v_mfma_f32_16x16x32_bf16 a[8:11],a[232:235],v[208:211],a[8:11]
buffer_load_dwordx4 v251,s[28:31],s54 offen offset:1024 nt lds
v_mfma_f32_16x16x32_bf16 a[12:15],a[232:235],v[212:215],a[12:15]
ds_read_b128 a[216:219],v240 offset:49408
v_mfma_f32_16x16x32_bf16 a[16:19],a[232:235],v[216:219],a[16:19]
v_mfma_f32_16x16x32_bf16 a[20:23],a[236:239],v[200:203],a[20:23]
ds_read_b128 a[220:223],v241 offset:49408
v_mfma_f32_16x16x32_bf16 a[24:27],a[236:239],v[204:207],a[24:27]
v_mfma_f32_16x16x32_bf16 a[28:31],a[236:239],v[208:211],a[28:31]
v_mfma_f32_16x16x32_bf16 a[32:35],a[236:239],v[212:215],a[32:35]
v_mfma_f32_16x16x32_bf16 a[36:39],a[236:239],v[216:219],a[36:39]
v_mfma_f32_16x16x32_bf16 a[40:43],a[240:243],v[200:203],a[40:43]
v_mfma_f32_16x16x32_bf16 a[44:47],a[240:243],v[204:207],a[44:47]
v_mfma_f32_16x16x32_bf16 a[48:51],a[240:243],v[208:211],a[48:51]
buffer_load_dwordx4 v252,s[28:31],s54 offen offset:2048 nt lds
v_mfma_f32_16x16x32_bf16 a[52:55],a[240:243],v[212:215],a[52:55]
v_mfma_f32_16x16x32_bf16 a[56:59],a[240:243],v[216:219],a[56:59]
v_mfma_f32_16x16x32_bf16 a[60:63],a[244:247],v[200:203],a[60:63]
v_cvt_pk_bf16_f32 v200,v160,v161
v_cvt_pk_bf16_f32 v201,v162,v163
v_mfma_f32_16x16x32_bf16 a[64:67],a[244:247],v[204:207],a[64:67]
v_cvt_pk_bf16_f32 v204,v164,v165
v_cvt_pk_bf16_f32 v205,v166,v167
v_mfma_f32_16x16x32_bf16 a[68:71],a[244:247],v[208:211],a[68:71]
v_cvt_pk_bf16_f32 v208,v168,v169
v_cvt_pk_bf16_f32 v209,v170,v171
v_mfma_f32_16x16x32_bf16 a[72:75],a[244:247],v[212:215],a[72:75]
v_cvt_pk_bf16_f32 v212,v172,v173
v_cvt_pk_bf16_f32 v213,v174,v175
v_mfma_f32_16x16x32_bf16 a[76:79],a[244:247],v[216:219],a[76:79]
s_waitcnt lgkmcnt(0)
v_cvt_pk_bf16_f32 v216,v176,v177
v_cvt_pk_bf16_f32 v217,v178,v179
s_branch LA_%=
LAX_%=:
ds_read_b64_tr_b16 a[200:201],v242
ds_read_b64_tr_b16 a[202:203],v242 offset:6144
ds_read_b64_tr_b16 a[204:205],v243
ds_read_b64_tr_b16 a[206:207],v243 offset:6144
ds_read_b64_tr_b16 a[208:209],v244
ds_read_b64_tr_b16 a[210:211],v244 offset:6144
ds_read_b64_tr_b16 a[212:213],v245
ds_read_b64_tr_b16 a[214:215],v245 offset:6144
ds_read_b64_tr_b16 a[216:217],v242 offset:128
ds_read_b64_tr_b16 a[218:219],v242 offset:6272
ds_read_b64_tr_b16 a[220:221],v243 offset:128
ds_read_b64_tr_b16 a[222:223],v243 offset:6272
ds_read_b64_tr_b16 a[224:225],v244 offset:128
ds_read_b64_tr_b16 a[226:227],v244 offset:6272
ds_read_b64_tr_b16 a[228:229],v245 offset:128
ds_read_b64_tr_b16 a[230:231],v245 offset:6272
ds_read_b64_tr_b16 a[232:233],v242 offset:256
ds_read_b64_tr_b16 a[234:235],v242 offset:6400
ds_read_b64_tr_b16 a[236:237],v243 offset:256
ds_read_b64_tr_b16 a[238:239],v243 offset:6400
ds_read_b64_tr_b16 a[240:241],v244 offset:256
ds_read_b64_tr_b16 a[242:243],v244 offset:6400
ds_read_b64_tr_b16 a[244:245],v245 offset:256
ds_read_b64_tr_b16 a[246:247],v245 offset:6400
v_exp_f32 v196,v196
v_exp_f32 v197,v197
v_exp_f32 v198,v198
v_exp_f32 v199,v199
v_cvt_pk_bf16_f32 v202,v180,v181
v_cvt_pk_bf16_f32 v203,v182,v183
v_cvt_pk_bf16_f32 v206,v184,v185
v_cvt_pk_bf16_f32 v207,v186,v187
v_cvt_pk_bf16_f32 v210,v188,v189
v_cvt_pk_bf16_f32 v211,v190,v191
v_cvt_pk_bf16_f32 v214,v192,v193
v_cvt_pk_bf16_f32 v215,v194,v195
v_cvt_pk_bf16_f32 v218,v196,v197
v_cvt_pk_bf16_f32 v219,v198,v199
v_add_f32 v246,v246,v180
v_add_f32 v246,v246,v181
v_add_f32 v246,v246,v182
v_add_f32 v246,v246,v183
v_add_f32 v247,v247,v184
v_add_f32 v247,v247,v185
v_add_f32 v247,v247,v186
v_add_f32 v247,v247,v187
v_add_f32 v248,v248,v188
v_add_f32 v248,v248,v189
v_add_f32 v248,v248,v190
v_add_f32 v248,v248,v191
v_add_f32 v249,v249,v192
v_add_f32 v249,v249,v193
v_add_f32 v249,v249,v194
v_add_f32 v249,v249,v195
v_add_f32 v253,v253,v196
v_add_f32 v253,v253,v197
v_add_f32 v253,v253,v198
v_add_f32 v253,v253,v199
v_max3_f32 v160,v246,v247,v248
v_min3_f32 v161,v246,v247,v248
v_max3_f32 v160,v160,v249,v253
v_min3_f32 v161,v161,v249,v253
v_mov_b32 v254,163824
v_cmp_lt_f32 vcc,0x53800000,v160
v_cmp_gt_f32 s[64:65],s5,v161
s_nop 1
s_or_b64 vcc,vcc,s[64:65]
s_cbranch_scc0 LNOBAD_%=
ds_write_b32 v254,v254
LNOBAD_%=:
s_mov_b32 s10,4
buffer_load_dword v162,off,s[8:11],0 sc1
s_waitcnt vmcnt(0) lgkmcnt(0)
s_barrier
ds_read_b32 v255,v254
v_readfirstlane_b32 s73,v162
s_lshl_b32 s68,s48,5
s_lshl_b32 s69,s49,1
s_add_u32 s68,s68,s69
s_add_u32 s68,s68,s18
s_lshl_b32 s69,s62,9
s_add_u32 s69,s69,s68
s_mul_i32 s69,s69,61440
s_mul_i32 s70,s19,5120
s_add_u32 s69,s69,s70
s_add_u32 s69,s69,65536
s_add_u32 s36,s98,s69
s_addc_u32 s37,s99,0
s_mov_b32 s38,56320
s_lshl_b32 s64,s62,8
s_lshl_b32 s65,s48,4
s_add_u32 s64,s64,s65
s_add_u32 s64,s64,s49
s_lshl_b32 s64,s64,6
s_add_u32 s8,s98,s64
s_addc_u32 s9,s99,0
s_cmp_eq_u32 s16,0
s_cselect_b32 s10,4,0
s_waitcnt lgkmcnt(0)
v_readfirstlane_b32 s64,v255
s_xor_b32 s60,s60,1
s_nop 0
s_mul_i32 s64,s64,s60
s_cmp_lg_u32 s64,0
s_cbranch_scc1 LSLOW_%=
s_mov_b32 s60,0
s_cmp_eq_u32 s61,0
s_cbranch_scc0 LTOO_%=
s_xor_b32 s62,s62,1
s_mov_b32 s61,1
s_cmp_eq_u32 s62,0
s_cselect_b64 s[20:21],s[76:77],s[86:87]
s_mul_i32 s64,s48,204800
s_add_u32 s20,s20,s64
s_addc_u32 s21,s21,0
s_mov_b32 s22,204800
s_cmp_eq_u32 s62,0
s_cselect_b64 s[24:25],s[82:83],s[92:93]
s_lshl_b32 s64,s49,1
s_add_u32 s64,s64,s18
s_mul_i32 s64,s64,384
s_add_u32 s24,s24,s64
s_addc_u32 s25,s25,0
s_sub_u32 s24,s24,2048
s_subb_u32 s25,s25,0
s_mov_b32 s26,7854464
v_mbcnt_lo_u32_b32 v254,-1,0
v_mbcnt_hi_u32_b32 v254,-1,v254
v_add_u32 v240,0,v254
v_mul_u32_u24 v255,2731,v240
v_lshrrev_b32 v255,16,v255
v_mul_u32_u24 v243,24,v255
v_sub_u32 v240,v240,v243
v_and_b32 v243,2,v255
v_lshlrev_b32 v243,1,v243
v_xor_b32 v240,v240,v243
v_lshlrev_b32 v240,4,v240
v_mul_u32_u24 v255,12288,v255
v_add_u32 v240,v240,v255
s_mul_i32 s64,s19,98304
s_add_u32 s64,s64,2048
v_add_u32 v240,s64,v240
v_add_u32 v241,64,v254
v_mul_u32_u24 v255,2731,v241
v_lshrrev_b32 v255,16,v255
v_mul_u32_u24 v243,24,v255
v_sub_u32 v241,v241,v243
v_and_b32 v243,2,v255
v_lshlrev_b32 v243,1,v243
v_xor_b32 v241,v241,v243
v_lshlrev_b32 v241,4,v241
v_mul_u32_u24 v255,12288,v255
v_add_u32 v241,v241,v255
s_mul_i32 s64,s19,98304
s_add_u32 s64,s64,1024
v_add_u32 v241,s64,v241
v_add_u32 v242,128,v254
v_mul_u32_u24 v255,2731,v242
v_lshrrev_b32 v255,16,v255
v_mul_u32_u24 v243,24,v255
v_sub_u32 v242,v242,v243
v_and_b32 v243,2,v255
v_lshlrev_b32 v243,1,v243
v_xor_b32 v242,v242,v243
v_lshlrev_b32 v242,4,v242
v_mul_u32_u24 v255,12288,v255
v_add_u32 v242,v242,v255
s_mul_i32 s64,s19,98304
s_add_u32 s64,s64,0
v_add_u32 v242,s64,v242
v_lshrrev_b32 v255,3,v254
v_and_b32 v243,6,v255
v_xor_b32 v243,v243,v254
v_and_b32 v243,7,v243
v_lshlrev_b32 v243,4,v243
v_mul_u32_u24 v255,1280,v255
v_add_u32 v243,v243,v255
s_mov_b32 s35,s62
s_mov_b32 s52,0
s_mul_i32 s53,s16,51200
s_mul_i32 s59,s16,5120
s_add_u32 s59,s59,98336
s_mul_i32 s64,s18,6144
s_mul_i32 s65,s19,12296
s_add_u32 s58,s64,s65
s_lshl_b32 s68,s48,5
s_lshl_b32 s69,s49,1
s_add_u32 s68,s68,s69
s_add_u32 s68,s68,s18
s_mul_i32 s69,s68,196608
s_cmp_eq_u32 s62,0
s_cselect_b64 s[28:29],s[78:79],s[88:89]
s_add_u32 s28,s28,s69
s_addc_u32 s29,s29,0
s_cmp_eq_u32 s62,0
s_cselect_b64 s[40:41],s[80:81],s[90:91]
s_add_u32 s40,s40,s69
s_addc_u32 s41,s41,0
s_mov_b32 s30,196608
s_mov_b32 s42,196608
s_mul_i32 s54,s19,6144
s_mov_b32 s55,s54
v_mfma_f32_16x16x32_bf16 v[0:3],a[200:203],v[200:203],v[0:3]
s_add_u32 m0,s58,0
v_mov_b32 v160,v246
v_mfma_f32_16x16x32_bf16 v[4:7],a[200:203],v[204:207],v[4:7]
buffer_load_dwordx4 v240,s[24:27],s52 offen lds
v_mfma_f32_16x16x32_bf16 v[8:11],a[200:203],v[208:211],v[8:11]
v_permlane32_swap_b32 v160,v246
v_mov_b32 v161,v247
v_mfma_f32_16x16x32_bf16 v[12:15],a[200:203],v[212:215],v[12:15]
v_add_f32 v246,v246,v160
v_mov_b32 v162,v248
v_mfma_f32_16x16x32_bf16 v[16:19],a[200:203],v[216:219],v[16:19]
v_mov_b32 v160,v246
v_permlane32_swap_b32 v161,v247
v_mfma_f32_16x16x32_bf16 v[20:23],a[204:207],v[200:203],v[20:23]
v_add_f32 v247,v247,v161
v_permlane32_swap_b32 v162,v248
v_mfma_f32_16x16x32_bf16 v[24:27],a[204:207],v[204:207],v[24:27]
buffer_load_dwordx4 v241,s[24:27],s52 offen offset:1024 lds
v_mfma_f32_16x16x32_bf16 v[28:31],a[204:207],v[208:211],v[28:31]
v_permlane16_swap_b32 v160,v246
v_mov_b32 v161,v247
v_mfma_f32_16x16x32_bf16 v[32:35],a[204:207],v[212:215],v[32:35]
v_add_f32 v246,v246,v160
v_add_f32 v248,v248,v162
v_mfma_f32_16x16x32_bf16 v[36:39],a[204:207],v[216:219],v[36:39]
v_rcp_f32 v170,v246
v_mfma_f32_16x16x32_bf16 v[40:43],a[208:211],v[200:203],v[40:43]
v_mov_b32 v171,v170
v_permlane16_swap_b32 v161,v247
v_mfma_f32_16x16x32_bf16 v[44:47],a[208:211],v[204:207],v[44:47]
buffer_load_dwordx4 v242,s[24:27],s52 offen offset:2048 lds
v_mfma_f32_16x16x32_bf16 v[48:51],a[208:211],v[208:211],v[48:51]
s_add_u32 m0,s58,3072
v_add_f32 v247,v247,v161
v_mov_b32 v162,v248
v_mfma_f32_16x16x32_bf16 v[52:55],a[208:211],v[212:215],v[52:55]
s_add_u32 s66,s52,196608
v_rcp_f32 v172,v247
v_mfma_f32_16x16x32_bf16 v[56:59],a[208:211],v[216:219],v[56:59]
v_mov_b32 v173,v172
v_permlane16_swap_b32 v162,v248
v_mfma_f32_16x16x32_bf16 v[60:63],a[212:215],v[200:203],v[60:63]
v_add_f32 v248,v248,v162
v_mov_b32 v163,v249
v_mfma_f32_16x16x32_bf16 v[64:67],a[212:215],v[204:207],v[64:67]
buffer_load_dwordx4 v240,s[24:27],s66 offen lds
v_mfma_f32_16x16x32_bf16 v[68:71],a[212:215],v[208:211],v[68:71]
v_rcp_f32 v174,v248
v_mfma_f32_16x16x32_bf16 v[72:75],a[212:215],v[212:215],v[72:75]
v_mov_b32 v175,v174
v_permlane32_swap_b32 v163,v249
v_mfma_f32_16x16x32_bf16 v[76:79],a[212:215],v[216:219],v[76:79]
v_add_f32 v249,v249,v163
v_mov_b32 v164,v253
v_mfma_f32_16x16x32_bf16 v[80:83],a[216:219],v[200:203],v[80:83]
v_mov_b32 v163,v249
v_mfma_f32_16x16x32_bf16 v[84:87],a[216:219],v[204:207],v[84:87]
buffer_load_dwordx4 v241,s[24:27],s66 offen offset:1024 lds
v_mfma_f32_16x16x32_bf16 v[88:91],a[216:219],v[208:211],v[88:91]
v_permlane16_swap_b32 v163,v249
v_permlane32_swap_b32 v164,v253
v_mfma_f32_16x16x32_bf16 v[92:95],a[216:219],v[212:215],v[92:95]
v_add_f32 v249,v249,v163
v_add_f32 v253,v253,v164
v_mfma_f32_16x16x32_bf16 v[96:99],a[216:219],v[216:219],v[96:99]
v_rcp_f32 v176,v249
v_mfma_f32_16x16x32_bf16 v[100:103],a[220:223],v[200:203],v[100:103]
v_mov_b32 v177,v176
v_mov_b32 v164,v253
v_mfma_f32_16x16x32_bf16 v[104:107],a[220:223],v[204:207],v[104:107]
buffer_load_dwordx4 v242,s[24:27],s66 offen offset:2048 lds
v_mfma_f32_16x16x32_bf16 v[108:111],a[220:223],v[208:211],v[108:111]
s_add_u32 s52,s52,393216
v_permlane16_swap_b32 v164,v253
v_mfma_f32_16x16x32_bf16 v[112:115],a[220:223],v[212:215],v[112:115]
s_add_u32 m0,s59,40960
v_add_f32 v253,v253,v164
v_mfma_f32_16x16x32_bf16 v[116:119],a[220:223],v[216:219],v[116:119]
v_rcp_f32 v178,v253
v_mfma_f32_16x16x32_bf16 v[120:123],a[224:227],v[200:203],v[120:123]
v_mov_b32 v179,v178
v_mfma_f32_16x16x32_bf16 v[124:127],a[224:227],v[204:207],v[124:127]
buffer_load_dwordx4 v243,s[20:23],s53 offen lds
v_mfma_f32_16x16x32_bf16 v[128:131],a[224:227],v[208:211],v[128:131]
s_add_u32 s67,s53,9216
v_mfma_f32_16x16x32_bf16 v[132:135],a[224:227],v[212:215],v[132:135]
v_mfma_f32_16x16x32_bf16 v[136:139],a[224:227],v[216:219],v[136:139]
v_mfma_f32_16x16x32_bf16 v[140:143],a[228:231],v[200:203],v[140:143]
v_mfma_f32_16x16x32_bf16 v[144:147],a[228:231],v[204:207],v[144:147]
buffer_load_dwordx4 v243,s[20:23],s67 offen offset:1024 lds
v_mfma_f32_16x16x32_bf16 v[148:151],a[228:231],v[208:211],v[148:151]
s_add_u32 s67,s53,18432
v_mfma_f32_16x16x32_bf16 v[152:155],a[228:231],v[212:215],v[152:155]
v_mfma_f32_16x16x32_bf16 v[156:159],a[228:231],v[216:219],v[156:159]
v_mfma_f32_16x16x32_bf16 a[0:3],a[232:235],v[200:203],a[0:3]
v_mfma_f32_16x16x32_bf16 a[4:7],a[232:235],v[204:207],a[4:7]
buffer_load_dwordx4 v243,s[20:23],s67 offen offset:2048 lds
v_mfma_f32_16x16x32_bf16 a[8:11],a[232:235],v[208:211],a[8:11]
s_add_u32 s67,s53,27648
v_mfma_f32_16x16x32_bf16 a[12:15],a[232:235],v[212:215],a[12:15]
v_mfma_f32_16x16x32_bf16 a[16:19],a[232:235],v[216:219],a[16:19]
v_mfma_f32_16x16x32_bf16 a[20:23],a[236:239],v[200:203],a[20:23]
v_mfma_f32_16x16x32_bf16 a[24:27],a[236:239],v[204:207],a[24:27]
buffer_load_dwordx4 v243,s[20:23],s67 offen offset:3072 lds
v_mfma_f32_16x16x32_bf16 a[28:31],a[236:239],v[208:211],a[28:31]
s_add_u32 m0,s59,45056
v_mfma_f32_16x16x32_bf16 a[32:35],a[236:239],v[212:215],a[32:35]
s_add_u32 s67,s53,40960
v_mfma_f32_16x16x32_bf16 a[36:39],a[236:239],v[216:219],a[36:39]
v_mfma_f32_16x16x32_bf16 a[40:43],a[240:243],v[200:203],a[40:43]
v_mfma_f32_16x16x32_bf16 a[44:47],a[240:243],v[204:207],a[44:47]
buffer_load_dwordx4 v243,s[20:23],s67 offen lds
v_mfma_f32_16x16x32_bf16 a[48:51],a[240:243],v[208:211],a[48:51]
v_mfma_f32_16x16x32_bf16 a[52:55],a[240:243],v[212:215],a[52:55]
v_mfma_f32_16x16x32_bf16 a[56:59],a[240:243],v[216:219],a[56:59]
v_mfma_f32_16x16x32_bf16 a[60:63],a[244:247],v[200:203],a[60:63]
v_mfma_f32_16x16x32_bf16 a[64:67],a[244:247],v[204:207],a[64:67]
v_mfma_f32_16x16x32_bf16 a[68:71],a[244:247],v[208:211],a[68:71]
v_mfma_f32_16x16x32_bf16 a[72:75],a[244:247],v[212:215],a[72:75]
v_mfma_f32_16x16x32_bf16 a[76:79],a[244:247],v[216:219],a[76:79]
v_mbcnt_lo_u32_b32 v182,-1,0
v_mbcnt_hi_u32_b32 v182,-1,v182
v_lshlrev_b32 v182,4,v182
v_pk_mul_f32 v[44:45],v[44:45],v[172:173]
v_pk_mul_f32 v[46:47],v[46:47],v[172:173]
v_pk_mul_f32 v[64:65],v[64:65],v[172:173]
v_pk_mul_f32 v[66:67],v[66:67],v[172:173]
v_cvt_pk_bf16_f32 v44,v44,v45
v_cvt_pk_bf16_f32 v45,v46,v47
v_cvt_pk_bf16_f32 v46,v64,v65
v_cvt_pk_bf16_f32 v47,v66,v67
v_accvgpr_write_b32 a104,v44
v_accvgpr_write_b32 a105,v45
v_accvgpr_write_b32 a106,v46
v_accvgpr_write_b32 a107,v47
v_pk_mul_f32 v[48:49],v[48:49],v[174:175]
v_pk_mul_f32 v[50:51],v[50:51],v[174:175]
v_pk_mul_f32 v[68:69],v[68:69],v[174:175]
v_pk_mul_f32 v[70:71],v[70:71],v[174:175]
v_cvt_pk_bf16_f32 v48,v48,v49
v_cvt_pk_bf16_f32 v49,v50,v51
v_cvt_pk_bf16_f32 v50,v68,v69
v_cvt_pk_bf16_f32 v51,v70,v71
v_accvgpr_write_b32 a108,v48
v_accvgpr_write_b32 a109,v49
v_accvgpr_write_b32 a110,v50
s_add_u32 s53,s53,128
s_add_u32 m0,s58,24584
s_nop 0
buffer_load_dwordx4 v240,s[24:27],s52 offen lds
v_accvgpr_write_b32 a111,v51
v_pk_mul_f32 v[0:1],v[0:1],v[170:171]
v_pk_mul_f32 v[2:3],v[2:3],v[170:171]
v_pk_mul_f32 v[20:21],v[20:21],v[170:171]
v_pk_mul_f32 v[22:23],v[22:23],v[170:171]
v_cvt_pk_bf16_f32 v0,v0,v1
v_cvt_pk_bf16_f32 v1,v2,v3
v_cvt_pk_bf16_f32 v2,v20,v21
v_cvt_pk_bf16_f32 v3,v22,v23
s_mov_b32 s72,0
buffer_store_dwordx4 v[0:3],v182,s[36:39],s72 offen sc1
v_pk_mul_f32 v[52:53],v[52:53],v[176:177]
v_pk_mul_f32 v[54:55],v[54:55],v[176:177]
v_pk_mul_f32 v[72:73],v[72:73],v[176:177]
v_pk_mul_f32 v[74:75],v[74:75],v[176:177]
v_cvt_pk_bf16_f32 v52,v52,v53
v_cvt_pk_bf16_f32 v53,v54,v55
v_cvt_pk_bf16_f32 v54,v72,v73
v_cvt_pk_bf16_f32 v55,v74,v75
v_accvgpr_write_b32 a112,v52
v_accvgpr_write_b32 a113,v53
v_accvgpr_write_b32 a114,v54
v_accvgpr_write_b32 a115,v55
buffer_load_dwordx4 v241,s[24:27],s52 offen offset:1024 lds
v_pk_mul_f32 v[56:57],v[56:57],v[178:179]
v_pk_mul_f32 v[58:59],v[58:59],v[178:179]
v_pk_mul_f32 v[76:77],v[76:77],v[178:179]
v_pk_mul_f32 v[78:79],v[78:79],v[178:179]
v_cvt_pk_bf16_f32 v56,v56,v57
v_cvt_pk_bf16_f32 v57,v58,v59
v_cvt_pk_bf16_f32 v58,v76,v77
v_cvt_pk_bf16_f32 v59,v78,v79
v_accvgpr_write_b32 a116,v56
v_accvgpr_write_b32 a117,v57
v_accvgpr_write_b32 a118,v58
v_accvgpr_write_b32 a119,v59
v_pk_mul_f32 v[80:81],v[80:81],v[170:171]
v_pk_mul_f32 v[82:83],v[82:83],v[170:171]
v_pk_mul_f32 v[100:101],v[100:101],v[170:171]
v_pk_mul_f32 v[102:103],v[102:103],v[170:171]
v_cvt_pk_bf16_f32 v80,v80,v81
v_cvt_pk_bf16_f32 v81,v82,v83
v_cvt_pk_bf16_f32 v82,v100,v101
v_cvt_pk_bf16_f32 v83,v102,v103
v_accvgpr_write_b32 a120,v80
v_accvgpr_write_b32 a121,v81
buffer_load_dwordx4 v242,s[24:27],s52 offen offset:2048 lds
v_accvgpr_write_b32 a122,v82
v_accvgpr_write_b32 a123,v83
v_pk_mul_f32 v[84:85],v[84:85],v[172:173]
v_pk_mul_f32 v[86:87],v[86:87],v[172:173]
v_pk_mul_f32 v[104:105],v[104:105],v[172:173]
v_pk_mul_f32 v[106:107],v[106:107],v[172:173]
v_cvt_pk_bf16_f32 v84,v84,v85
v_cvt_pk_bf16_f32 v85,v86,v87
v_cvt_pk_bf16_f32 v86,v104,v105
v_cvt_pk_bf16_f32 v87,v106,v107
v_accvgpr_write_b32 a124,v84
v_accvgpr_write_b32 a125,v85
v_accvgpr_write_b32 a126,v86
v_accvgpr_write_b32 a127,v87
v_pk_mul_f32 v[4:5],v[4:5],v[172:173]
v_pk_mul_f32 v[6:7],v[6:7],v[172:173]
v_pk_mul_f32 v[24:25],v[24:25],v[172:173]
v_pk_mul_f32 v[26:27],v[26:27],v[172:173]
v_cvt_pk_bf16_f32 v4,v4,v5
v_cvt_pk_bf16_f32 v5,v6,v7
v_cvt_pk_bf16_f32 v6,v24,v25
v_cvt_pk_bf16_f32 v7,v26,v27
s_mov_b32 s72,1024
s_add_u32 m0,s58,27656
s_add_u32 s66,s52,196608
buffer_load_dwordx4 v240,s[24:27],s66 offen lds
buffer_store_dwordx4 v[4:7],v182,s[36:39],s72 offen sc1
v_pk_mul_f32 v[88:89],v[88:89],v[174:175]
v_pk_mul_f32 v[90:91],v[90:91],v[174:175]
v_pk_mul_f32 v[108:109],v[108:109],v[174:175]
v_pk_mul_f32 v[110:111],v[110:111],v[174:175]
v_cvt_pk_bf16_f32 v88,v88,v89
v_cvt_pk_bf16_f32 v89,v90,v91
v_cvt_pk_bf16_f32 v90,v108,v109
v_cvt_pk_bf16_f32 v91,v110,v111
v_accvgpr_write_b32 a128,v88
v_accvgpr_write_b32 a129,v89
v_accvgpr_write_b32 a130,v90
v_accvgpr_write_b32 a131,v91
v_pk_mul_f32 v[92:93],v[92:93],v[176:177]
v_pk_mul_f32 v[94:95],v[94:95],v[176:177]
v_pk_mul_f32 v[112:113],v[112:113],v[176:177]
v_pk_mul_f32 v[114:115],v[114:115],v[176:177]
v_cvt_pk_bf16_f32 v92,v92,v93
v_cvt_pk_bf16_f32 v93,v94,v95
v_cvt_pk_bf16_f32 v94,v112,v113
v_cvt_pk_bf16_f32 v95,v114,v115
v_accvgpr_write_b32 a132,v92
v_accvgpr_write_b32 a133,v93
buffer_load_dwordx4 v241,s[24:27],s66 offen offset:1024 lds
v_accvgpr_write_b32 a134,v94
v_accvgpr_write_b32 a135,v95
v_pk_mul_f32 v[96:97],v[96:97],v[178:179]
v_pk_mul_f32 v[98:99],v[98:99],v[178:179]
v_pk_mul_f32 v[116:117],v[116:117],v[178:179]
v_pk_mul_f32 v[118:119],v[118:119],v[178:179]
v_cvt_pk_bf16_f32 v96,v96,v97
v_cvt_pk_bf16_f32 v97,v98,v99
v_cvt_pk_bf16_f32 v98,v116,v117
v_cvt_pk_bf16_f32 v99,v118,v119
v_accvgpr_write_b32 a136,v96
v_accvgpr_write_b32 a137,v97
v_accvgpr_write_b32 a138,v98
v_accvgpr_write_b32 a139,v99
v_pk_mul_f32 v[120:121],v[120:121],v[170:171]
v_pk_mul_f32 v[122:123],v[122:123],v[170:171]
v_pk_mul_f32 v[140:141],v[140:141],v[170:171]
v_pk_mul_f32 v[142:143],v[142:143],v[170:171]
v_cvt_pk_bf16_f32 v120,v120,v121
v_cvt_pk_bf16_f32 v121,v122,v123
v_cvt_pk_bf16_f32 v122,v140,v141
v_cvt_pk_bf16_f32 v123,v142,v143
buffer_load_dwordx4 v242,s[24:27],s66 offen offset:2048 lds
v_accvgpr_write_b32 a140,v120
v_accvgpr_write_b32 a141,v121
v_accvgpr_write_b32 a142,v122
v_accvgpr_write_b32 a143,v123
v_pk_mul_f32 v[8:9],v[8:9],v[174:175]
v_pk_mul_f32 v[10:11],v[10:11],v[174:175]
v_pk_mul_f32 v[28:29],v[28:29],v[174:175]
v_pk_mul_f32 v[30:31],v[30:31],v[174:175]
v_cvt_pk_bf16_f32 v8,v8,v9
v_cvt_pk_bf16_f32 v9,v10,v11
v_cvt_pk_bf16_f32 v10,v28,v29
v_cvt_pk_bf16_f32 v11,v30,v31
s_mov_b32 s72,2048
buffer_store_dwordx4 v[8:11],v182,s[36:39],s72 offen sc1
v_pk_mul_f32 v[124:125],v[124:125],v[172:173]
v_pk_mul_f32 v[126:127],v[126:127],v[172:173]
v_pk_mul_f32 v[144:145],v[144:145],v[172:173]
v_pk_mul_f32 v[146:147],v[146:147],v[172:173]
v_cvt_pk_bf16_f32 v124,v124,v125
v_cvt_pk_bf16_f32 v125,v126,v127
v_cvt_pk_bf16_f32 v126,v144,v145
v_cvt_pk_bf16_f32 v127,v146,v147
v_accvgpr_write_b32 a144,v124
s_add_u32 s52,s52,393216
s_add_u32 m0,s59,0
s_nop 0
buffer_load_dwordx4 v243,s[20:23],s53 offen lds
v_accvgpr_write_b32 a145,v125
v_accvgpr_write_b32 a146,v126
v_accvgpr_write_b32 a147,v127
v_pk_mul_f32 v[128:129],v[128:129],v[174:175]
v_pk_mul_f32 v[130:131],v[130:131],v[174:175]
v_pk_mul_f32 v[148:149],v[148:149],v[174:175]
v_pk_mul_f32 v[150:151],v[150:151],v[174:175]
v_cvt_pk_bf16_f32 v128,v128,v129
v_cvt_pk_bf16_f32 v129,v130,v131
v_cvt_pk_bf16_f32 v130,v148,v149
v_cvt_pk_bf16_f32 v131,v150,v151
v_accvgpr_write_b32 a148,v128
v_accvgpr_write_b32 a149,v129
v_accvgpr_write_b32 a150,v130
v_accvgpr_write_b32 a151,v131
v_pk_mul_f32 v[132:133],v[132:133],v[176:177]
v_pk_mul_f32 v[134:135],v[134:135],v[176:177]
v_pk_mul_f32 v[152:153],v[152:153],v[176:177]
v_pk_mul_f32 v[154:155],v[154:155],v[176:177]
v_cvt_pk_bf16_f32 v132,v132,v133
v_cvt_pk_bf16_f32 v133,v134,v135
v_cvt_pk_bf16_f32 v134,v152,v153
v_cvt_pk_bf16_f32 v135,v154,v155
s_add_u32 s67,s53,9216
buffer_load_dwordx4 v243,s[20:23],s67 offen offset:1024 lds
v_accvgpr_write_b32 a152,v132
v_accvgpr_write_b32 a153,v133
v_accvgpr_write_b32 a154,v134
v_accvgpr_write_b32 a155,v135
v_pk_mul_f32 v[136:137],v[136:137],v[178:179]
v_pk_mul_f32 v[138:139],v[138:139],v[178:179]
v_pk_mul_f32 v[156:157],v[156:157],v[178:179]
v_pk_mul_f32 v[158:159],v[158:159],v[178:179]
v_cvt_pk_bf16_f32 v136,v136,v137
v_cvt_pk_bf16_f32 v137,v138,v139
v_cvt_pk_bf16_f32 v138,v156,v157
v_cvt_pk_bf16_f32 v139,v158,v159
v_accvgpr_write_b32 a156,v136
v_accvgpr_write_b32 a157,v137
v_accvgpr_write_b32 a158,v138
v_accvgpr_write_b32 a159,v139
v_pk_mul_f32 v[12:13],v[12:13],v[176:177]
v_pk_mul_f32 v[14:15],v[14:15],v[176:177]
v_pk_mul_f32 v[32:33],v[32:33],v[176:177]
v_pk_mul_f32 v[34:35],v[34:35],v[176:177]
v_cvt_pk_bf16_f32 v12,v12,v13
v_cvt_pk_bf16_f32 v13,v14,v15
s_add_u32 s67,s53,18432
buffer_load_dwordx4 v243,s[20:23],s67 offen offset:2048 lds
v_cvt_pk_bf16_f32 v14,v32,v33
v_cvt_pk_bf16_f32 v15,v34,v35
s_mov_b32 s72,3072
buffer_store_dwordx4 v[12:15],v182,s[36:39],s72 offen sc1
v_accvgpr_read_b32 v184,a0
v_accvgpr_read_b32 v185,a1
v_accvgpr_read_b32 v186,a2
v_accvgpr_read_b32 v187,a3
v_accvgpr_read_b32 v188,a20
v_accvgpr_read_b32 v189,a21
v_accvgpr_read_b32 v190,a22
v_accvgpr_read_b32 v191,a23
v_pk_mul_f32 v[184:185],v[184:185],v[170:171]
v_pk_mul_f32 v[186:187],v[186:187],v[170:171]
v_pk_mul_f32 v[188:189],v[188:189],v[170:171]
v_pk_mul_f32 v[190:191],v[190:191],v[170:171]
v_cvt_pk_bf16_f32 v184,v184,v185
v_cvt_pk_bf16_f32 v185,v186,v187
v_cvt_pk_bf16_f32 v186,v188,v189
v_cvt_pk_bf16_f32 v187,v190,v191
v_accvgpr_write_b32 a160,v184
v_accvgpr_write_b32 a161,v185
v_accvgpr_write_b32 a162,v186
s_add_u32 s67,s53,27648
buffer_load_dwordx4 v243,s[20:23],s67 offen offset:3072 lds
v_accvgpr_write_b32 a163,v187
v_accvgpr_read_b32 v192,a4
v_accvgpr_read_b32 v193,a5
v_accvgpr_read_b32 v194,a6
v_accvgpr_read_b32 v195,a7
v_accvgpr_read_b32 v196,a24
v_accvgpr_read_b32 v197,a25
v_accvgpr_read_b32 v198,a26
v_accvgpr_read_b32 v199,a27
v_pk_mul_f32 v[192:193],v[192:193],v[172:173]
v_pk_mul_f32 v[194:195],v[194:195],v[172:173]
v_pk_mul_f32 v[196:197],v[196:197],v[172:173]
v_pk_mul_f32 v[198:199],v[198:199],v[172:173]
v_cvt_pk_bf16_f32 v192,v192,v193
v_cvt_pk_bf16_f32 v193,v194,v195
v_cvt_pk_bf16_f32 v194,v196,v197
v_cvt_pk_bf16_f32 v195,v198,v199
v_accvgpr_write_b32 a164,v192
v_accvgpr_write_b32 a165,v193
v_accvgpr_write_b32 a166,v194
v_accvgpr_write_b32 a167,v195
v_accvgpr_read_b32 v200,a8
v_accvgpr_read_b32 v201,a9
s_add_u32 m0,s59,4096
s_add_u32 s67,s53,40960
buffer_load_dwordx4 v243,s[20:23],s67 offen lds
v_accvgpr_read_b32 v202,a10
v_accvgpr_read_b32 v203,a11
v_accvgpr_read_b32 v204,a28
v_accvgpr_read_b32 v205,a29
v_accvgpr_read_b32 v206,a30
v_accvgpr_read_b32 v207,a31
v_pk_mul_f32 v[200:201],v[200:201],v[174:175]
v_pk_mul_f32 v[202:203],v[202:203],v[174:175]
v_pk_mul_f32 v[204:205],v[204:205],v[174:175]
v_pk_mul_f32 v[206:207],v[206:207],v[174:175]
v_cvt_pk_bf16_f32 v200,v200,v201
v_cvt_pk_bf16_f32 v201,v202,v203
v_cvt_pk_bf16_f32 v202,v204,v205
v_cvt_pk_bf16_f32 v203,v206,v207
v_accvgpr_write_b32 a168,v200
v_accvgpr_write_b32 a169,v201
v_accvgpr_write_b32 a170,v202
v_accvgpr_write_b32 a171,v203
v_accvgpr_read_b32 v208,a12
v_accvgpr_read_b32 v209,a13
v_accvgpr_read_b32 v210,a14
v_accvgpr_read_b32 v211,a15
s_add_u32 s53,s53,128
s_add_u32 m0,s58,49168
s_nop 0
buffer_load_dwordx4 v240,s[24:27],s52 offen lds
v_accvgpr_read_b32 v212,a32
v_accvgpr_read_b32 v213,a33
v_accvgpr_read_b32 v214,a34
v_accvgpr_read_b32 v215,a35
v_pk_mul_f32 v[208:209],v[208:209],v[176:177]
v_pk_mul_f32 v[210:211],v[210:211],v[176:177]
v_pk_mul_f32 v[212:213],v[212:213],v[176:177]
v_pk_mul_f32 v[214:215],v[214:215],v[176:177]
v_cvt_pk_bf16_f32 v208,v208,v209
v_cvt_pk_bf16_f32 v209,v210,v211
v_cvt_pk_bf16_f32 v210,v212,v213
v_cvt_pk_bf16_f32 v211,v214,v215
v_accvgpr_write_b32 a172,v208
v_accvgpr_write_b32 a173,v209
v_accvgpr_write_b32 a174,v210
v_accvgpr_write_b32 a175,v211
v_pk_mul_f32 v[16:17],v[16:17],v[178:179]
v_pk_mul_f32 v[18:19],v[18:19],v[178:179]
v_pk_mul_f32 v[36:37],v[36:37],v[178:179]
v_pk_mul_f32 v[38:39],v[38:39],v[178:179]
v_cvt_pk_bf16_f32 v16,v16,v17
v_cvt_pk_bf16_f32 v17,v18,v19
v_cvt_pk_bf16_f32 v18,v36,v37
buffer_load_dwordx4 v241,s[24:27],s52 offen offset:1024 lds
v_cvt_pk_bf16_f32 v19,v38,v39
s_mov_b32 s72,4096
buffer_store_dwordx4 v[16:19],v182,s[36:39],s72 offen sc1
v_accvgpr_read_b32 v184,a16
v_accvgpr_read_b32 v185,a17
v_accvgpr_read_b32 v186,a18
v_accvgpr_read_b32 v187,a19
v_accvgpr_read_b32 v188,a36
v_accvgpr_read_b32 v189,a37
v_accvgpr_read_b32 v190,a38
v_accvgpr_read_b32 v191,a39
v_pk_mul_f32 v[184:185],v[184:185],v[178:179]
v_pk_mul_f32 v[186:187],v[186:187],v[178:179]
v_pk_mul_f32 v[188:189],v[188:189],v[178:179]
v_pk_mul_f32 v[190:191],v[190:191],v[178:179]
v_cvt_pk_bf16_f32 v184,v184,v185
v_cvt_pk_bf16_f32 v185,v186,v187
v_cvt_pk_bf16_f32 v186,v188,v189
v_cvt_pk_bf16_f32 v187,v190,v191
v_accvgpr_write_b32 a176,v184
v_accvgpr_write_b32 a177,v185
v_accvgpr_write_b32 a178,v186
v_accvgpr_write_b32 a179,v187
buffer_load_dwordx4 v242,s[24:27],s52 offen offset:2048 lds
v_accvgpr_read_b32 v192,a40
v_accvgpr_read_b32 v193,a41
v_accvgpr_read_b32 v194,a42
v_accvgpr_read_b32 v195,a43
v_accvgpr_read_b32 v196,a60
v_accvgpr_read_b32 v197,a61
v_accvgpr_read_b32 v198,a62
v_accvgpr_read_b32 v199,a63
v_pk_mul_f32 v[192:193],v[192:193],v[170:171]
v_pk_mul_f32 v[194:195],v[194:195],v[170:171]
v_pk_mul_f32 v[196:197],v[196:197],v[170:171]
v_pk_mul_f32 v[198:199],v[198:199],v[170:171]
v_cvt_pk_bf16_f32 v192,v192,v193
v_cvt_pk_bf16_f32 v193,v194,v195
v_cvt_pk_bf16_f32 v194,v196,v197
v_cvt_pk_bf16_f32 v195,v198,v199
v_accvgpr_write_b32 a180,v192
v_accvgpr_write_b32 a181,v193
v_accvgpr_write_b32 a182,v194
v_accvgpr_write_b32 a183,v195
v_accvgpr_read_b32 v200,a44
v_accvgpr_read_b32 v201,a45
s_add_u32 m0,s58,52240
s_add_u32 s66,s52,196608
buffer_load_dwordx4 v240,s[24:27],s66 offen lds
v_accvgpr_read_b32 v202,a46
v_accvgpr_read_b32 v203,a47
v_accvgpr_read_b32 v204,a64
v_accvgpr_read_b32 v205,a65
v_accvgpr_read_b32 v206,a66
v_accvgpr_read_b32 v207,a67
v_pk_mul_f32 v[200:201],v[200:201],v[172:173]
v_pk_mul_f32 v[202:203],v[202:203],v[172:173]
v_pk_mul_f32 v[204:205],v[204:205],v[172:173]
v_pk_mul_f32 v[206:207],v[206:207],v[172:173]
v_cvt_pk_bf16_f32 v200,v200,v201
v_cvt_pk_bf16_f32 v201,v202,v203
v_cvt_pk_bf16_f32 v202,v204,v205
v_cvt_pk_bf16_f32 v203,v206,v207
v_accvgpr_write_b32 a184,v200
v_accvgpr_write_b32 a185,v201
v_accvgpr_write_b32 a186,v202
v_accvgpr_write_b32 a187,v203
v_accvgpr_read_b32 v208,a48
v_accvgpr_read_b32 v209,a49
v_accvgpr_read_b32 v210,a50
v_accvgpr_read_b32 v211,a51
v_accvgpr_read_b32 v212,a68
buffer_load_dwordx4 v241,s[24:27],s66 offen offset:1024 lds
v_accvgpr_read_b32 v213,a69
v_accvgpr_read_b32 v214,a70
v_accvgpr_read_b32 v215,a71
v_pk_mul_f32 v[208:209],v[208:209],v[174:175]
v_pk_mul_f32 v[210:211],v[210:211],v[174:175]
v_pk_mul_f32 v[212:213],v[212:213],v[174:175]
v_pk_mul_f32 v[214:215],v[214:215],v[174:175]
v_cvt_pk_bf16_f32 v208,v208,v209
v_cvt_pk_bf16_f32 v209,v210,v211
v_cvt_pk_bf16_f32 v210,v212,v213
v_cvt_pk_bf16_f32 v211,v214,v215
v_accvgpr_write_b32 a188,v208
v_accvgpr_write_b32 a189,v209
v_accvgpr_write_b32 a190,v210
v_accvgpr_write_b32 a191,v211
v_pk_mul_f32 v[40:41],v[40:41],v[170:171]
v_pk_mul_f32 v[42:43],v[42:43],v[170:171]
v_pk_mul_f32 v[60:61],v[60:61],v[170:171]
v_pk_mul_f32 v[62:63],v[62:63],v[170:171]
v_cvt_pk_bf16_f32 v40,v40,v41
v_cvt_pk_bf16_f32 v41,v42,v43
v_cvt_pk_bf16_f32 v42,v60,v61
v_cvt_pk_bf16_f32 v43,v62,v63
buffer_load_dwordx4 v242,s[24:27],s66 offen offset:2048 lds
s_add_u32 s52,s52,393216
s_mov_b32 s72,10240
buffer_store_dwordx4 v[40:43],v182,s[36:39],s72 offen sc1
v_accvgpr_read_b32 v184,a52
v_accvgpr_read_b32 v185,a53
v_accvgpr_read_b32 v186,a54
v_accvgpr_read_b32 v187,a55
v_accvgpr_read_b32 v188,a72
v_accvgpr_read_b32 v189,a73
v_accvgpr_read_b32 v190,a74
v_accvgpr_read_b32 v191,a75
v_pk_mul_f32 v[184:185],v[184:185],v[176:177]
v_pk_mul_f32 v[186:187],v[186:187],v[176:177]
v_pk_mul_f32 v[188:189],v[188:189],v[176:177]
v_pk_mul_f32 v[190:191],v[190:191],v[176:177]
v_cvt_pk_bf16_f32 v184,v184,v185
v_cvt_pk_bf16_f32 v185,v186,v187
v_cvt_pk_bf16_f32 v186,v188,v189
v_cvt_pk_bf16_f32 v187,v190,v191
v_accvgpr_write_b32 a192,v184
v_accvgpr_write_b32 a193,v185
v_accvgpr_write_b32 a194,v186
v_accvgpr_write_b32 a195,v187
v_accvgpr_read_b32 v192,a56
v_accvgpr_read_b32 v193,a57
v_accvgpr_read_b32 v194,a58
v_accvgpr_read_b32 v195,a59
v_accvgpr_read_b32 v196,a76
v_accvgpr_read_b32 v197,a77
v_accvgpr_read_b32 v198,a78
v_accvgpr_read_b32 v199,a79
v_pk_mul_f32 v[192:193],v[192:193],v[178:179]
v_pk_mul_f32 v[194:195],v[194:195],v[178:179]
v_pk_mul_f32 v[196:197],v[196:197],v[178:179]
v_pk_mul_f32 v[198:199],v[198:199],v[178:179]
v_cvt_pk_bf16_f32 v192,v192,v193
v_cvt_pk_bf16_f32 v193,v194,v195
v_cvt_pk_bf16_f32 v194,v196,v197
v_cvt_pk_bf16_f32 v195,v198,v199
v_accvgpr_write_b32 a196,v192
v_accvgpr_write_b32 a197,v193
v_accvgpr_write_b32 a198,v194
v_accvgpr_write_b32 a199,v195
s_branch LQM_%=
LTOO_%=:
s_lshl_b32 s35,s50,1
s_mov_b32 s51,4
s_mov_b32 s71,s50
s_cmp_eq_u32 s71,0
s_cselect_b64 s[24:25],s[84:85],s[94:95]
s_mul_i32 s64,s33,1966080
s_mul_i32 s65,s34,640
s_add_u32 s64,s64,s65
s_add_u32 s24,s24,s64
s_addc_u32 s25,s25,0
s_sub_u32 s24,s24,3072
s_subb_u32 s25,s25,0
s_mov_b32 s26,1968512
s_lshl_b32 s64,s71,9
s_lshl_b32 s65,s48,5
s_add_u32 s64,s64,s65
s_lshl_b32 s65,s33,3
s_add_u32 s64,s64,s65
s_mul_i32 s64,s64,61440
s_add_u32 s64,s64,65536
s_add_u32 s20,s98,s64
s_addc_u32 s21,s99,0
s_mov_b32 s22,491520
s_lshl_b32 s64,s50,8
s_lshl_b32 s65,s48,4
s_add_u32 s64,s64,s65
s_lshl_b32 s65,s33,2
s_add_u32 s64,s64,s65
s_mul_i32 s65,s50,0
s_add_u32 s64,s64,s65
s_lshl_b32 s64,s64,6
s_add_u32 s12,s98,s64
s_addc_u32 s13,s99,0
s_mov_b32 s14,196
s_and_b32 s64,s35,3
s_mul_i32 s52,s64,491520
s_mul_i32 s65,s16,10240
s_add_u32 s52,s52,s65
s_mul_i32 s53,s64,122880
s_mul_i32 s65,s16,5120
s_add_u32 s53,s53,s65
s_mul_i32 s58,s16,5120
s_add_u32 s59,s58,81920
v_mbcnt_lo_u32_b32 v254,-1,0
v_mbcnt_hi_u32_b32 v254,-1,v254
v_add_u32 v240,0,v254
v_mul_u32_u24 v255,1639,v240
v_lshrrev_b32 v255,16,v255
v_mul_u32_u24 v245,40,v255
v_sub_u32 v240,v240,v245
v_and_b32 v245,6,v255
v_xor_b32 v240,v240,v245
v_lshlrev_b32 v240,4,v240
v_mul_u32_u24 v255,1280,v255
v_add_u32 v240,v240,v255
v_add_u32 v240,3072,v240
v_add_u32 v241,64,v254
v_mul_u32_u24 v255,1639,v241
v_lshrrev_b32 v255,16,v255
v_mul_u32_u24 v245,40,v255
v_sub_u32 v241,v241,v245
v_and_b32 v245,6,v255
v_xor_b32 v241,v241,v245
v_lshlrev_b32 v241,4,v241
v_mul_u32_u24 v255,1280,v255
v_add_u32 v241,v241,v255
v_add_u32 v241,2048,v241
v_add_u32 v242,128,v254
v_mul_u32_u24 v255,1639,v242
v_lshrrev_b32 v255,16,v255
v_mul_u32_u24 v245,40,v255
v_sub_u32 v242,v242,v245
v_and_b32 v245,6,v255
v_xor_b32 v242,v242,v245
v_lshlrev_b32 v242,4,v242
v_mul_u32_u24 v255,1280,v255
v_add_u32 v242,v242,v255
v_add_u32 v242,1024,v242
v_add_u32 v243,192,v254
v_mul_u32_u24 v255,1639,v243
v_lshrrev_b32 v255,16,v255
v_mul_u32_u24 v245,40,v255
v_sub_u32 v243,v243,v245
v_and_b32 v245,6,v255
v_xor_b32 v243,v243,v245
v_lshlrev_b32 v243,4,v243
v_mul_u32_u24 v255,1280,v255
v_add_u32 v243,v243,v255
v_add_u32 v243,0,v243
v_add_u32 v244,256,v254
v_mul_u32_u24 v255,1639,v244
v_lshrrev_b32 v255,16,v255
v_mul_u32_u24 v245,40,v255
v_sub_u32 v244,v244,v245
v_and_b32 v245,6,v255
v_xor_b32 v244,v244,v245
v_lshlrev_b32 v244,4,v244
v_mul_u32_u24 v255,1280,v255
v_add_u32 v244,v244,v255
v_add_u32 v244,3072,v244
v_lshlrev_b32 v245,4,v254
s_cmp_eq_u32 s50,0
s_cselect_b64 s[28:29],s[84:85],s[94:95]
s_mul_i32 s65,s33,1966080
s_add_u32 s28,s28,s65
s_addc_u32 s29,s29,0
s_mov_b32 s30,1966080
s_and_b32 s64,s48,7
s_lshl_b32 s64,s64,1
s_add_u32 s64,s64,s34
s_lshl_b32 s64,s64,2
s_add_u32 s64,s64,s16
s_mul_i32 s54,s64,1280
s_mul_i32 s65,s50,983040
s_add_u32 s54,s54,s65
s_add_u32 s54,s54,573440
s_cmp_eq_u32 s73,0
s_cbranch_scc0 LPUBD_%=
s_lshl_b32 s69,s50,7
v_mbcnt_lo_u32_b32 v254,-1,0
v_mbcnt_hi_u32_b32 v254,-1,v254
v_lshlrev_b32 v254,4,v254
LPRE_%=:
s_mov_b32 s71,65536
LPRE_%=L:
s_sleep 2
buffer_load_dword v255,v254,s[12:15],s69 offen sc1
s_waitcnt vmcnt(0)
v_readfirstlane_b32 s70,v255
s_sub_u32 s71,s71,1
s_cmp_eq_u32 s71,0
s_cbranch_scc1 LPRE_%=X
s_cmp_eq_u32 s70,0
s_cbranch_scc1 LPRE_%=L
LPRE_%=X:
LPUBD_%=:
v_mfma_f32_16x16x32_bf16 v[0:3],a[200:203],v[200:203],v[0:3]
s_add_u32 m0,s58,0
v_mov_b32 v160,v246
v_mfma_f32_16x16x32_bf16 v[4:7],a[200:203],v[204:207],v[4:7]
buffer_load_dwordx4 v240,s[24:27],s52 offen lds
v_mfma_f32_16x16x32_bf16 v[8:11],a[200:203],v[208:211],v[8:11]
s_add_u32 m0,s58,0
v_permlane32_swap_b32 v160,v246
v_mov_b32 v161,v247
v_mfma_f32_16x16x32_bf16 v[12:15],a[200:203],v[212:215],v[12:15]
v_add_f32 v246,v246,v160
v_mov_b32 v162,v248
v_mfma_f32_16x16x32_bf16 v[16:19],a[200:203],v[216:219],v[16:19]
v_mov_b32 v160,v246
v_permlane32_swap_b32 v161,v247
v_mfma_f32_16x16x32_bf16 v[20:23],a[204:207],v[200:203],v[20:23]
v_add_f32 v247,v247,v161
v_permlane32_swap_b32 v162,v248
v_mfma_f32_16x16x32_bf16 v[24:27],a[204:207],v[204:207],v[24:27]
buffer_load_dwordx4 v241,s[24:27],s52 offen offset:1024 lds
v_mfma_f32_16x16x32_bf16 v[28:31],a[204:207],v[208:211],v[28:31]
s_add_u32 m0,s58,0
v_permlane16_swap_b32 v160,v246
v_mov_b32 v161,v247
v_mfma_f32_16x16x32_bf16 v[32:35],a[204:207],v[212:215],v[32:35]
v_add_f32 v246,v246,v160
v_add_f32 v248,v248,v162
v_mfma_f32_16x16x32_bf16 v[36:39],a[204:207],v[216:219],v[36:39]
v_rcp_f32 v170,v246
v_mfma_f32_16x16x32_bf16 v[40:43],a[208:211],v[200:203],v[40:43]
v_mov_b32 v171,v170
v_permlane16_swap_b32 v161,v247
v_mfma_f32_16x16x32_bf16 v[44:47],a[208:211],v[204:207],v[44:47]
buffer_load_dwordx4 v242,s[24:27],s52 offen offset:2048 lds
v_mfma_f32_16x16x32_bf16 v[48:51],a[208:211],v[208:211],v[48:51]
s_add_u32 m0,s58,0
v_add_f32 v247,v247,v161
v_mov_b32 v162,v248
v_mfma_f32_16x16x32_bf16 v[52:55],a[208:211],v[212:215],v[52:55]
v_rcp_f32 v172,v247
v_mfma_f32_16x16x32_bf16 v[56:59],a[208:211],v[216:219],v[56:59]
v_mov_b32 v173,v172
v_permlane16_swap_b32 v162,v248
v_mfma_f32_16x16x32_bf16 v[60:63],a[212:215],v[200:203],v[60:63]
v_add_f32 v248,v248,v162
v_mov_b32 v163,v249
v_mfma_f32_16x16x32_bf16 v[64:67],a[212:215],v[204:207],v[64:67]
buffer_load_dwordx4 v243,s[24:27],s52 offen offset:3072 lds
v_mfma_f32_16x16x32_bf16 v[68:71],a[212:215],v[208:211],v[68:71]
s_add_u32 m0,s58,4096
v_rcp_f32 v174,v248
v_mfma_f32_16x16x32_bf16 v[72:75],a[212:215],v[212:215],v[72:75]
v_mov_b32 v175,v174
v_permlane32_swap_b32 v163,v249
v_mfma_f32_16x16x32_bf16 v[76:79],a[212:215],v[216:219],v[76:79]
v_add_f32 v249,v249,v163
v_mov_b32 v164,v253
v_mfma_f32_16x16x32_bf16 v[80:83],a[216:219],v[200:203],v[80:83]
v_mov_b32 v163,v249
v_mfma_f32_16x16x32_bf16 v[84:87],a[216:219],v[204:207],v[84:87]
buffer_load_dwordx4 v244,s[24:27],s52 offen lds
v_mfma_f32_16x16x32_bf16 v[88:91],a[216:219],v[208:211],v[88:91]
s_add_u32 s52,s52,40960
v_permlane16_swap_b32 v163,v249
v_permlane32_swap_b32 v164,v253
v_mfma_f32_16x16x32_bf16 v[92:95],a[216:219],v[212:215],v[92:95]
s_add_u32 m0,s59,0
v_add_f32 v249,v249,v163
v_add_f32 v253,v253,v164
v_mfma_f32_16x16x32_bf16 v[96:99],a[216:219],v[216:219],v[96:99]
v_rcp_f32 v176,v249
v_mfma_f32_16x16x32_bf16 v[100:103],a[220:223],v[200:203],v[100:103]
v_mov_b32 v177,v176
v_mov_b32 v164,v253
v_mfma_f32_16x16x32_bf16 v[104:107],a[220:223],v[204:207],v[104:107]
buffer_load_dwordx4 v245,s[20:23],s53 offen sc1 lds
v_mfma_f32_16x16x32_bf16 v[108:111],a[220:223],v[208:211],v[108:111]
s_add_u32 m0,s59,0
v_permlane16_swap_b32 v164,v253
v_mfma_f32_16x16x32_bf16 v[112:115],a[220:223],v[212:215],v[112:115]
v_add_f32 v253,v253,v164
v_mfma_f32_16x16x32_bf16 v[116:119],a[220:223],v[216:219],v[116:119]
v_rcp_f32 v178,v253
v_mfma_f32_16x16x32_bf16 v[120:123],a[224:227],v[200:203],v[120:123]
v_mov_b32 v179,v178
v_mfma_f32_16x16x32_bf16 v[124:127],a[224:227],v[204:207],v[124:127]
buffer_load_dwordx4 v245,s[20:23],s53 offen offset:1024 sc1 lds
v_mfma_f32_16x16x32_bf16 v[128:131],a[224:227],v[208:211],v[128:131]
s_add_u32 m0,s59,0
v_mfma_f32_16x16x32_bf16 v[132:135],a[224:227],v[212:215],v[132:135]
v_mfma_f32_16x16x32_bf16 v[136:139],a[224:227],v[216:219],v[136:139]
v_mfma_f32_16x16x32_bf16 v[140:143],a[228:231],v[200:203],v[140:143]
v_mfma_f32_16x16x32_bf16 v[144:147],a[228:231],v[204:207],v[144:147]
buffer_load_dwordx4 v245,s[20:23],s53 offen offset:2048 sc1 lds
v_mfma_f32_16x16x32_bf16 v[148:151],a[228:231],v[208:211],v[148:151]
s_add_u32 m0,s59,0
v_mfma_f32_16x16x32_bf16 v[152:155],a[228:231],v[212:215],v[152:155]
v_mfma_f32_16x16x32_bf16 v[156:159],a[228:231],v[216:219],v[156:159]
v_mfma_f32_16x16x32_bf16 a[0:3],a[232:235],v[200:203],a[0:3]
v_mfma_f32_16x16x32_bf16 a[4:7],a[232:235],v[204:207],a[4:7]
buffer_load_dwordx4 v245,s[20:23],s53 offen offset:3072 sc1 lds
v_mfma_f32_16x16x32_bf16 a[8:11],a[232:235],v[208:211],a[8:11]
s_add_u32 m0,s59,4096
v_mfma_f32_16x16x32_bf16 a[12:15],a[232:235],v[212:215],a[12:15]
s_add_u32 s67,s53,4096
v_mfma_f32_16x16x32_bf16 a[16:19],a[232:235],v[216:219],a[16:19]
v_mfma_f32_16x16x32_bf16 a[20:23],a[236:239],v[200:203],a[20:23]
v_mfma_f32_16x16x32_bf16 a[24:27],a[236:239],v[204:207],a[24:27]
buffer_load_dwordx4 v245,s[20:23],s67 offen sc1 lds
v_mfma_f32_16x16x32_bf16 a[28:31],a[236:239],v[208:211],a[28:31]
s_add_u32 s53,s53,20480
v_mfma_f32_16x16x32_bf16 a[32:35],a[236:239],v[212:215],a[32:35]
s_add_u32 m0,s58,20480
v_mfma_f32_16x16x32_bf16 a[36:39],a[236:239],v[216:219],a[36:39]
v_mfma_f32_16x16x32_bf16 a[40:43],a[240:243],v[200:203],a[40:43]
v_mfma_f32_16x16x32_bf16 a[44:47],a[240:243],v[204:207],a[44:47]
buffer_load_dwordx4 v240,s[24:27],s52 offen lds
v_mfma_f32_16x16x32_bf16 a[48:51],a[240:243],v[208:211],a[48:51]
v_mfma_f32_16x16x32_bf16 a[52:55],a[240:243],v[212:215],a[52:55]
v_mfma_f32_16x16x32_bf16 a[56:59],a[240:243],v[216:219],a[56:59]
v_mfma_f32_16x16x32_bf16 a[60:63],a[244:247],v[200:203],a[60:63]
v_mfma_f32_16x16x32_bf16 a[64:67],a[244:247],v[204:207],a[64:67]
v_mfma_f32_16x16x32_bf16 a[68:71],a[244:247],v[208:211],a[68:71]
v_mfma_f32_16x16x32_bf16 a[72:75],a[244:247],v[212:215],a[72:75]
v_mfma_f32_16x16x32_bf16 a[76:79],a[244:247],v[216:219],a[76:79]
v_mbcnt_lo_u32_b32 v182,-1,0
v_mbcnt_hi_u32_b32 v182,-1,v182
v_lshlrev_b32 v182,4,v182
v_pk_mul_f32 v[44:45],v[44:45],v[172:173]
v_pk_mul_f32 v[46:47],v[46:47],v[172:173]
v_pk_mul_f32 v[64:65],v[64:65],v[172:173]
v_pk_mul_f32 v[66:67],v[66:67],v[172:173]
v_cvt_pk_bf16_f32 v44,v44,v45
v_cvt_pk_bf16_f32 v45,v46,v47
v_cvt_pk_bf16_f32 v46,v64,v65
v_cvt_pk_bf16_f32 v47,v66,v67
v_accvgpr_write_b32 a104,v44
v_accvgpr_write_b32 a105,v45
v_accvgpr_write_b32 a106,v46
v_accvgpr_write_b32 a107,v47
v_pk_mul_f32 v[48:49],v[48:49],v[174:175]
v_pk_mul_f32 v[50:51],v[50:51],v[174:175]
v_pk_mul_f32 v[68:69],v[68:69],v[174:175]
v_pk_mul_f32 v[70:71],v[70:71],v[174:175]
v_cvt_pk_bf16_f32 v48,v48,v49
v_cvt_pk_bf16_f32 v49,v50,v51
v_cvt_pk_bf16_f32 v50,v68,v69
v_cvt_pk_bf16_f32 v51,v70,v71
v_accvgpr_write_b32 a108,v48
v_accvgpr_write_b32 a109,v49
v_accvgpr_write_b32 a110,v50
v_accvgpr_write_b32 a111,v51
v_pk_mul_f32 v[0:1],v[0:1],v[170:171]
v_pk_mul_f32 v[2:3],v[2:3],v[170:171]
v_pk_mul_f32 v[20:21],v[20:21],v[170:171]
v_pk_mul_f32 v[22:23],v[22:23],v[170:171]
s_add_u32 m0,s58,20480
s_nop 0
buffer_load_dwordx4 v241,s[24:27],s52 offen offset:1024 lds
v_cvt_pk_bf16_f32 v0,v0,v1
v_cvt_pk_bf16_f32 v1,v2,v3
v_cvt_pk_bf16_f32 v2,v20,v21
v_cvt_pk_bf16_f32 v3,v22,v23
s_mov_b32 s72,0
buffer_store_dwordx4 v[0:3],v182,s[36:39],s72 offen sc1
v_pk_mul_f32 v[52:53],v[52:53],v[176:177]
v_pk_mul_f32 v[54:55],v[54:55],v[176:177]
v_pk_mul_f32 v[72:73],v[72:73],v[176:177]
v_pk_mul_f32 v[74:75],v[74:75],v[176:177]
v_cvt_pk_bf16_f32 v52,v52,v53
v_cvt_pk_bf16_f32 v53,v54,v55
v_cvt_pk_bf16_f32 v54,v72,v73
v_cvt_pk_bf16_f32 v55,v74,v75
v_accvgpr_write_b32 a112,v52
v_accvgpr_write_b32 a113,v53
v_accvgpr_write_b32 a114,v54
v_accvgpr_write_b32 a115,v55
v_pk_mul_f32 v[56:57],v[56:57],v[178:179]
v_pk_mul_f32 v[58:59],v[58:59],v[178:179]
v_pk_mul_f32 v[76:77],v[76:77],v[178:179]
v_pk_mul_f32 v[78:79],v[78:79],v[178:179]
v_cvt_pk_bf16_f32 v56,v56,v57
v_cvt_pk_bf16_f32 v57,v58,v59
v_cvt_pk_bf16_f32 v58,v76,v77
v_cvt_pk_bf16_f32 v59,v78,v79
v_accvgpr_write_b32 a116,v56
v_accvgpr_write_b32 a117,v57
s_add_u32 m0,s58,20480
s_nop 0
buffer_load_dwordx4 v242,s[24:27],s52 offen offset:2048 lds
v_accvgpr_write_b32 a118,v58
v_accvgpr_write_b32 a119,v59
v_pk_mul_f32 v[80:81],v[80:81],v[170:171]
v_pk_mul_f32 v[82:83],v[82:83],v[170:171]
v_pk_mul_f32 v[100:101],v[100:101],v[170:171]
v_pk_mul_f32 v[102:103],v[102:103],v[170:171]
v_cvt_pk_bf16_f32 v80,v80,v81
v_cvt_pk_bf16_f32 v81,v82,v83
v_cvt_pk_bf16_f32 v82,v100,v101
v_cvt_pk_bf16_f32 v83,v102,v103
v_accvgpr_write_b32 a120,v80
v_accvgpr_write_b32 a121,v81
v_accvgpr_write_b32 a122,v82
v_accvgpr_write_b32 a123,v83
v_pk_mul_f32 v[84:85],v[84:85],v[172:173]
v_pk_mul_f32 v[86:87],v[86:87],v[172:173]
v_pk_mul_f32 v[104:105],v[104:105],v[172:173]
v_pk_mul_f32 v[106:107],v[106:107],v[172:173]
v_cvt_pk_bf16_f32 v84,v84,v85
v_cvt_pk_bf16_f32 v85,v86,v87
v_cvt_pk_bf16_f32 v86,v104,v105
v_cvt_pk_bf16_f32 v87,v106,v107
v_accvgpr_write_b32 a124,v84
v_accvgpr_write_b32 a125,v85
v_accvgpr_write_b32 a126,v86
v_accvgpr_write_b32 a127,v87
v_pk_mul_f32 v[4:5],v[4:5],v[172:173]
s_add_u32 m0,s58,20480
s_nop 0
buffer_load_dwordx4 v243,s[24:27],s52 offen offset:3072 lds
v_pk_mul_f32 v[6:7],v[6:7],v[172:173]
v_pk_mul_f32 v[24:25],v[24:25],v[172:173]
v_pk_mul_f32 v[26:27],v[26:27],v[172:173]
v_cvt_pk_bf16_f32 v4,v4,v5
v_cvt_pk_bf16_f32 v5,v6,v7
v_cvt_pk_bf16_f32 v6,v24,v25
v_cvt_pk_bf16_f32 v7,v26,v27
s_mov_b32 s72,1024
buffer_store_dwordx4 v[4:7],v182,s[36:39],s72 offen sc1
v_pk_mul_f32 v[88:89],v[88:89],v[174:175]
v_pk_mul_f32 v[90:91],v[90:91],v[174:175]
v_pk_mul_f32 v[108:109],v[108:109],v[174:175]
v_pk_mul_f32 v[110:111],v[110:111],v[174:175]
v_cvt_pk_bf16_f32 v88,v88,v89
v_cvt_pk_bf16_f32 v89,v90,v91
v_cvt_pk_bf16_f32 v90,v108,v109
v_cvt_pk_bf16_f32 v91,v110,v111
v_accvgpr_write_b32 a128,v88
v_accvgpr_write_b32 a129,v89
v_accvgpr_write_b32 a130,v90
v_accvgpr_write_b32 a131,v91
v_pk_mul_f32 v[92:93],v[92:93],v[176:177]
v_pk_mul_f32 v[94:95],v[94:95],v[176:177]
v_pk_mul_f32 v[112:113],v[112:113],v[176:177]
v_pk_mul_f32 v[114:115],v[114:115],v[176:177]
v_cvt_pk_bf16_f32 v92,v92,v93
v_cvt_pk_bf16_f32 v93,v94,v95
v_cvt_pk_bf16_f32 v94,v112,v113
s_add_u32 m0,s58,24576
s_nop 0
buffer_load_dwordx4 v244,s[24:27],s52 offen lds
v_cvt_pk_bf16_f32 v95,v114,v115
v_accvgpr_write_b32 a132,v92
v_accvgpr_write_b32 a133,v93
v_accvgpr_write_b32 a134,v94
v_accvgpr_write_b32 a135,v95
v_pk_mul_f32 v[96:97],v[96:97],v[178:179]
v_pk_mul_f32 v[98:99],v[98:99],v[178:179]
v_pk_mul_f32 v[116:117],v[116:117],v[178:179]
v_pk_mul_f32 v[118:119],v[118:119],v[178:179]
v_cvt_pk_bf16_f32 v96,v96,v97
v_cvt_pk_bf16_f32 v97,v98,v99
v_cvt_pk_bf16_f32 v98,v116,v117
v_cvt_pk_bf16_f32 v99,v118,v119
v_accvgpr_write_b32 a136,v96
v_accvgpr_write_b32 a137,v97
v_accvgpr_write_b32 a138,v98
v_accvgpr_write_b32 a139,v99
v_pk_mul_f32 v[120:121],v[120:121],v[170:171]
v_pk_mul_f32 v[122:123],v[122:123],v[170:171]
v_pk_mul_f32 v[140:141],v[140:141],v[170:171]
v_pk_mul_f32 v[142:143],v[142:143],v[170:171]
v_cvt_pk_bf16_f32 v120,v120,v121
v_cvt_pk_bf16_f32 v121,v122,v123
v_cvt_pk_bf16_f32 v122,v140,v141
v_cvt_pk_bf16_f32 v123,v142,v143
v_accvgpr_write_b32 a140,v120
v_accvgpr_write_b32 a141,v121
s_add_u32 s52,s52,40960
s_add_u32 m0,s59,20480
s_nop 0
buffer_load_dwordx4 v245,s[20:23],s53 offen sc1 lds
v_accvgpr_write_b32 a142,v122
v_accvgpr_write_b32 a143,v123
v_pk_mul_f32 v[8:9],v[8:9],v[174:175]
v_pk_mul_f32 v[10:11],v[10:11],v[174:175]
v_pk_mul_f32 v[28:29],v[28:29],v[174:175]
v_pk_mul_f32 v[30:31],v[30:31],v[174:175]
v_cvt_pk_bf16_f32 v8,v8,v9
v_cvt_pk_bf16_f32 v9,v10,v11
v_cvt_pk_bf16_f32 v10,v28,v29
v_cvt_pk_bf16_f32 v11,v30,v31
s_mov_b32 s72,2048
buffer_store_dwordx4 v[8:11],v182,s[36:39],s72 offen sc1
v_pk_mul_f32 v[124:125],v[124:125],v[172:173]
v_pk_mul_f32 v[126:127],v[126:127],v[172:173]
v_pk_mul_f32 v[144:145],v[144:145],v[172:173]
v_pk_mul_f32 v[146:147],v[146:147],v[172:173]
v_cvt_pk_bf16_f32 v124,v124,v125
v_cvt_pk_bf16_f32 v125,v126,v127
v_cvt_pk_bf16_f32 v126,v144,v145
v_cvt_pk_bf16_f32 v127,v146,v147
v_accvgpr_write_b32 a144,v124
v_accvgpr_write_b32 a145,v125
v_accvgpr_write_b32 a146,v126
v_accvgpr_write_b32 a147,v127
v_pk_mul_f32 v[128:129],v[128:129],v[174:175]
v_pk_mul_f32 v[130:131],v[130:131],v[174:175]
v_pk_mul_f32 v[148:149],v[148:149],v[174:175]
v_pk_mul_f32 v[150:151],v[150:151],v[174:175]
s_add_u32 m0,s59,20480
s_nop 0
buffer_load_dwordx4 v245,s[20:23],s53 offen offset:1024 sc1 lds
v_cvt_pk_bf16_f32 v128,v128,v129
v_cvt_pk_bf16_f32 v129,v130,v131
v_cvt_pk_bf16_f32 v130,v148,v149
v_cvt_pk_bf16_f32 v131,v150,v151
v_accvgpr_write_b32 a148,v128
v_accvgpr_write_b32 a149,v129
v_accvgpr_write_b32 a150,v130
v_accvgpr_write_b32 a151,v131
v_pk_mul_f32 v[132:133],v[132:133],v[176:177]
v_pk_mul_f32 v[134:135],v[134:135],v[176:177]
v_pk_mul_f32 v[152:153],v[152:153],v[176:177]
v_pk_mul_f32 v[154:155],v[154:155],v[176:177]
v_cvt_pk_bf16_f32 v132,v132,v133
v_cvt_pk_bf16_f32 v133,v134,v135
v_cvt_pk_bf16_f32 v134,v152,v153
v_cvt_pk_bf16_f32 v135,v154,v155
v_accvgpr_write_b32 a152,v132
v_accvgpr_write_b32 a153,v133
v_accvgpr_write_b32 a154,v134
v_accvgpr_write_b32 a155,v135
v_pk_mul_f32 v[136:137],v[136:137],v[178:179]
v_pk_mul_f32 v[138:139],v[138:139],v[178:179]
v_pk_mul_f32 v[156:157],v[156:157],v[178:179]
v_pk_mul_f32 v[158:159],v[158:159],v[178:179]
v_cvt_pk_bf16_f32 v136,v136,v137
v_cvt_pk_bf16_f32 v137,v138,v139
v_cvt_pk_bf16_f32 v138,v156,v157
s_add_u32 m0,s59,20480
s_nop 0
buffer_load_dwordx4 v245,s[20:23],s53 offen offset:2048 sc1 lds
v_cvt_pk_bf16_f32 v139,v158,v159
v_accvgpr_write_b32 a156,v136
v_accvgpr_write_b32 a157,v137
v_accvgpr_write_b32 a158,v138
v_accvgpr_write_b32 a159,v139
v_pk_mul_f32 v[12:13],v[12:13],v[176:177]
v_pk_mul_f32 v[14:15],v[14:15],v[176:177]
v_pk_mul_f32 v[32:33],v[32:33],v[176:177]
v_pk_mul_f32 v[34:35],v[34:35],v[176:177]
v_cvt_pk_bf16_f32 v12,v12,v13
v_cvt_pk_bf16_f32 v13,v14,v15
v_cvt_pk_bf16_f32 v14,v32,v33
v_cvt_pk_bf16_f32 v15,v34,v35
s_mov_b32 s72,3072
buffer_store_dwordx4 v[12:15],v182,s[36:39],s72 offen sc1
v_accvgpr_read_b32 v184,a0
v_accvgpr_read_b32 v185,a1
v_accvgpr_read_b32 v186,a2
v_accvgpr_read_b32 v187,a3
v_accvgpr_read_b32 v188,a20
v_accvgpr_read_b32 v189,a21
v_accvgpr_read_b32 v190,a22
v_accvgpr_read_b32 v191,a23
v_pk_mul_f32 v[184:185],v[184:185],v[170:171]
v_pk_mul_f32 v[186:187],v[186:187],v[170:171]
v_pk_mul_f32 v[188:189],v[188:189],v[170:171]
v_pk_mul_f32 v[190:191],v[190:191],v[170:171]
v_cvt_pk_bf16_f32 v184,v184,v185
s_add_u32 m0,s59,20480
s_nop 0
buffer_load_dwordx4 v245,s[20:23],s53 offen offset:3072 sc1 lds
v_cvt_pk_bf16_f32 v185,v186,v187
v_cvt_pk_bf16_f32 v186,v188,v189
v_cvt_pk_bf16_f32 v187,v190,v191
v_accvgpr_write_b32 a160,v184
v_accvgpr_write_b32 a161,v185
v_accvgpr_write_b32 a162,v186
v_accvgpr_write_b32 a163,v187
v_accvgpr_read_b32 v192,a4
v_accvgpr_read_b32 v193,a5
v_accvgpr_read_b32 v194,a6
v_accvgpr_read_b32 v195,a7
v_accvgpr_read_b32 v196,a24
v_accvgpr_read_b32 v197,a25
v_accvgpr_read_b32 v198,a26
v_accvgpr_read_b32 v199,a27
v_pk_mul_f32 v[192:193],v[192:193],v[172:173]
v_pk_mul_f32 v[194:195],v[194:195],v[172:173]
v_pk_mul_f32 v[196:197],v[196:197],v[172:173]
v_pk_mul_f32 v[198:199],v[198:199],v[172:173]
v_cvt_pk_bf16_f32 v192,v192,v193
v_cvt_pk_bf16_f32 v193,v194,v195
v_cvt_pk_bf16_f32 v194,v196,v197
v_cvt_pk_bf16_f32 v195,v198,v199
v_accvgpr_write_b32 a164,v192
v_accvgpr_write_b32 a165,v193
v_accvgpr_write_b32 a166,v194
v_accvgpr_write_b32 a167,v195
s_add_u32 m0,s59,24576
s_add_u32 s67,s53,4096
buffer_load_dwordx4 v245,s[20:23],s67 offen sc1 lds
v_accvgpr_read_b32 v200,a8
v_accvgpr_read_b32 v201,a9
v_accvgpr_read_b32 v202,a10
v_accvgpr_read_b32 v203,a11
v_accvgpr_read_b32 v204,a28
v_accvgpr_read_b32 v205,a29
v_accvgpr_read_b32 v206,a30
v_accvgpr_read_b32 v207,a31
v_pk_mul_f32 v[200:201],v[200:201],v[174:175]
v_pk_mul_f32 v[202:203],v[202:203],v[174:175]
v_pk_mul_f32 v[204:205],v[204:205],v[174:175]
v_pk_mul_f32 v[206:207],v[206:207],v[174:175]
v_cvt_pk_bf16_f32 v200,v200,v201
v_cvt_pk_bf16_f32 v201,v202,v203
v_cvt_pk_bf16_f32 v202,v204,v205
v_cvt_pk_bf16_f32 v203,v206,v207
v_accvgpr_write_b32 a168,v200
v_accvgpr_write_b32 a169,v201
v_accvgpr_write_b32 a170,v202
v_accvgpr_write_b32 a171,v203
v_accvgpr_read_b32 v208,a12
v_accvgpr_read_b32 v209,a13
v_accvgpr_read_b32 v210,a14
v_accvgpr_read_b32 v211,a15
v_accvgpr_read_b32 v212,a32
v_accvgpr_read_b32 v213,a33
v_accvgpr_read_b32 v214,a34
v_accvgpr_read_b32 v215,a35
s_add_u32 s53,s53,20480
s_add_u32 m0,s58,40960
s_nop 0
buffer_load_dwordx4 v240,s[24:27],s52 offen lds
v_pk_mul_f32 v[208:209],v[208:209],v[176:177]
v_pk_mul_f32 v[210:211],v[210:211],v[176:177]
v_pk_mul_f32 v[212:213],v[212:213],v[176:177]
v_pk_mul_f32 v[214:215],v[214:215],v[176:177]
v_cvt_pk_bf16_f32 v208,v208,v209
v_cvt_pk_bf16_f32 v209,v210,v211
v_cvt_pk_bf16_f32 v210,v212,v213
v_cvt_pk_bf16_f32 v211,v214,v215
v_accvgpr_write_b32 a172,v208
v_accvgpr_write_b32 a173,v209
v_accvgpr_write_b32 a174,v210
v_accvgpr_write_b32 a175,v211
v_pk_mul_f32 v[16:17],v[16:17],v[178:179]
v_pk_mul_f32 v[18:19],v[18:19],v[178:179]
v_pk_mul_f32 v[36:37],v[36:37],v[178:179]
v_pk_mul_f32 v[38:39],v[38:39],v[178:179]
v_cvt_pk_bf16_f32 v16,v16,v17
v_cvt_pk_bf16_f32 v17,v18,v19
v_cvt_pk_bf16_f32 v18,v36,v37
v_cvt_pk_bf16_f32 v19,v38,v39
s_mov_b32 s72,4096
buffer_store_dwordx4 v[16:19],v182,s[36:39],s72 offen sc1
v_accvgpr_read_b32 v184,a16
v_accvgpr_read_b32 v185,a17
v_accvgpr_read_b32 v186,a18
v_accvgpr_read_b32 v187,a19
v_accvgpr_read_b32 v188,a36
s_add_u32 m0,s58,40960
s_nop 0
buffer_load_dwordx4 v241,s[24:27],s52 offen offset:1024 lds
v_accvgpr_read_b32 v189,a37
v_accvgpr_read_b32 v190,a38
v_accvgpr_read_b32 v191,a39
v_pk_mul_f32 v[184:185],v[184:185],v[178:179]
v_pk_mul_f32 v[186:187],v[186:187],v[178:179]
v_pk_mul_f32 v[188:189],v[188:189],v[178:179]
v_pk_mul_f32 v[190:191],v[190:191],v[178:179]
v_cvt_pk_bf16_f32 v184,v184,v185
v_cvt_pk_bf16_f32 v185,v186,v187
v_cvt_pk_bf16_f32 v186,v188,v189
v_cvt_pk_bf16_f32 v187,v190,v191
v_accvgpr_write_b32 a176,v184
v_accvgpr_write_b32 a177,v185
v_accvgpr_write_b32 a178,v186
v_accvgpr_write_b32 a179,v187
v_accvgpr_read_b32 v192,a40
v_accvgpr_read_b32 v193,a41
v_accvgpr_read_b32 v194,a42
v_accvgpr_read_b32 v195,a43
v_accvgpr_read_b32 v196,a60
v_accvgpr_read_b32 v197,a61
v_accvgpr_read_b32 v198,a62
v_accvgpr_read_b32 v199,a63
v_pk_mul_f32 v[192:193],v[192:193],v[170:171]
v_pk_mul_f32 v[194:195],v[194:195],v[170:171]
v_pk_mul_f32 v[196:197],v[196:197],v[170:171]
v_pk_mul_f32 v[198:199],v[198:199],v[170:171]
v_cvt_pk_bf16_f32 v192,v192,v193
s_add_u32 m0,s58,40960
s_nop 0
buffer_load_dwordx4 v242,s[24:27],s52 offen offset:2048 lds
v_cvt_pk_bf16_f32 v193,v194,v195
v_cvt_pk_bf16_f32 v194,v196,v197
v_cvt_pk_bf16_f32 v195,v198,v199
v_accvgpr_write_b32 a180,v192
v_accvgpr_write_b32 a181,v193
v_accvgpr_write_b32 a182,v194
v_accvgpr_write_b32 a183,v195
v_accvgpr_read_b32 v200,a44
v_accvgpr_read_b32 v201,a45
v_accvgpr_read_b32 v202,a46
v_accvgpr_read_b32 v203,a47
v_accvgpr_read_b32 v204,a64
v_accvgpr_read_b32 v205,a65
v_accvgpr_read_b32 v206,a66
v_accvgpr_read_b32 v207,a67
v_pk_mul_f32 v[200:201],v[200:201],v[172:173]
v_pk_mul_f32 v[202:203],v[202:203],v[172:173]
v_pk_mul_f32 v[204:205],v[204:205],v[172:173]
v_pk_mul_f32 v[206:207],v[206:207],v[172:173]
v_cvt_pk_bf16_f32 v200,v200,v201
v_cvt_pk_bf16_f32 v201,v202,v203
v_cvt_pk_bf16_f32 v202,v204,v205
v_cvt_pk_bf16_f32 v203,v206,v207
v_accvgpr_write_b32 a184,v200
v_accvgpr_write_b32 a185,v201
v_accvgpr_write_b32 a186,v202
v_accvgpr_write_b32 a187,v203
s_add_u32 m0,s58,40960
s_nop 0
buffer_load_dwordx4 v243,s[24:27],s52 offen offset:3072 lds
v_accvgpr_read_b32 v208,a48
v_accvgpr_read_b32 v209,a49
v_accvgpr_read_b32 v210,a50
v_accvgpr_read_b32 v211,a51
v_accvgpr_read_b32 v212,a68
v_accvgpr_read_b32 v213,a69
v_accvgpr_read_b32 v214,a70
v_accvgpr_read_b32 v215,a71
v_pk_mul_f32 v[208:209],v[208:209],v[174:175]
v_pk_mul_f32 v[210:211],v[210:211],v[174:175]
v_pk_mul_f32 v[212:213],v[212:213],v[174:175]
v_pk_mul_f32 v[214:215],v[214:215],v[174:175]
v_cvt_pk_bf16_f32 v208,v208,v209
v_cvt_pk_bf16_f32 v209,v210,v211
v_cvt_pk_bf16_f32 v210,v212,v213
v_cvt_pk_bf16_f32 v211,v214,v215
v_accvgpr_write_b32 a188,v208
v_accvgpr_write_b32 a189,v209
v_accvgpr_write_b32 a190,v210
v_accvgpr_write_b32 a191,v211
v_pk_mul_f32 v[40:41],v[40:41],v[170:171]
v_pk_mul_f32 v[42:43],v[42:43],v[170:171]
v_pk_mul_f32 v[60:61],v[60:61],v[170:171]
v_pk_mul_f32 v[62:63],v[62:63],v[170:171]
v_cvt_pk_bf16_f32 v40,v40,v41
v_cvt_pk_bf16_f32 v41,v42,v43
v_cvt_pk_bf16_f32 v42,v60,v61
v_cvt_pk_bf16_f32 v43,v62,v63
s_add_u32 m0,s58,45056
s_nop 0
buffer_load_dwordx4 v244,s[24:27],s52 offen lds
s_add_u32 s52,s52,40960
s_mov_b32 s72,10240
buffer_store_dwordx4 v[40:43],v182,s[36:39],s72 offen sc1
v_accvgpr_read_b32 v184,a52
v_accvgpr_read_b32 v185,a53
v_accvgpr_read_b32 v186,a54
v_accvgpr_read_b32 v187,a55
v_accvgpr_read_b32 v188,a72
v_accvgpr_read_b32 v189,a73
v_accvgpr_read_b32 v190,a74
v_accvgpr_read_b32 v191,a75
v_pk_mul_f32 v[184:185],v[184:185],v[176:177]
v_pk_mul_f32 v[186:187],v[186:187],v[176:177]
v_pk_mul_f32 v[188:189],v[188:189],v[176:177]
v_pk_mul_f32 v[190:191],v[190:191],v[176:177]
v_cvt_pk_bf16_f32 v184,v184,v185
v_cvt_pk_bf16_f32 v185,v186,v187
v_cvt_pk_bf16_f32 v186,v188,v189
v_cvt_pk_bf16_f32 v187,v190,v191
v_accvgpr_write_b32 a192,v184
v_accvgpr_write_b32 a193,v185
v_accvgpr_write_b32 a194,v186
v_accvgpr_write_b32 a195,v187
v_accvgpr_read_b32 v192,a56
v_accvgpr_read_b32 v193,a57
v_accvgpr_read_b32 v194,a58
v_accvgpr_read_b32 v195,a59
v_accvgpr_read_b32 v196,a76
v_accvgpr_read_b32 v197,a77
v_accvgpr_read_b32 v198,a78
v_accvgpr_read_b32 v199,a79
v_pk_mul_f32 v[192:193],v[192:193],v[178:179]
v_pk_mul_f32 v[194:195],v[194:195],v[178:179]
v_pk_mul_f32 v[196:197],v[196:197],v[178:179]
v_pk_mul_f32 v[198:199],v[198:199],v[178:179]
v_cvt_pk_bf16_f32 v192,v192,v193
v_cvt_pk_bf16_f32 v193,v194,v195
v_cvt_pk_bf16_f32 v194,v196,v197
v_cvt_pk_bf16_f32 v195,v198,v199
v_accvgpr_write_b32 a196,v192
v_accvgpr_write_b32 a197,v193
v_accvgpr_write_b32 a198,v194
v_accvgpr_write_b32 a199,v195
v_mbcnt_lo_u32_b32 v254,-1,0
v_mbcnt_hi_u32_b32 v254,-1,v254
v_lshrrev_b32 v239,4,v254
v_lshlrev_b32 v239,2,v239
v_bfe_u32 v255,v254,2,2
v_add_u32 v239,v239,v255
v_mul_u32_u24 v239,640,v239
v_bfe_u32 v255,v254,1,1
v_lshlrev_b32 v255,4,v255
v_add_u32 v239,v239,v255
v_and_b32 v255,1,v254
v_lshlrev_b32 v255,3,v255
v_add_u32 v239,v239,v255
v_lshrrev_b32 v255,2,v254
v_and_b32 v255,6,v255
s_mul_i32 s64,s16,10
s_add_u32 s65,s64,0
v_xor_b32 v230,s65,v255
v_lshlrev_b32 v230,4,v230
v_add_u32 v230,v230,v239
s_add_u32 s65,s64,2
v_xor_b32 v231,s65,v255
v_lshlrev_b32 v231,4,v231
v_add_u32 v231,v231,v239
s_add_u32 s65,s64,4
v_xor_b32 v232,s65,v255
v_lshlrev_b32 v232,4,v232
v_add_u32 v232,v232,v239
s_add_u32 s65,s64,6
v_xor_b32 v233,s65,v255
v_lshlrev_b32 v233,4,v233
v_add_u32 v233,v233,v239
s_add_u32 s65,s64,8
v_xor_b32 v234,s65,v255
v_lshlrev_b32 v234,4,v234
v_add_u32 v234,v234,v239
v_add_u32 v235,40960,v230
v_add_u32 v236,40960,v231
v_add_u32 v237,40960,v232
v_add_u32 v238,40960,v233
v_add_u32 v239,40960,v234
v_lshlrev_b32 v247,4,v254
v_add_u32 v247,81920,v247
v_mbcnt_lo_u32_b32 v253,-1,0
v_mbcnt_hi_u32_b32 v253,-1,v253
v_lshlrev_b32 v253,4,v253
v_mbcnt_lo_u32_b32 v255,-1,0
v_mbcnt_hi_u32_b32 v255,-1,v255
v_min_u32 v254,9,v255
v_lshlrev_b32 v254,7,v254
v_min_u32 v255,19,v255
v_mul_u32_u24 v255,128,v255
s_lshl_b32 s69,s50,7
s_mov_b32 s7,0
s_waitcnt vmcnt(15)
s_barrier
ds_read_b128 a[40:43],v247
ds_read_b128 a[44:47],v247 offset:1024
ds_read_b128 a[48:51],v247 offset:2048
ds_read_b128 a[52:55],v247 offset:3072
ds_read_b128 a[56:59],v247 offset:4096
ds_read_b64_tr_b16 a[0:1],v230
ds_read_b64_tr_b16 a[2:3],v230 offset:10240
ds_read_b64_tr_b16 a[4:5],v231
ds_read_b64_tr_b16 a[6:7],v231 offset:10240
ds_read_b64_tr_b16 a[8:9],v232
ds_read_b64_tr_b16 a[10:11],v232 offset:10240
ds_read_b64_tr_b16 a[12:13],v233
ds_read_b64_tr_b16 a[14:15],v233 offset:10240
ds_read_b64_tr_b16 a[16:17],v234
ds_read_b64_tr_b16 a[18:19],v234 offset:10240
s_waitcnt lgkmcnt(0)
v_mfma_f32_16x16x32_bf16 v[0:3],a[0:3],a[40:43],0
v_mfma_f32_16x16x32_bf16 v[40:43],a[4:7],a[40:43],0
ds_read_b128 a[60:63],v247 offset:5120
v_mfma_f32_16x16x32_bf16 v[80:83],a[8:11],a[40:43],0
s_add_u32 m0,s59,40960
v_mfma_f32_16x16x32_bf16 v[120:123],a[12:15],a[40:43],0
buffer_load_dwordx4 v245,s[20:23],s53 offen sc1 lds
v_mfma_f32_16x16x32_bf16 v[160:163],a[16:19],a[40:43],0
s_add_u32 m0,s59,40960
ds_read_b128 a[64:67],v247 offset:6144
v_mfma_f32_16x16x32_bf16 v[4:7],a[0:3],a[44:47],0
buffer_load_dwordx4 v245,s[20:23],s53 offen offset:1024 sc1 lds
v_mfma_f32_16x16x32_bf16 v[44:47],a[4:7],a[44:47],0
s_add_u32 m0,s59,40960
ds_read_b128 a[68:71],v247 offset:7168
v_mfma_f32_16x16x32_bf16 v[84:87],a[8:11],a[44:47],0
buffer_load_dwordx4 v245,s[20:23],s53 offen offset:2048 sc1 lds
v_mfma_f32_16x16x32_bf16 v[124:127],a[12:15],a[44:47],0
s_add_u32 m0,s59,40960
ds_read_b128 a[72:75],v247 offset:8192
v_mfma_f32_16x16x32_bf16 v[164:167],a[16:19],a[44:47],0
buffer_load_dwordx4 v245,s[20:23],s53 offen offset:3072 sc1 lds
v_mfma_f32_16x16x32_bf16 v[8:11],a[0:3],a[48:51],0
s_add_u32 m0,s59,45056
ds_read_b128 a[76:79],v247 offset:9216
v_mfma_f32_16x16x32_bf16 v[48:51],a[4:7],a[48:51],0
s_add_u32 s67,s53,4096
v_mfma_f32_16x16x32_bf16 v[88:91],a[8:11],a[48:51],0
buffer_load_dwordx4 v245,s[20:23],s67 offen sc1 lds
v_mfma_f32_16x16x32_bf16 v[128:131],a[12:15],a[48:51],0
s_add_u32 s53,s53,20480
v_mfma_f32_16x16x32_bf16 v[168:171],a[16:19],a[48:51],0
v_mfma_f32_16x16x32_bf16 v[12:15],a[0:3],a[52:55],0
s_mov_b32 s72,11264
v_mfma_f32_16x16x32_bf16 v[52:55],a[4:7],a[52:55],0
buffer_store_dwordx4 a[104:107],v253,s[36:39],s72 offen sc1
v_mfma_f32_16x16x32_bf16 v[92:95],a[8:11],a[52:55],0
v_mfma_f32_16x16x32_bf16 v[132:135],a[12:15],a[52:55],0
v_mfma_f32_16x16x32_bf16 v[172:175],a[16:19],a[52:55],0
v_mfma_f32_16x16x32_bf16 v[16:19],a[0:3],a[56:59],0
s_mov_b32 s72,12288
v_mfma_f32_16x16x32_bf16 v[56:59],a[4:7],a[56:59],0
buffer_store_dwordx4 a[108:111],v253,s[36:39],s72 offen sc1
v_mfma_f32_16x16x32_bf16 v[96:99],a[8:11],a[56:59],0
v_mfma_f32_16x16x32_bf16 v[136:139],a[12:15],a[56:59],0
v_mfma_f32_16x16x32_bf16 v[176:179],a[16:19],a[56:59],0
s_waitcnt vmcnt(17) lgkmcnt(0)
s_barrier
v_mfma_f32_16x16x32_bf16 v[20:23],a[0:3],a[60:63],0
ds_read_b128 a[40:43],v247 offset:10240
v_mfma_f32_16x16x32_bf16 v[60:63],a[4:7],a[60:63],0
ds_read_b128 a[44:47],v247 offset:11264
v_mfma_f32_16x16x32_bf16 v[100:103],a[8:11],a[60:63],0
s_mov_b32 s72,13312
ds_read_b128 a[48:51],v247 offset:12288
v_mfma_f32_16x16x32_bf16 v[140:143],a[12:15],a[60:63],0
buffer_store_dwordx4 a[112:115],v253,s[36:39],s72 offen sc1
v_mfma_f32_16x16x32_bf16 v[180:183],a[16:19],a[60:63],0
ds_read_b128 a[52:55],v247 offset:13312
ds_read_b128 a[56:59],v247 offset:14336
v_mfma_f32_16x16x32_bf16 v[24:27],a[0:3],a[64:67],0
ds_read_b64_tr_b16 a[20:21],v230 offset:20480
v_mfma_f32_16x16x32_bf16 v[64:67],a[4:7],a[64:67],0
ds_read_b64_tr_b16 a[22:23],v230 offset:30720
v_mfma_f32_16x16x32_bf16 v[104:107],a[8:11],a[64:67],0
s_mov_b32 s72,14336
ds_read_b64_tr_b16 a[24:25],v231 offset:20480
v_mfma_f32_16x16x32_bf16 v[144:147],a[12:15],a[64:67],0
buffer_store_dwordx4 a[116:119],v253,s[36:39],s72 offen sc1
v_mfma_f32_16x16x32_bf16 v[184:187],a[16:19],a[64:67],0
ds_read_b64_tr_b16 a[26:27],v231 offset:30720
ds_read_b64_tr_b16 a[28:29],v232 offset:20480
v_mfma_f32_16x16x32_bf16 v[28:31],a[0:3],a[68:71],0
ds_read_b64_tr_b16 a[30:31],v232 offset:30720
v_mfma_f32_16x16x32_bf16 v[68:71],a[4:7],a[68:71],0
ds_read_b64_tr_b16 a[32:33],v233 offset:20480
v_mfma_f32_16x16x32_bf16 v[108:111],a[8:11],a[68:71],0
s_mov_b32 s72,20480
ds_read_b64_tr_b16 a[34:35],v233 offset:30720
v_mfma_f32_16x16x32_bf16 v[148:151],a[12:15],a[68:71],0
buffer_store_dwordx4 a[120:123],v253,s[36:39],s72 offen sc1
v_mfma_f32_16x16x32_bf16 v[188:191],a[16:19],a[68:71],0
ds_read_b64_tr_b16 a[36:37],v234 offset:20480
ds_read_b64_tr_b16 a[38:39],v234 offset:30720
v_mfma_f32_16x16x32_bf16 v[32:35],a[0:3],a[72:75],0
v_mfma_f32_16x16x32_bf16 v[72:75],a[4:7],a[72:75],0
v_mfma_f32_16x16x32_bf16 v[112:115],a[8:11],a[72:75],0
s_mov_b32 s72,21504
v_mfma_f32_16x16x32_bf16 v[152:155],a[12:15],a[72:75],0
s_cmp_eq_u32 s51,3
buffer_store_dwordx4 a[124:127],v253,s[36:39],s72 offen sc1
v_mfma_f32_16x16x32_bf16 v[192:195],a[16:19],a[72:75],0
s_cselect_b32 s66,s50,0
v_mfma_f32_16x16x32_bf16 v[36:39],a[0:3],a[76:79],0
s_mul_i32 s74,s66,-1966080
v_mfma_f32_16x16x32_bf16 v[76:79],a[4:7],a[76:79],0
s_add_u32 s74,s74,40960
v_mfma_f32_16x16x32_bf16 v[116:119],a[8:11],a[76:79],0
s_mul_i32 s75,s66,-491520
v_mfma_f32_16x16x32_bf16 v[156:159],a[12:15],a[76:79],0
s_add_u32 s75,s75,20480
v_mfma_f32_16x16x32_bf16 v[196:199],a[16:19],a[76:79],0
s_waitcnt lgkmcnt(0)
v_mfma_f32_16x16x32_bf16 v[0:3],a[20:23],a[40:43],v[0:3]
s_mul_i32 s6,s66,-1966080
v_mfma_f32_16x16x32_bf16 v[40:43],a[24:27],a[40:43],v[40:43]
s_add_u32 s6,s6,81920
ds_read_b128 a[60:63],v247 offset:15360
v_mfma_f32_16x16x32_bf16 v[80:83],a[28:31],a[40:43],v[80:83]
v_mfma_f32_16x16x32_bf16 v[120:123],a[32:35],a[40:43],v[120:123]
ds_read_b128 a[64:67],v247 offset:16384
v_mfma_f32_16x16x32_bf16 v[160:163],a[36:39],a[40:43],v[160:163]
v_mfma_f32_16x16x32_bf16 v[4:7],a[20:23],a[44:47],v[4:7]
ds_read_b128 a[68:71],v247 offset:17408
v_mfma_f32_16x16x32_bf16 v[44:47],a[24:27],a[44:47],v[44:47]
v_mfma_f32_16x16x32_bf16 v[84:87],a[28:31],a[44:47],v[84:87]
ds_read_b128 a[72:75],v247 offset:18432
v_mfma_f32_16x16x32_bf16 v[124:127],a[32:35],a[44:47],v[124:127]
v_mfma_f32_16x16x32_bf16 v[164:167],a[36:39],a[44:47],v[164:167]
s_add_u32 m0,s58,61440
ds_read_b128 a[76:79],v247 offset:19456
v_mfma_f32_16x16x32_bf16 v[8:11],a[20:23],a[48:51],v[8:11]
buffer_load_dwordx4 v240,s[24:27],s52 offen lds
v_mfma_f32_16x16x32_bf16 v[48:51],a[24:27],a[48:51],v[48:51]
v_mfma_f32_16x16x32_bf16 v[88:91],a[28:31],a[48:51],v[88:91]
s_add_u32 m0,s58,61440
v_mfma_f32_16x16x32_bf16 v[128:131],a[32:35],a[48:51],v[128:131]
buffer_load_dwordx4 v241,s[24:27],s52 offen offset:1024 lds
v_mfma_f32_16x16x32_bf16 v[168:171],a[36:39],a[48:51],v[168:171]
v_mfma_f32_16x16x32_bf16 v[12:15],a[20:23],a[52:55],v[12:15]
s_add_u32 m0,s58,61440
v_mfma_f32_16x16x32_bf16 v[52:55],a[24:27],a[52:55],v[52:55]
buffer_load_dwordx4 v242,s[24:27],s52 offen offset:2048 lds
v_mfma_f32_16x16x32_bf16 v[92:95],a[28:31],a[52:55],v[92:95]
v_mfma_f32_16x16x32_bf16 v[132:135],a[32:35],a[52:55],v[132:135]
s_add_u32 m0,s58,61440
v_mfma_f32_16x16x32_bf16 v[172:175],a[36:39],a[52:55],v[172:175]
buffer_load_dwordx4 v243,s[24:27],s52 offen offset:3072 lds
v_mfma_f32_16x16x32_bf16 v[16:19],a[20:23],a[56:59],v[16:19]
v_mfma_f32_16x16x32_bf16 v[56:59],a[24:27],a[56:59],v[56:59]
s_add_u32 m0,s58,65536
v_mfma_f32_16x16x32_bf16 v[96:99],a[28:31],a[56:59],v[96:99]
buffer_load_dwordx4 v244,s[24:27],s52 offen lds
v_mfma_f32_16x16x32_bf16 v[136:139],a[32:35],a[56:59],v[136:139]
s_add_u32 s52,s52,40960
v_mfma_f32_16x16x32_bf16 v[176:179],a[36:39],a[56:59],v[176:179]
s_waitcnt vmcnt(16) lgkmcnt(0)
s_barrier
v_mfma_f32_16x16x32_bf16 v[20:23],a[20:23],a[60:63],v[20:23]
ds_read_b128 a[40:43],v247 offset:20480
v_mfma_f32_16x16x32_bf16 v[60:63],a[24:27],a[60:63],v[60:63]
ds_read_b128 a[44:47],v247 offset:21504
v_mfma_f32_16x16x32_bf16 v[100:103],a[28:31],a[60:63],v[100:103]
s_add_u32 m0,s58,0
ds_read_b128 a[48:51],v247 offset:22528
v_mfma_f32_16x16x32_bf16 v[140:143],a[32:35],a[60:63],v[140:143]
buffer_load_dwordx4 v240,s[24:27],s52 offen lds
v_mfma_f32_16x16x32_bf16 v[180:183],a[36:39],a[60:63],v[180:183]
ds_read_b128 a[52:55],v247 offset:23552
ds_read_b128 a[56:59],v247 offset:24576
v_mfma_f32_16x16x32_bf16 v[24:27],a[20:23],a[64:67],v[24:27]
s_add_u32 m0,s58,0
ds_read_b64_tr_b16 a[0:1],v235
v_mfma_f32_16x16x32_bf16 v[64:67],a[24:27],a[64:67],v[64:67]
buffer_load_dwordx4 v241,s[24:27],s52 offen offset:1024 lds
v_mfma_f32_16x16x32_bf16 v[104:107],a[28:31],a[64:67],v[104:107]
ds_read_b64_tr_b16 a[2:3],v235 offset:10240
ds_read_b64_tr_b16 a[4:5],v236
v_mfma_f32_16x16x32_bf16 v[144:147],a[32:35],a[64:67],v[144:147]
s_add_u32 m0,s58,0
ds_read_b64_tr_b16 a[6:7],v236 offset:10240
v_mfma_f32_16x16x32_bf16 v[184:187],a[36:39],a[64:67],v[184:187]
buffer_load_dwordx4 v242,s[24:27],s52 offen offset:2048 lds
v_mfma_f32_16x16x32_bf16 v[28:31],a[20:23],a[68:71],v[28:31]
ds_read_b64_tr_b16 a[8:9],v237
ds_read_b64_tr_b16 a[10:11],v237 offset:10240
v_mfma_f32_16x16x32_bf16 v[68:71],a[24:27],a[68:71],v[68:71]
s_add_u32 m0,s58,0
ds_read_b64_tr_b16 a[12:13],v238
v_mfma_f32_16x16x32_bf16 v[108:111],a[28:31],a[68:71],v[108:111]
buffer_load_dwordx4 v243,s[24:27],s52 offen offset:3072 lds
v_mfma_f32_16x16x32_bf16 v[148:151],a[32:35],a[68:71],v[148:151]
ds_read_b64_tr_b16 a[14:15],v238 offset:10240
ds_read_b64_tr_b16 a[16:17],v239
v_mfma_f32_16x16x32_bf16 v[188:191],a[36:39],a[68:71],v[188:191]
s_add_u32 m0,s58,4096
ds_read_b64_tr_b16 a[18:19],v239 offset:10240
v_mfma_f32_16x16x32_bf16 v[32:35],a[20:23],a[72:75],v[32:35]
buffer_load_dwordx4 v244,s[24:27],s52 offen lds
v_mfma_f32_16x16x32_bf16 v[72:75],a[24:27],a[72:75],v[72:75]
s_add_u32 s52,s52,40960
v_mfma_f32_16x16x32_bf16 v[112:115],a[28:31],a[72:75],v[112:115]
v_mfma_f32_16x16x32_bf16 v[152:155],a[32:35],a[72:75],v[152:155]
v_mfma_f32_16x16x32_bf16 v[192:195],a[36:39],a[72:75],v[192:195]
v_mfma_f32_16x16x32_bf16 v[36:39],a[20:23],a[76:79],v[36:39]
v_mfma_f32_16x16x32_bf16 v[76:79],a[24:27],a[76:79],v[76:79]
v_mfma_f32_16x16x32_bf16 v[116:119],a[28:31],a[76:79],v[116:119]
v_mfma_f32_16x16x32_bf16 v[156:159],a[32:35],a[76:79],v[156:159]
v_mfma_f32_16x16x32_bf16 v[196:199],a[36:39],a[76:79],v[196:199]
s_waitcnt lgkmcnt(0)
v_mfma_f32_16x16x32_bf16 v[0:3],a[0:3],a[40:43],v[0:3]
v_mfma_f32_16x16x32_bf16 v[40:43],a[4:7],a[40:43],v[40:43]
ds_read_b128 a[60:63],v247 offset:25600
v_mfma_f32_16x16x32_bf16 v[80:83],a[8:11],a[40:43],v[80:83]
s_add_u32 m0,s59,0
v_mfma_f32_16x16x32_bf16 v[120:123],a[12:15],a[40:43],v[120:123]
buffer_load_dwordx4 v245,s[20:23],s53 offen sc1 lds
v_mfma_f32_16x16x32_bf16 v[160:163],a[16:19],a[40:43],v[160:163]
s_add_u32 m0,s59,0
ds_read_b128 a[64:67],v247 offset:26624
v_mfma_f32_16x16x32_bf16 v[4:7],a[0:3],a[44:47],v[4:7]
buffer_load_dwordx4 v245,s[20:23],s53 offen offset:1024 sc1 lds
v_mfma_f32_16x16x32_bf16 v[44:47],a[4:7],a[44:47],v[44:47]
s_add_u32 m0,s59,0
ds_read_b128 a[68:71],v247 offset:27648
v_mfma_f32_16x16x32_bf16 v[84:87],a[8:11],a[44:47],v[84:87]
buffer_load_dwordx4 v245,s[20:23],s53 offen offset:2048 sc1 lds
v_mfma_f32_16x16x32_bf16 v[124:127],a[12:15],a[44:47],v[124:127]
s_add_u32 m0,s59,0
ds_read_b128 a[72:75],v247 offset:28672
v_mfma_f32_16x16x32_bf16 v[164:167],a[16:19],a[44:47],v[164:167]
buffer_load_dwordx4 v245,s[20:23],s53 offen offset:3072 sc1 lds
v_mfma_f32_16x16x32_bf16 v[8:11],a[0:3],a[48:51],v[8:11]
s_add_u32 m0,s59,4096
ds_read_b128 a[76:79],v247 offset:29696
v_mfma_f32_16x16x32_bf16 v[48:51],a[4:7],a[48:51],v[48:51]
s_add_u32 s67,s53,4096
v_mfma_f32_16x16x32_bf16 v[88:91],a[8:11],a[48:51],v[88:91]
buffer_load_dwordx4 v245,s[20:23],s67 offen sc1 lds
v_mfma_f32_16x16x32_bf16 v[128:131],a[12:15],a[48:51],v[128:131]
s_add_u32 s53,s53,20480
v_mfma_f32_16x16x32_bf16 v[168:171],a[16:19],a[48:51],v[168:171]
s_add_u32 s69,s69,64
v_mfma_f32_16x16x32_bf16 v[12:15],a[0:3],a[52:55],v[12:15]
s_mov_b32 s72,22528
v_mfma_f32_16x16x32_bf16 v[52:55],a[4:7],a[52:55],v[52:55]
s_and_b32 s69,s69,255
buffer_store_dwordx4 a[128:131],v253,s[36:39],s72 offen sc1
v_mfma_f32_16x16x32_bf16 v[92:95],a[8:11],a[52:55],v[92:95]
s_sub_u32 s51,s51,1
v_mfma_f32_16x16x32_bf16 v[132:135],a[12:15],a[52:55],v[132:135]
v_mfma_f32_16x16x32_bf16 v[172:175],a[16:19],a[52:55],v[172:175]
v_mfma_f32_16x16x32_bf16 v[16:19],a[0:3],a[56:59],v[16:19]
s_mov_b32 s72,23552
v_mfma_f32_16x16x32_bf16 v[56:59],a[4:7],a[56:59],v[56:59]
buffer_store_dwordx4 a[132:135],v253,s[36:39],s72 offen sc1
v_mfma_f32_16x16x32_bf16 v[96:99],a[8:11],a[56:59],v[96:99]
v_mfma_f32_16x16x32_bf16 v[136:139],a[12:15],a[56:59],v[136:139]
v_mfma_f32_16x16x32_bf16 v[176:179],a[16:19],a[56:59],v[176:179]
s_waitcnt vmcnt(12) lgkmcnt(0)
s_barrier
v_mfma_f32_16x16x32_bf16 v[20:23],a[0:3],a[60:63],v[20:23]
ds_read_b128 a[40:43],v247 offset:30720
v_mfma_f32_16x16x32_bf16 v[60:63],a[4:7],a[60:63],v[60:63]
ds_read_b128 a[44:47],v247 offset:31744
v_mfma_f32_16x16x32_bf16 v[100:103],a[8:11],a[60:63],v[100:103]
s_mov_b32 s72,24576
ds_read_b128 a[48:51],v247 offset:32768
v_mfma_f32_16x16x32_bf16 v[140:143],a[12:15],a[60:63],v[140:143]
buffer_store_dwordx4 a[136:139],v253,s[36:39],s72 offen sc1
v_mfma_f32_16x16x32_bf16 v[180:183],a[16:19],a[60:63],v[180:183]
ds_read_b128 a[52:55],v247 offset:33792
ds_read_b128 a[56:59],v247 offset:34816
v_mfma_f32_16x16x32_bf16 v[24:27],a[0:3],a[64:67],v[24:27]
ds_read_b64_tr_b16 a[20:21],v235 offset:20480
v_mfma_f32_16x16x32_bf16 v[64:67],a[4:7],a[64:67],v[64:67]
ds_read_b64_tr_b16 a[22:23],v235 offset:30720
v_mfma_f32_16x16x32_bf16 v[104:107],a[8:11],a[64:67],v[104:107]
s_mov_b32 s72,30720
ds_read_b64_tr_b16 a[24:25],v236 offset:20480
v_mfma_f32_16x16x32_bf16 v[144:147],a[12:15],a[64:67],v[144:147]
buffer_store_dwordx4 a[140:143],v253,s[36:39],s72 offen sc1
v_mfma_f32_16x16x32_bf16 v[184:187],a[16:19],a[64:67],v[184:187]
ds_read_b64_tr_b16 a[26:27],v236 offset:30720
ds_read_b64_tr_b16 a[28:29],v237 offset:20480
v_mfma_f32_16x16x32_bf16 v[28:31],a[0:3],a[68:71],v[28:31]
ds_read_b64_tr_b16 a[30:31],v237 offset:30720
v_mfma_f32_16x16x32_bf16 v[68:71],a[4:7],a[68:71],v[68:71]
ds_read_b64_tr_b16 a[32:33],v238 offset:20480
v_mfma_f32_16x16x32_bf16 v[108:111],a[8:11],a[68:71],v[108:111]
s_mov_b32 s72,31744
ds_read_b64_tr_b16 a[34:35],v238 offset:30720
v_mfma_f32_16x16x32_bf16 v[148:151],a[12:15],a[68:71],v[148:151]
buffer_store_dwordx4 a[144:147],v253,s[36:39],s72 offen sc1
v_mfma_f32_16x16x32_bf16 v[188:191],a[16:19],a[68:71],v[188:191]
ds_read_b64_tr_b16 a[36:37],v239 offset:20480
ds_read_b64_tr_b16 a[38:39],v239 offset:30720
v_mfma_f32_16x16x32_bf16 v[32:35],a[0:3],a[72:75],v[32:35]
v_mfma_f32_16x16x32_bf16 v[72:75],a[4:7],a[72:75],v[72:75]
v_mfma_f32_16x16x32_bf16 v[112:115],a[8:11],a[72:75],v[112:115]
s_mov_b32 s72,32768
v_mfma_f32_16x16x32_bf16 v[152:155],a[12:15],a[72:75],v[152:155]
buffer_store_dwordx4 a[148:151],v253,s[36:39],s72 offen sc1
v_mfma_f32_16x16x32_bf16 v[192:195],a[16:19],a[72:75],v[192:195]
v_mfma_f32_16x16x32_bf16 v[36:39],a[0:3],a[76:79],v[36:39]
v_mfma_f32_16x16x32_bf16 v[76:79],a[4:7],a[76:79],v[76:79]
v_mfma_f32_16x16x32_bf16 v[116:119],a[8:11],a[76:79],v[116:119]
v_mfma_f32_16x16x32_bf16 v[156:159],a[12:15],a[76:79],v[156:159]
v_mfma_f32_16x16x32_bf16 v[196:199],a[16:19],a[76:79],v[196:199]
s_waitcnt lgkmcnt(0)
v_mfma_f32_16x16x32_bf16 v[0:3],a[20:23],a[40:43],v[0:3]
v_mfma_f32_16x16x32_bf16 v[40:43],a[24:27],a[40:43],v[40:43]
ds_read_b128 a[60:63],v247 offset:35840
v_mfma_f32_16x16x32_bf16 v[80:83],a[28:31],a[40:43],v[80:83]
v_mfma_f32_16x16x32_bf16 v[120:123],a[32:35],a[40:43],v[120:123]
ds_read_b128 a[64:67],v247 offset:36864
v_mfma_f32_16x16x32_bf16 v[160:163],a[36:39],a[40:43],v[160:163]
v_mfma_f32_16x16x32_bf16 v[4:7],a[20:23],a[44:47],v[4:7]
ds_read_b128 a[68:71],v247 offset:37888
v_mfma_f32_16x16x32_bf16 v[44:47],a[24:27],a[44:47],v[44:47]
v_mfma_f32_16x16x32_bf16 v[84:87],a[28:31],a[44:47],v[84:87]
ds_read_b128 a[72:75],v247 offset:38912
v_mfma_f32_16x16x32_bf16 v[124:127],a[32:35],a[44:47],v[124:127]
v_mfma_f32_16x16x32_bf16 v[164:167],a[36:39],a[44:47],v[164:167]
s_add_u32 m0,s58,20480
ds_read_b128 a[76:79],v247 offset:39936
v_mfma_f32_16x16x32_bf16 v[8:11],a[20:23],a[48:51],v[8:11]
buffer_load_dwordx4 v240,s[24:27],s52 offen lds
v_mfma_f32_16x16x32_bf16 v[48:51],a[24:27],a[48:51],v[48:51]
v_mfma_f32_16x16x32_bf16 v[88:91],a[28:31],a[48:51],v[88:91]
s_add_u32 m0,s58,20480
v_mfma_f32_16x16x32_bf16 v[128:131],a[32:35],a[48:51],v[128:131]
buffer_load_dwordx4 v241,s[24:27],s52 offen offset:1024 lds
v_mfma_f32_16x16x32_bf16 v[168:171],a[36:39],a[48:51],v[168:171]
v_mfma_f32_16x16x32_bf16 v[12:15],a[20:23],a[52:55],v[12:15]
s_add_u32 m0,s58,20480
v_mfma_f32_16x16x32_bf16 v[52:55],a[24:27],a[52:55],v[52:55]
buffer_load_dwordx4 v242,s[24:27],s52 offen offset:2048 lds
v_mfma_f32_16x16x32_bf16 v[92:95],a[28:31],a[52:55],v[92:95]
v_mfma_f32_16x16x32_bf16 v[132:135],a[32:35],a[52:55],v[132:135]
s_add_u32 m0,s58,20480
v_mfma_f32_16x16x32_bf16 v[172:175],a[36:39],a[52:55],v[172:175]
buffer_load_dwordx4 v243,s[24:27],s52 offen offset:3072 lds
v_mfma_f32_16x16x32_bf16 v[16:19],a[20:23],a[56:59],v[16:19]
v_mfma_f32_16x16x32_bf16 v[56:59],a[24:27],a[56:59],v[56:59]
s_add_u32 m0,s58,24576
v_mfma_f32_16x16x32_bf16 v[96:99],a[28:31],a[56:59],v[96:99]
buffer_load_dwordx4 v244,s[24:27],s52 offen lds
v_mfma_f32_16x16x32_bf16 v[136:139],a[32:35],a[56:59],v[136:139]
s_add_u32 s52,s52,40960
v_mfma_f32_16x16x32_bf16 v[176:179],a[36:39],a[56:59],v[176:179]
s_waitcnt vmcnt(16) lgkmcnt(0)
s_barrier
v_mfma_f32_16x16x32_bf16 v[20:23],a[20:23],a[60:63],v[20:23]
ds_read_b128 a[40:43],v247 offset:40960
v_mfma_f32_16x16x32_bf16 v[60:63],a[24:27],a[60:63],v[60:63]
ds_read_b128 a[44:47],v247 offset:41984
v_mfma_f32_16x16x32_bf16 v[100:103],a[28:31],a[60:63],v[100:103]
s_add_u32 m0,s58,40960
ds_read_b128 a[48:51],v247 offset:43008
v_mfma_f32_16x16x32_bf16 v[140:143],a[32:35],a[60:63],v[140:143]
buffer_load_dwordx4 v240,s[24:27],s52 offen lds
v_mfma_f32_16x16x32_bf16 v[180:183],a[36:39],a[60:63],v[180:183]
ds_read_b128 a[52:55],v247 offset:44032
ds_read_b128 a[56:59],v247 offset:45056
v_mfma_f32_16x16x32_bf16 v[24:27],a[20:23],a[64:67],v[24:27]
s_add_u32 m0,s58,40960
ds_read_b64_tr_b16 a[0:1],v230
v_mfma_f32_16x16x32_bf16 v[64:67],a[24:27],a[64:67],v[64:67]
buffer_load_dwordx4 v241,s[24:27],s52 offen offset:1024 lds
v_mfma_f32_16x16x32_bf16 v[104:107],a[28:31],a[64:67],v[104:107]
ds_read_b64_tr_b16 a[2:3],v230 offset:10240
ds_read_b64_tr_b16 a[4:5],v231
v_mfma_f32_16x16x32_bf16 v[144:147],a[32:35],a[64:67],v[144:147]
s_add_u32 m0,s58,40960
ds_read_b64_tr_b16 a[6:7],v231 offset:10240
v_mfma_f32_16x16x32_bf16 v[184:187],a[36:39],a[64:67],v[184:187]
buffer_load_dwordx4 v242,s[24:27],s52 offen offset:2048 lds
v_mfma_f32_16x16x32_bf16 v[28:31],a[20:23],a[68:71],v[28:31]
ds_read_b64_tr_b16 a[8:9],v232
ds_read_b64_tr_b16 a[10:11],v232 offset:10240
v_mfma_f32_16x16x32_bf16 v[68:71],a[24:27],a[68:71],v[68:71]
s_add_u32 m0,s58,40960
ds_read_b64_tr_b16 a[12:13],v233
v_mfma_f32_16x16x32_bf16 v[108:111],a[28:31],a[68:71],v[108:111]
buffer_load_dwordx4 v243,s[24:27],s52 offen offset:3072 lds
v_mfma_f32_16x16x32_bf16 v[148:151],a[32:35],a[68:71],v[148:151]
ds_read_b64_tr_b16 a[14:15],v233 offset:10240
ds_read_b64_tr_b16 a[16:17],v234
v_mfma_f32_16x16x32_bf16 v[188:191],a[36:39],a[68:71],v[188:191]
s_add_u32 m0,s58,45056
ds_read_b64_tr_b16 a[18:19],v234 offset:10240
v_mfma_f32_16x16x32_bf16 v[32:35],a[20:23],a[72:75],v[32:35]
buffer_load_dwordx4 v244,s[24:27],s52 offen lds
v_mfma_f32_16x16x32_bf16 v[72:75],a[24:27],a[72:75],v[72:75]
s_add_u32 s52,s52,40960
v_mfma_f32_16x16x32_bf16 v[112:115],a[28:31],a[72:75],v[112:115]
v_mfma_f32_16x16x32_bf16 v[152:155],a[32:35],a[72:75],v[152:155]
v_mfma_f32_16x16x32_bf16 v[192:195],a[36:39],a[72:75],v[192:195]
v_mfma_f32_16x16x32_bf16 v[36:39],a[20:23],a[76:79],v[36:39]
v_mfma_f32_16x16x32_bf16 v[76:79],a[24:27],a[76:79],v[76:79]
v_mfma_f32_16x16x32_bf16 v[116:119],a[28:31],a[76:79],v[116:119]
v_mfma_f32_16x16x32_bf16 v[156:159],a[32:35],a[76:79],v[156:159]
v_mfma_f32_16x16x32_bf16 v[196:199],a[36:39],a[76:79],v[196:199]
s_waitcnt lgkmcnt(0)
v_mfma_f32_16x16x32_bf16 v[0:3],a[0:3],a[40:43],v[0:3]
v_mfma_f32_16x16x32_bf16 v[40:43],a[4:7],a[40:43],v[40:43]
ds_read_b128 a[60:63],v247 offset:46080
v_mfma_f32_16x16x32_bf16 v[80:83],a[8:11],a[40:43],v[80:83]
s_add_u32 m0,s59,20480
v_mfma_f32_16x16x32_bf16 v[120:123],a[12:15],a[40:43],v[120:123]
buffer_load_dwordx4 v245,s[20:23],s53 offen sc1 lds
v_mfma_f32_16x16x32_bf16 v[160:163],a[16:19],a[40:43],v[160:163]
s_add_u32 m0,s59,20480
ds_read_b128 a[64:67],v247 offset:47104
v_mfma_f32_16x16x32_bf16 v[4:7],a[0:3],a[44:47],v[4:7]
buffer_load_dwordx4 v245,s[20:23],s53 offen offset:1024 sc1 lds
v_mfma_f32_16x16x32_bf16 v[44:47],a[4:7],a[44:47],v[44:47]
s_add_u32 m0,s59,20480
ds_read_b128 a[68:71],v247 offset:48128
v_mfma_f32_16x16x32_bf16 v[84:87],a[8:11],a[44:47],v[84:87]
buffer_load_dwordx4 v245,s[20:23],s53 offen offset:2048 sc1 lds
v_mfma_f32_16x16x32_bf16 v[124:127],a[12:15],a[44:47],v[124:127]
s_add_u32 m0,s59,20480
ds_read_b128 a[72:75],v247 offset:49152
v_mfma_f32_16x16x32_bf16 v[164:167],a[16:19],a[44:47],v[164:167]
buffer_load_dwordx4 v245,s[20:23],s53 offen offset:3072 sc1 lds
v_mfma_f32_16x16x32_bf16 v[8:11],a[0:3],a[48:51],v[8:11]
s_add_u32 m0,s59,24576
ds_read_b128 a[76:79],v247 offset:50176
v_mfma_f32_16x16x32_bf16 v[48:51],a[4:7],a[48:51],v[48:51]
s_add_u32 s67,s53,4096
v_mfma_f32_16x16x32_bf16 v[88:91],a[8:11],a[48:51],v[88:91]
buffer_load_dwordx4 v245,s[20:23],s67 offen sc1 lds
v_mfma_f32_16x16x32_bf16 v[128:131],a[12:15],a[48:51],v[128:131]
s_add_u32 s53,s53,20480
v_mfma_f32_16x16x32_bf16 v[168:171],a[16:19],a[48:51],v[168:171]
buffer_load_dword v246,v253,s[12:15],s69 offen sc1
v_mfma_f32_16x16x32_bf16 v[12:15],a[0:3],a[52:55],v[12:15]
s_mov_b32 s72,33792
v_mfma_f32_16x16x32_bf16 v[52:55],a[4:7],a[52:55],v[52:55]
buffer_store_dwordx4 a[152:155],v253,s[36:39],s72 offen sc1
v_mfma_f32_16x16x32_bf16 v[92:95],a[8:11],a[52:55],v[92:95]
v_mfma_f32_16x16x32_bf16 v[132:135],a[12:15],a[52:55],v[132:135]
v_mfma_f32_16x16x32_bf16 v[172:175],a[16:19],a[52:55],v[172:175]
v_mfma_f32_16x16x32_bf16 v[16:19],a[0:3],a[56:59],v[16:19]
s_mov_b32 s72,34816
v_mfma_f32_16x16x32_bf16 v[56:59],a[4:7],a[56:59],v[56:59]
buffer_store_dwordx4 a[156:159],v253,s[36:39],s72 offen sc1
v_mfma_f32_16x16x32_bf16 v[96:99],a[8:11],a[56:59],v[96:99]
v_mfma_f32_16x16x32_bf16 v[136:139],a[12:15],a[56:59],v[136:139]
v_mfma_f32_16x16x32_bf16 v[176:179],a[16:19],a[56:59],v[176:179]
s_waitcnt vmcnt(13) lgkmcnt(0)
s_barrier
v_mfma_f32_16x16x32_bf16 v[20:23],a[0:3],a[60:63],v[20:23]
ds_read_b128 a[40:43],v247 offset:51200
v_mfma_f32_16x16x32_bf16 v[60:63],a[4:7],a[60:63],v[60:63]
ds_read_b128 a[44:47],v247 offset:52224
v_mfma_f32_16x16x32_bf16 v[100:103],a[8:11],a[60:63],v[100:103]
s_mov_b32 s72,40960
ds_read_b128 a[48:51],v247 offset:53248
v_mfma_f32_16x16x32_bf16 v[140:143],a[12:15],a[60:63],v[140:143]
buffer_store_dwordx4 a[160:163],v253,s[36:39],s72 offen sc1
v_mfma_f32_16x16x32_bf16 v[180:183],a[16:19],a[60:63],v[180:183]
ds_read_b128 a[52:55],v247 offset:54272
ds_read_b128 a[56:59],v247 offset:55296
v_mfma_f32_16x16x32_bf16 v[24:27],a[0:3],a[64:67],v[24:27]
ds_read_b64_tr_b16 a[20:21],v230 offset:20480
v_mfma_f32_16x16x32_bf16 v[64:67],a[4:7],a[64:67],v[64:67]
ds_read_b64_tr_b16 a[22:23],v230 offset:30720
v_mfma_f32_16x16x32_bf16 v[104:107],a[8:11],a[64:67],v[104:107]
s_mov_b32 s72,41984
ds_read_b64_tr_b16 a[24:25],v231 offset:20480
v_mfma_f32_16x16x32_bf16 v[144:147],a[12:15],a[64:67],v[144:147]
buffer_store_dwordx4 a[164:167],v253,s[36:39],s72 offen sc1
v_mfma_f32_16x16x32_bf16 v[184:187],a[16:19],a[64:67],v[184:187]
ds_read_b64_tr_b16 a[26:27],v231 offset:30720
ds_read_b64_tr_b16 a[28:29],v232 offset:20480
v_mfma_f32_16x16x32_bf16 v[28:31],a[0:3],a[68:71],v[28:31]
ds_read_b64_tr_b16 a[30:31],v232 offset:30720
v_mfma_f32_16x16x32_bf16 v[68:71],a[4:7],a[68:71],v[68:71]
ds_read_b64_tr_b16 a[32:33],v233 offset:20480
v_mfma_f32_16x16x32_bf16 v[108:111],a[8:11],a[68:71],v[108:111]
s_mov_b32 s72,43008
ds_read_b64_tr_b16 a[34:35],v233 offset:30720
v_mfma_f32_16x16x32_bf16 v[148:151],a[12:15],a[68:71],v[148:151]
buffer_store_dwordx4 a[168:171],v253,s[36:39],s72 offen sc1
v_mfma_f32_16x16x32_bf16 v[188:191],a[16:19],a[68:71],v[188:191]
ds_read_b64_tr_b16 a[36:37],v234 offset:20480
ds_read_b64_tr_b16 a[38:39],v234 offset:30720
v_mfma_f32_16x16x32_bf16 v[32:35],a[0:3],a[72:75],v[32:35]
v_mfma_f32_16x16x32_bf16 v[72:75],a[4:7],a[72:75],v[72:75]
v_mfma_f32_16x16x32_bf16 v[112:115],a[8:11],a[72:75],v[112:115]
s_mov_b32 s72,44032
v_mfma_f32_16x16x32_bf16 v[152:155],a[12:15],a[72:75],v[152:155]
buffer_store_dwordx4 a[172:175],v253,s[36:39],s72 offen sc1
v_mfma_f32_16x16x32_bf16 v[192:195],a[16:19],a[72:75],v[192:195]
v_mfma_f32_16x16x32_bf16 v[36:39],a[0:3],a[76:79],v[36:39]
v_mfma_f32_16x16x32_bf16 v[76:79],a[4:7],a[76:79],v[76:79]
v_mfma_f32_16x16x32_bf16 v[116:119],a[8:11],a[76:79],v[116:119]
v_mfma_f32_16x16x32_bf16 v[156:159],a[12:15],a[76:79],v[156:159]
v_mfma_f32_16x16x32_bf16 v[196:199],a[16:19],a[76:79],v[196:199]
s_waitcnt lgkmcnt(0)
v_mfma_f32_16x16x32_bf16 v[0:3],a[20:23],a[40:43],v[0:3]
v_mfma_f32_16x16x32_bf16 v[40:43],a[24:27],a[40:43],v[40:43]
ds_read_b128 a[60:63],v247 offset:56320
v_mfma_f32_16x16x32_bf16 v[80:83],a[28:31],a[40:43],v[80:83]
v_mfma_f32_16x16x32_bf16 v[120:123],a[32:35],a[40:43],v[120:123]
ds_read_b128 a[64:67],v247 offset:57344
v_mfma_f32_16x16x32_bf16 v[160:163],a[36:39],a[40:43],v[160:163]
v_mfma_f32_16x16x32_bf16 v[4:7],a[20:23],a[44:47],v[4:7]
ds_read_b128 a[68:71],v247 offset:58368
v_mfma_f32_16x16x32_bf16 v[44:47],a[24:27],a[44:47],v[44:47]
v_mfma_f32_16x16x32_bf16 v[84:87],a[28:31],a[44:47],v[84:87]
ds_read_b128 a[72:75],v247 offset:59392
v_mfma_f32_16x16x32_bf16 v[124:127],a[32:35],a[44:47],v[124:127]
v_mfma_f32_16x16x32_bf16 v[164:167],a[36:39],a[44:47],v[164:167]
s_add_u32 m0,s58,61440
ds_read_b128 a[76:79],v247 offset:60416
v_mfma_f32_16x16x32_bf16 v[8:11],a[20:23],a[48:51],v[8:11]
buffer_load_dwordx4 v240,s[24:27],s52 offen lds
v_mfma_f32_16x16x32_bf16 v[48:51],a[24:27],a[48:51],v[48:51]
v_mfma_f32_16x16x32_bf16 v[88:91],a[28:31],a[48:51],v[88:91]
s_add_u32 m0,s58,61440
v_mfma_f32_16x16x32_bf16 v[128:131],a[32:35],a[48:51],v[128:131]
buffer_load_dwordx4 v241,s[24:27],s52 offen offset:1024 lds
v_mfma_f32_16x16x32_bf16 v[168:171],a[36:39],a[48:51],v[168:171]
v_mfma_f32_16x16x32_bf16 v[12:15],a[20:23],a[52:55],v[12:15]
s_add_u32 m0,s58,61440
v_mfma_f32_16x16x32_bf16 v[52:55],a[24:27],a[52:55],v[52:55]
buffer_load_dwordx4 v242,s[24:27],s52 offen offset:2048 lds
v_mfma_f32_16x16x32_bf16 v[92:95],a[28:31],a[52:55],v[92:95]
v_mfma_f32_16x16x32_bf16 v[132:135],a[32:35],a[52:55],v[132:135]
s_add_u32 m0,s58,61440
v_mfma_f32_16x16x32_bf16 v[172:175],a[36:39],a[52:55],v[172:175]
buffer_load_dwordx4 v243,s[24:27],s52 offen offset:3072 lds
v_mfma_f32_16x16x32_bf16 v[16:19],a[20:23],a[56:59],v[16:19]
v_mfma_f32_16x16x32_bf16 v[56:59],a[24:27],a[56:59],v[56:59]
s_add_u32 m0,s58,65536
v_mfma_f32_16x16x32_bf16 v[96:99],a[28:31],a[56:59],v[96:99]
buffer_load_dwordx4 v244,s[24:27],s52 offen lds
v_mfma_f32_16x16x32_bf16 v[136:139],a[32:35],a[56:59],v[136:139]
s_add_u32 s52,s52,40960
v_mfma_f32_16x16x32_bf16 v[176:179],a[36:39],a[56:59],v[176:179]
s_waitcnt vmcnt(17) lgkmcnt(0)
s_barrier
v_mfma_f32_16x16x32_bf16 v[20:23],a[20:23],a[60:63],v[20:23]
ds_read_b128 a[40:43],v247
v_mfma_f32_16x16x32_bf16 v[60:63],a[24:27],a[60:63],v[60:63]
ds_read_b128 a[44:47],v247 offset:1024
v_mfma_f32_16x16x32_bf16 v[100:103],a[28:31],a[60:63],v[100:103]
s_add_u32 m0,s58,0
ds_read_b128 a[48:51],v247 offset:2048
v_mfma_f32_16x16x32_bf16 v[140:143],a[32:35],a[60:63],v[140:143]
buffer_load_dwordx4 v240,s[24:27],s52 offen lds
v_mfma_f32_16x16x32_bf16 v[180:183],a[36:39],a[60:63],v[180:183]
ds_read_b128 a[52:55],v247 offset:3072
ds_read_b128 a[56:59],v247 offset:4096
v_mfma_f32_16x16x32_bf16 v[24:27],a[20:23],a[64:67],v[24:27]
s_add_u32 m0,s58,0
ds_read_b64_tr_b16 a[0:1],v235
v_mfma_f32_16x16x32_bf16 v[64:67],a[24:27],a[64:67],v[64:67]
buffer_load_dwordx4 v241,s[24:27],s52 offen offset:1024 lds
v_mfma_f32_16x16x32_bf16 v[104:107],a[28:31],a[64:67],v[104:107]
ds_read_b64_tr_b16 a[2:3],v235 offset:10240
ds_read_b64_tr_b16 a[4:5],v236
v_mfma_f32_16x16x32_bf16 v[144:147],a[32:35],a[64:67],v[144:147]
s_add_u32 m0,s58,0
ds_read_b64_tr_b16 a[6:7],v236 offset:10240
v_mfma_f32_16x16x32_bf16 v[184:187],a[36:39],a[64:67],v[184:187]
buffer_load_dwordx4 v242,s[24:27],s52 offen offset:2048 lds
v_mfma_f32_16x16x32_bf16 v[28:31],a[20:23],a[68:71],v[28:31]
ds_read_b64_tr_b16 a[8:9],v237
ds_read_b64_tr_b16 a[10:11],v237 offset:10240
v_mfma_f32_16x16x32_bf16 v[68:71],a[24:27],a[68:71],v[68:71]
s_add_u32 m0,s58,0
ds_read_b64_tr_b16 a[12:13],v238
v_mfma_f32_16x16x32_bf16 v[108:111],a[28:31],a[68:71],v[108:111]
buffer_load_dwordx4 v243,s[24:27],s52 offen offset:3072 lds
v_mfma_f32_16x16x32_bf16 v[148:151],a[32:35],a[68:71],v[148:151]
ds_read_b64_tr_b16 a[14:15],v238 offset:10240
ds_read_b64_tr_b16 a[16:17],v239
v_mfma_f32_16x16x32_bf16 v[188:191],a[36:39],a[68:71],v[188:191]
s_add_u32 m0,s58,4096
ds_read_b64_tr_b16 a[18:19],v239 offset:10240
v_mfma_f32_16x16x32_bf16 v[32:35],a[20:23],a[72:75],v[32:35]
buffer_load_dwordx4 v244,s[24:27],s52 offen lds
v_mfma_f32_16x16x32_bf16 v[72:75],a[24:27],a[72:75],v[72:75]
s_add_u32 s52,s52,40960
v_mfma_f32_16x16x32_bf16 v[112:115],a[28:31],a[72:75],v[112:115]
v_mfma_f32_16x16x32_bf16 v[152:155],a[32:35],a[72:75],v[152:155]
v_mfma_f32_16x16x32_bf16 v[192:195],a[36:39],a[72:75],v[192:195]
v_mfma_f32_16x16x32_bf16 v[36:39],a[20:23],a[76:79],v[36:39]
v_mfma_f32_16x16x32_bf16 v[76:79],a[24:27],a[76:79],v[76:79]
v_mfma_f32_16x16x32_bf16 v[116:119],a[28:31],a[76:79],v[116:119]
v_mfma_f32_16x16x32_bf16 v[156:159],a[32:35],a[76:79],v[156:159]
v_mfma_f32_16x16x32_bf16 v[196:199],a[36:39],a[76:79],v[196:199]
s_waitcnt lgkmcnt(0)
v_mfma_f32_16x16x32_bf16 v[0:3],a[0:3],a[40:43],v[0:3]
v_mfma_f32_16x16x32_bf16 v[40:43],a[4:7],a[40:43],v[40:43]
buffer_load_dword a250,v254,s[28:31],s54 offen
v_mfma_f32_16x16x32_bf16 v[80:83],a[8:11],a[40:43],v[80:83]
s_add_u32 m0,s59,40960
ds_read_b128 a[60:63],v247 offset:5120
v_mfma_f32_16x16x32_bf16 v[120:123],a[12:15],a[40:43],v[120:123]
buffer_load_dwordx4 v245,s[20:23],s53 offen sc1 lds
v_mfma_f32_16x16x32_bf16 v[160:163],a[16:19],a[40:43],v[160:163]
s_add_u32 m0,s59,40960
ds_read_b128 a[64:67],v247 offset:6144
v_mfma_f32_16x16x32_bf16 v[4:7],a[0:3],a[44:47],v[4:7]
buffer_load_dwordx4 v245,s[20:23],s53 offen offset:1024 sc1 lds
v_mfma_f32_16x16x32_bf16 v[44:47],a[4:7],a[44:47],v[44:47]
s_add_u32 m0,s59,40960
ds_read_b128 a[68:71],v247 offset:7168
v_mfma_f32_16x16x32_bf16 v[84:87],a[8:11],a[44:47],v[84:87]
buffer_load_dwordx4 v245,s[20:23],s53 offen offset:2048 sc1 lds
v_mfma_f32_16x16x32_bf16 v[124:127],a[12:15],a[44:47],v[124:127]
s_add_u32 m0,s59,40960
ds_read_b128 a[72:75],v247 offset:8192
v_mfma_f32_16x16x32_bf16 v[164:167],a[16:19],a[44:47],v[164:167]
buffer_load_dwordx4 v245,s[20:23],s53 offen offset:3072 sc1 lds
v_mfma_f32_16x16x32_bf16 v[8:11],a[0:3],a[48:51],v[8:11]
s_add_u32 m0,s59,45056
ds_read_b128 a[76:79],v247 offset:9216
v_mfma_f32_16x16x32_bf16 v[48:51],a[4:7],a[48:51],v[48:51]
s_add_u32 s67,s53,4096
v_mfma_f32_16x16x32_bf16 v[88:91],a[8:11],a[48:51],v[88:91]
buffer_load_dwordx4 v245,s[20:23],s67 offen sc1 lds
v_mfma_f32_16x16x32_bf16 v[128:131],a[12:15],a[48:51],v[128:131]
s_add_u32 s53,s53,s75
v_mfma_f32_16x16x32_bf16 v[168:171],a[16:19],a[48:51],v[168:171]
s_add_u32 s54,s54,81920
v_mfma_f32_16x16x32_bf16 v[12:15],a[0:3],a[52:55],v[12:15]
s_mov_b32 s72,45056
v_mfma_f32_16x16x32_bf16 v[52:55],a[4:7],a[52:55],v[52:55]
buffer_store_dwordx4 a[176:179],v253,s[36:39],s72 offen sc1
v_mfma_f32_16x16x32_bf16 v[92:95],a[8:11],a[52:55],v[92:95]
v_mfma_f32_16x16x32_bf16 v[132:135],a[12:15],a[52:55],v[132:135]
v_mfma_f32_16x16x32_bf16 v[172:175],a[16:19],a[52:55],v[172:175]
v_mfma_f32_16x16x32_bf16 v[16:19],a[0:3],a[56:59],v[16:19]
s_mov_b32 s72,51200
v_mfma_f32_16x16x32_bf16 v[56:59],a[4:7],a[56:59],v[56:59]
buffer_store_dwordx4 a[180:183],v253,s[36:39],s72 offen sc1
v_mfma_f32_16x16x32_bf16 v[96:99],a[8:11],a[56:59],v[96:99]
v_mfma_f32_16x16x32_bf16 v[136:139],a[12:15],a[56:59],v[136:139]
v_mfma_f32_16x16x32_bf16 v[176:179],a[16:19],a[56:59],v[176:179]
s_waitcnt vmcnt(13) lgkmcnt(0)
s_barrier
v_mfma_f32_16x16x32_bf16 v[20:23],a[0:3],a[60:63],v[20:23]
ds_read_b128 a[40:43],v247 offset:10240
v_mfma_f32_16x16x32_bf16 v[60:63],a[4:7],a[60:63],v[60:63]
ds_read_b128 a[44:47],v247 offset:11264
v_mfma_f32_16x16x32_bf16 v[100:103],a[8:11],a[60:63],v[100:103]
s_mov_b32 s72,52224
ds_read_b128 a[48:51],v247 offset:12288
v_mfma_f32_16x16x32_bf16 v[140:143],a[12:15],a[60:63],v[140:143]
buffer_store_dwordx4 a[184:187],v253,s[36:39],s72 offen sc1
v_mfma_f32_16x16x32_bf16 v[180:183],a[16:19],a[60:63],v[180:183]
ds_read_b128 a[52:55],v247 offset:13312
ds_read_b128 a[56:59],v247 offset:14336
v_mfma_f32_16x16x32_bf16 v[24:27],a[0:3],a[64:67],v[24:27]
ds_read_b64_tr_b16 a[20:21],v235 offset:20480
v_mfma_f32_16x16x32_bf16 v[64:67],a[4:7],a[64:67],v[64:67]
ds_read_b64_tr_b16 a[22:23],v235 offset:30720
v_mfma_f32_16x16x32_bf16 v[104:107],a[8:11],a[64:67],v[104:107]
s_mov_b32 s72,53248
ds_read_b64_tr_b16 a[24:25],v236 offset:20480
v_mfma_f32_16x16x32_bf16 v[144:147],a[12:15],a[64:67],v[144:147]
buffer_store_dwordx4 a[188:191],v253,s[36:39],s72 offen sc1
v_mfma_f32_16x16x32_bf16 v[184:187],a[16:19],a[64:67],v[184:187]
ds_read_b64_tr_b16 a[26:27],v236 offset:30720
ds_read_b64_tr_b16 a[28:29],v237 offset:20480
v_mfma_f32_16x16x32_bf16 v[28:31],a[0:3],a[68:71],v[28:31]
ds_read_b64_tr_b16 a[30:31],v237 offset:30720
v_mfma_f32_16x16x32_bf16 v[68:71],a[4:7],a[68:71],v[68:71]
ds_read_b64_tr_b16 a[32:33],v238 offset:20480
v_mfma_f32_16x16x32_bf16 v[108:111],a[8:11],a[68:71],v[108:111]
s_mov_b32 s72,54272
ds_read_b64_tr_b16 a[34:35],v238 offset:30720
v_mfma_f32_16x16x32_bf16 v[148:151],a[12:15],a[68:71],v[148:151]
buffer_store_dwordx4 a[192:195],v253,s[36:39],s72 offen sc1
v_mfma_f32_16x16x32_bf16 v[188:191],a[16:19],a[68:71],v[188:191]
ds_read_b64_tr_b16 a[36:37],v239 offset:20480
ds_read_b64_tr_b16 a[38:39],v239 offset:30720
v_mfma_f32_16x16x32_bf16 v[32:35],a[0:3],a[72:75],v[32:35]
s_waitcnt vmcnt(27)
v_readfirstlane_b32 s70,v246
v_mfma_f32_16x16x32_bf16 v[72:75],a[4:7],a[72:75],v[72:75]
v_mfma_f32_16x16x32_bf16 v[112:115],a[8:11],a[72:75],v[112:115]
s_mov_b32 s72,55296
v_mfma_f32_16x16x32_bf16 v[152:155],a[12:15],a[72:75],v[152:155]
buffer_store_dwordx4 a[196:199],v253,s[36:39],s72 offen sc1
v_mfma_f32_16x16x32_bf16 v[192:195],a[16:19],a[72:75],v[192:195]
s_cmp_eq_u32 s70,0
s_cbranch_scc1 LOSPIN1_%=
LOBACK1_%=:
v_mfma_f32_16x16x32_bf16 v[36:39],a[0:3],a[76:79],v[36:39]
v_mfma_f32_16x16x32_bf16 v[76:79],a[4:7],a[76:79],v[76:79]
v_mfma_f32_16x16x32_bf16 v[116:119],a[8:11],a[76:79],v[116:119]
v_mfma_f32_16x16x32_bf16 v[156:159],a[12:15],a[76:79],v[156:159]
v_mfma_f32_16x16x32_bf16 v[196:199],a[16:19],a[76:79],v[196:199]
s_waitcnt lgkmcnt(0)
v_mfma_f32_16x16x32_bf16 v[0:3],a[20:23],a[40:43],v[0:3]
v_mfma_f32_16x16x32_bf16 v[40:43],a[24:27],a[40:43],v[40:43]
ds_read_b128 a[60:63],v247 offset:15360
v_mfma_f32_16x16x32_bf16 v[80:83],a[28:31],a[40:43],v[80:83]
v_mfma_f32_16x16x32_bf16 v[120:123],a[32:35],a[40:43],v[120:123]
ds_read_b128 a[64:67],v247 offset:16384
v_mfma_f32_16x16x32_bf16 v[160:163],a[36:39],a[40:43],v[160:163]
v_mfma_f32_16x16x32_bf16 v[4:7],a[20:23],a[44:47],v[4:7]
ds_read_b128 a[68:71],v247 offset:17408
v_mfma_f32_16x16x32_bf16 v[44:47],a[24:27],a[44:47],v[44:47]
v_mfma_f32_16x16x32_bf16 v[84:87],a[28:31],a[44:47],v[84:87]
ds_read_b128 a[72:75],v247 offset:18432
v_mfma_f32_16x16x32_bf16 v[124:127],a[32:35],a[44:47],v[124:127]
v_mfma_f32_16x16x32_bf16 v[164:167],a[36:39],a[44:47],v[164:167]
s_add_u32 m0,s58,20480
ds_read_b128 a[76:79],v247 offset:19456
v_mfma_f32_16x16x32_bf16 v[8:11],a[20:23],a[48:51],v[8:11]
buffer_load_dwordx4 v240,s[24:27],s52 offen lds
v_mfma_f32_16x16x32_bf16 v[48:51],a[24:27],a[48:51],v[48:51]
v_mfma_f32_16x16x32_bf16 v[88:91],a[28:31],a[48:51],v[88:91]
s_add_u32 m0,s58,20480
v_mfma_f32_16x16x32_bf16 v[128:131],a[32:35],a[48:51],v[128:131]
buffer_load_dwordx4 v241,s[24:27],s52 offen offset:1024 lds
v_mfma_f32_16x16x32_bf16 v[168:171],a[36:39],a[48:51],v[168:171]
v_mfma_f32_16x16x32_bf16 v[12:15],a[20:23],a[52:55],v[12:15]
s_add_u32 m0,s58,20480
v_mfma_f32_16x16x32_bf16 v[52:55],a[24:27],a[52:55],v[52:55]
buffer_load_dwordx4 v242,s[24:27],s52 offen offset:2048 lds
v_mfma_f32_16x16x32_bf16 v[92:95],a[28:31],a[52:55],v[92:95]
v_mfma_f32_16x16x32_bf16 v[132:135],a[32:35],a[52:55],v[132:135]
s_add_u32 m0,s58,20480
v_mfma_f32_16x16x32_bf16 v[172:175],a[36:39],a[52:55],v[172:175]
buffer_load_dwordx4 v243,s[24:27],s52 offen offset:3072 lds
v_mfma_f32_16x16x32_bf16 v[16:19],a[20:23],a[56:59],v[16:19]
v_mfma_f32_16x16x32_bf16 v[56:59],a[24:27],a[56:59],v[56:59]
s_add_u32 m0,s58,24576
v_mfma_f32_16x16x32_bf16 v[96:99],a[28:31],a[56:59],v[96:99]
buffer_load_dwordx4 v244,s[24:27],s52 offen lds
v_mfma_f32_16x16x32_bf16 v[136:139],a[32:35],a[56:59],v[136:139]
s_add_u32 s52,s52,40960
v_mfma_f32_16x16x32_bf16 v[176:179],a[36:39],a[56:59],v[176:179]
s_waitcnt vmcnt(17) lgkmcnt(0)
s_barrier
v_mfma_f32_16x16x32_bf16 v[20:23],a[20:23],a[60:63],v[20:23]
ds_read_b128 a[40:43],v247 offset:20480
v_mfma_f32_16x16x32_bf16 v[60:63],a[24:27],a[60:63],v[60:63]
ds_read_b128 a[44:47],v247 offset:21504
v_mfma_f32_16x16x32_bf16 v[100:103],a[28:31],a[60:63],v[100:103]
s_add_u32 m0,s58,40960
ds_read_b128 a[48:51],v247 offset:22528
v_mfma_f32_16x16x32_bf16 v[140:143],a[32:35],a[60:63],v[140:143]
buffer_load_dwordx4 v240,s[24:27],s52 offen lds
v_mfma_f32_16x16x32_bf16 v[180:183],a[36:39],a[60:63],v[180:183]
ds_read_b128 a[52:55],v247 offset:23552
ds_read_b128 a[56:59],v247 offset:24576
v_mfma_f32_16x16x32_bf16 v[24:27],a[20:23],a[64:67],v[24:27]
s_add_u32 m0,s58,40960
ds_read_b64_tr_b16 a[0:1],v230
v_mfma_f32_16x16x32_bf16 v[64:67],a[24:27],a[64:67],v[64:67]
buffer_load_dwordx4 v241,s[24:27],s52 offen offset:1024 lds
v_mfma_f32_16x16x32_bf16 v[104:107],a[28:31],a[64:67],v[104:107]
ds_read_b64_tr_b16 a[2:3],v230 offset:10240
ds_read_b64_tr_b16 a[4:5],v231
v_mfma_f32_16x16x32_bf16 v[144:147],a[32:35],a[64:67],v[144:147]
s_add_u32 m0,s58,40960
ds_read_b64_tr_b16 a[6:7],v231 offset:10240
v_mfma_f32_16x16x32_bf16 v[184:187],a[36:39],a[64:67],v[184:187]
buffer_load_dwordx4 v242,s[24:27],s52 offen offset:2048 lds
v_mfma_f32_16x16x32_bf16 v[28:31],a[20:23],a[68:71],v[28:31]
ds_read_b64_tr_b16 a[8:9],v232
ds_read_b64_tr_b16 a[10:11],v232 offset:10240
v_mfma_f32_16x16x32_bf16 v[68:71],a[24:27],a[68:71],v[68:71]
s_add_u32 m0,s58,40960
ds_read_b64_tr_b16 a[12:13],v233
v_mfma_f32_16x16x32_bf16 v[108:111],a[28:31],a[68:71],v[108:111]
buffer_load_dwordx4 v243,s[24:27],s52 offen offset:3072 lds
v_mfma_f32_16x16x32_bf16 v[148:151],a[32:35],a[68:71],v[148:151]
ds_read_b64_tr_b16 a[14:15],v233 offset:10240
ds_read_b64_tr_b16 a[16:17],v234
v_mfma_f32_16x16x32_bf16 v[188:191],a[36:39],a[68:71],v[188:191]
s_add_u32 m0,s58,45056
ds_read_b64_tr_b16 a[18:19],v234 offset:10240
v_mfma_f32_16x16x32_bf16 v[32:35],a[20:23],a[72:75],v[32:35]
buffer_load_dwordx4 v244,s[24:27],s52 offen lds
v_mfma_f32_16x16x32_bf16 v[72:75],a[24:27],a[72:75],v[72:75]
s_add_u32 s52,s52,40960
v_mfma_f32_16x16x32_bf16 v[112:115],a[28:31],a[72:75],v[112:115]
v_mfma_f32_16x16x32_bf16 v[152:155],a[32:35],a[72:75],v[152:155]
v_mfma_f32_16x16x32_bf16 v[192:195],a[36:39],a[72:75],v[192:195]
v_mfma_f32_16x16x32_bf16 v[36:39],a[20:23],a[76:79],v[36:39]
v_mfma_f32_16x16x32_bf16 v[76:79],a[24:27],a[76:79],v[76:79]
v_mfma_f32_16x16x32_bf16 v[116:119],a[28:31],a[76:79],v[116:119]
v_mfma_f32_16x16x32_bf16 v[156:159],a[32:35],a[76:79],v[156:159]
v_mfma_f32_16x16x32_bf16 v[196:199],a[36:39],a[76:79],v[196:199]
s_waitcnt lgkmcnt(0)
v_mfma_f32_16x16x32_bf16 v[0:3],a[0:3],a[40:43],v[0:3]
v_mfma_f32_16x16x32_bf16 v[40:43],a[4:7],a[40:43],v[40:43]
buffer_load_dword a250,v254,s[28:31],s54 offen
v_mfma_f32_16x16x32_bf16 v[80:83],a[8:11],a[40:43],v[80:83]
s_add_u32 m0,s59,0
ds_read_b128 a[60:63],v247 offset:25600
v_mfma_f32_16x16x32_bf16 v[120:123],a[12:15],a[40:43],v[120:123]
buffer_load_dwordx4 v245,s[20:23],s53 offen sc1 lds
v_mfma_f32_16x16x32_bf16 v[160:163],a[16:19],a[40:43],v[160:163]
s_add_u32 m0,s59,0
ds_read_b128 a[64:67],v247 offset:26624
v_mfma_f32_16x16x32_bf16 v[4:7],a[0:3],a[44:47],v[4:7]
buffer_load_dwordx4 v245,s[20:23],s53 offen offset:1024 sc1 lds
v_mfma_f32_16x16x32_bf16 v[44:47],a[4:7],a[44:47],v[44:47]
s_add_u32 m0,s59,0
ds_read_b128 a[68:71],v247 offset:27648
v_mfma_f32_16x16x32_bf16 v[84:87],a[8:11],a[44:47],v[84:87]
buffer_load_dwordx4 v245,s[20:23],s53 offen offset:2048 sc1 lds
v_mfma_f32_16x16x32_bf16 v[124:127],a[12:15],a[44:47],v[124:127]
s_add_u32 m0,s59,0
ds_read_b128 a[72:75],v247 offset:28672
v_mfma_f32_16x16x32_bf16 v[164:167],a[16:19],a[44:47],v[164:167]
buffer_load_dwordx4 v245,s[20:23],s53 offen offset:3072 sc1 lds
v_mfma_f32_16x16x32_bf16 v[8:11],a[0:3],a[48:51],v[8:11]
s_add_u32 m0,s59,4096
ds_read_b128 a[76:79],v247 offset:29696
v_mfma_f32_16x16x32_bf16 v[48:51],a[4:7],a[48:51],v[48:51]
s_add_u32 s67,s53,4096
v_mfma_f32_16x16x32_bf16 v[88:91],a[8:11],a[48:51],v[88:91]
buffer_load_dwordx4 v245,s[20:23],s67 offen sc1 lds
v_mfma_f32_16x16x32_bf16 v[128:131],a[12:15],a[48:51],v[128:131]
s_add_u32 s53,s53,20480
v_mfma_f32_16x16x32_bf16 v[168:171],a[16:19],a[48:51],v[168:171]
s_add_u32 s54,s54,81920
v_mfma_f32_16x16x32_bf16 v[12:15],a[0:3],a[52:55],v[12:15]
v_mfma_f32_16x16x32_bf16 v[52:55],a[4:7],a[52:55],v[52:55]
v_mfma_f32_16x16x32_bf16 v[92:95],a[8:11],a[52:55],v[92:95]
v_mfma_f32_16x16x32_bf16 v[132:135],a[12:15],a[52:55],v[132:135]
v_mfma_f32_16x16x32_bf16 v[172:175],a[16:19],a[52:55],v[172:175]
v_mfma_f32_16x16x32_bf16 v[16:19],a[0:3],a[56:59],v[16:19]
v_mfma_f32_16x16x32_bf16 v[56:59],a[4:7],a[56:59],v[56:59]
v_mfma_f32_16x16x32_bf16 v[96:99],a[8:11],a[56:59],v[96:99]
v_mfma_f32_16x16x32_bf16 v[136:139],a[12:15],a[56:59],v[136:139]
v_mfma_f32_16x16x32_bf16 v[176:179],a[16:19],a[56:59],v[176:179]
s_waitcnt vmcnt(11) lgkmcnt(0)
s_barrier
v_mfma_f32_16x16x32_bf16 v[20:23],a[0:3],a[60:63],v[20:23]
ds_read_b128 a[40:43],v247 offset:30720
v_mfma_f32_16x16x32_bf16 v[60:63],a[4:7],a[60:63],v[60:63]
ds_read_b128 a[44:47],v247 offset:31744
v_mfma_f32_16x16x32_bf16 v[100:103],a[8:11],a[60:63],v[100:103]
ds_read_b128 a[48:51],v247 offset:32768
v_mfma_f32_16x16x32_bf16 v[140:143],a[12:15],a[60:63],v[140:143]
ds_read_b128 a[52:55],v247 offset:33792
v_mfma_f32_16x16x32_bf16 v[180:183],a[16:19],a[60:63],v[180:183]
ds_read_b128 a[56:59],v247 offset:34816
v_mfma_f32_16x16x32_bf16 v[24:27],a[0:3],a[64:67],v[24:27]
ds_read_b64_tr_b16 a[20:21],v230 offset:20480
v_mfma_f32_16x16x32_bf16 v[64:67],a[4:7],a[64:67],v[64:67]
ds_read_b64_tr_b16 a[22:23],v230 offset:30720
v_mfma_f32_16x16x32_bf16 v[104:107],a[8:11],a[64:67],v[104:107]
ds_read_b64_tr_b16 a[24:25],v231 offset:20480
v_mfma_f32_16x16x32_bf16 v[144:147],a[12:15],a[64:67],v[144:147]
ds_read_b64_tr_b16 a[26:27],v231 offset:30720
v_mfma_f32_16x16x32_bf16 v[184:187],a[16:19],a[64:67],v[184:187]
ds_read_b64_tr_b16 a[28:29],v232 offset:20480
v_mfma_f32_16x16x32_bf16 v[28:31],a[0:3],a[68:71],v[28:31]
ds_read_b64_tr_b16 a[30:31],v232 offset:30720
v_mfma_f32_16x16x32_bf16 v[68:71],a[4:7],a[68:71],v[68:71]
ds_read_b64_tr_b16 a[32:33],v233 offset:20480
v_mfma_f32_16x16x32_bf16 v[108:111],a[8:11],a[68:71],v[108:111]
ds_read_b64_tr_b16 a[34:35],v233 offset:30720
v_mfma_f32_16x16x32_bf16 v[148:151],a[12:15],a[68:71],v[148:151]
ds_read_b64_tr_b16 a[36:37],v234 offset:20480
v_mfma_f32_16x16x32_bf16 v[188:191],a[16:19],a[68:71],v[188:191]
ds_read_b64_tr_b16 a[38:39],v234 offset:30720
v_mfma_f32_16x16x32_bf16 v[32:35],a[0:3],a[72:75],v[32:35]
v_mfma_f32_16x16x32_bf16 v[72:75],a[4:7],a[72:75],v[72:75]
v_mfma_f32_16x16x32_bf16 v[112:115],a[8:11],a[72:75],v[112:115]
v_mfma_f32_16x16x32_bf16 v[152:155],a[12:15],a[72:75],v[152:155]
v_mfma_f32_16x16x32_bf16 v[192:195],a[16:19],a[72:75],v[192:195]
v_mfma_f32_16x16x32_bf16 v[36:39],a[0:3],a[76:79],v[36:39]
v_mfma_f32_16x16x32_bf16 v[76:79],a[4:7],a[76:79],v[76:79]
v_mfma_f32_16x16x32_bf16 v[116:119],a[8:11],a[76:79],v[116:119]
v_mfma_f32_16x16x32_bf16 v[156:159],a[12:15],a[76:79],v[156:159]
v_mfma_f32_16x16x32_bf16 v[196:199],a[16:19],a[76:79],v[196:199]
s_waitcnt lgkmcnt(0)
v_mfma_f32_16x16x32_bf16 v[0:3],a[20:23],a[40:43],v[0:3]
v_mfma_f32_16x16x32_bf16 v[40:43],a[24:27],a[40:43],v[40:43]
ds_read_b128 a[60:63],v247 offset:35840
v_mfma_f32_16x16x32_bf16 v[80:83],a[28:31],a[40:43],v[80:83]
v_mfma_f32_16x16x32_bf16 v[120:123],a[32:35],a[40:43],v[120:123]
ds_read_b128 a[64:67],v247 offset:36864
v_mfma_f32_16x16x32_bf16 v[160:163],a[36:39],a[40:43],v[160:163]
v_mfma_f32_16x16x32_bf16 v[4:7],a[20:23],a[44:47],v[4:7]
ds_read_b128 a[68:71],v247 offset:37888
v_mfma_f32_16x16x32_bf16 v[44:47],a[24:27],a[44:47],v[44:47]
v_mfma_f32_16x16x32_bf16 v[84:87],a[28:31],a[44:47],v[84:87]
ds_read_b128 a[72:75],v247 offset:38912
v_mfma_f32_16x16x32_bf16 v[124:127],a[32:35],a[44:47],v[124:127]
v_mfma_f32_16x16x32_bf16 v[164:167],a[36:39],a[44:47],v[164:167]
s_add_u32 m0,s58,61440
ds_read_b128 a[76:79],v247 offset:39936
v_mfma_f32_16x16x32_bf16 v[8:11],a[20:23],a[48:51],v[8:11]
buffer_load_dwordx4 v240,s[24:27],s52 offen lds
v_mfma_f32_16x16x32_bf16 v[48:51],a[24:27],a[48:51],v[48:51]
v_mfma_f32_16x16x32_bf16 v[88:91],a[28:31],a[48:51],v[88:91]
s_add_u32 m0,s58,61440
v_mfma_f32_16x16x32_bf16 v[128:131],a[32:35],a[48:51],v[128:131]
buffer_load_dwordx4 v241,s[24:27],s52 offen offset:1024 lds
v_mfma_f32_16x16x32_bf16 v[168:171],a[36:39],a[48:51],v[168:171]
v_mfma_f32_16x16x32_bf16 v[12:15],a[20:23],a[52:55],v[12:15]
s_add_u32 m0,s58,61440
v_mfma_f32_16x16x32_bf16 v[52:55],a[24:27],a[52:55],v[52:55]
buffer_load_dwordx4 v242,s[24:27],s52 offen offset:2048 lds
v_mfma_f32_16x16x32_bf16 v[92:95],a[28:31],a[52:55],v[92:95]
v_mfma_f32_16x16x32_bf16 v[132:135],a[32:35],a[52:55],v[132:135]
s_add_u32 m0,s58,61440
v_mfma_f32_16x16x32_bf16 v[172:175],a[36:39],a[52:55],v[172:175]
buffer_load_dwordx4 v243,s[24:27],s52 offen offset:3072 lds
v_mfma_f32_16x16x32_bf16 v[16:19],a[20:23],a[56:59],v[16:19]
v_mfma_f32_16x16x32_bf16 v[56:59],a[24:27],a[56:59],v[56:59]
s_add_u32 m0,s58,65536
v_mfma_f32_16x16x32_bf16 v[96:99],a[28:31],a[56:59],v[96:99]
buffer_load_dwordx4 v244,s[24:27],s52 offen lds
v_mfma_f32_16x16x32_bf16 v[136:139],a[32:35],a[56:59],v[136:139]
s_add_u32 s52,s52,s74
v_mfma_f32_16x16x32_bf16 v[176:179],a[36:39],a[56:59],v[176:179]
s_waitcnt vmcnt(11) lgkmcnt(0)
s_barrier
v_mfma_f32_16x16x32_bf16 v[20:23],a[20:23],a[60:63],v[20:23]
ds_read_b128 a[40:43],v247 offset:40960
v_mfma_f32_16x16x32_bf16 v[60:63],a[24:27],a[60:63],v[60:63]
ds_read_b128 a[44:47],v247 offset:41984
v_mfma_f32_16x16x32_bf16 v[100:103],a[28:31],a[60:63],v[100:103]
s_add_u32 m0,s58,0
ds_read_b128 a[48:51],v247 offset:43008
v_mfma_f32_16x16x32_bf16 v[140:143],a[32:35],a[60:63],v[140:143]
buffer_load_dwordx4 v240,s[24:27],s52 offen lds
v_mfma_f32_16x16x32_bf16 v[180:183],a[36:39],a[60:63],v[180:183]
ds_read_b128 a[52:55],v247 offset:44032
ds_read_b128 a[56:59],v247 offset:45056
v_mfma_f32_16x16x32_bf16 v[24:27],a[20:23],a[64:67],v[24:27]
s_add_u32 m0,s58,0
ds_read_b64_tr_b16 a[0:1],v235
v_mfma_f32_16x16x32_bf16 v[64:67],a[24:27],a[64:67],v[64:67]
buffer_load_dwordx4 v241,s[24:27],s52 offen offset:1024 lds
v_mfma_f32_16x16x32_bf16 v[104:107],a[28:31],a[64:67],v[104:107]
ds_read_b64_tr_b16 a[2:3],v235 offset:10240
ds_read_b64_tr_b16 a[4:5],v236
v_mfma_f32_16x16x32_bf16 v[144:147],a[32:35],a[64:67],v[144:147]
s_add_u32 m0,s58,0
ds_read_b64_tr_b16 a[6:7],v236 offset:10240
v_mfma_f32_16x16x32_bf16 v[184:187],a[36:39],a[64:67],v[184:187]
buffer_load_dwordx4 v242,s[24:27],s52 offen offset:2048 lds
v_mfma_f32_16x16x32_bf16 v[28:31],a[20:23],a[68:71],v[28:31]
ds_read_b64_tr_b16 a[8:9],v237
ds_read_b64_tr_b16 a[10:11],v237 offset:10240
v_mfma_f32_16x16x32_bf16 v[68:71],a[24:27],a[68:71],v[68:71]
s_add_u32 m0,s58,0
ds_read_b64_tr_b16 a[12:13],v238
v_mfma_f32_16x16x32_bf16 v[108:111],a[28:31],a[68:71],v[108:111]
buffer_load_dwordx4 v243,s[24:27],s52 offen offset:3072 lds
v_mfma_f32_16x16x32_bf16 v[148:151],a[32:35],a[68:71],v[148:151]
ds_read_b64_tr_b16 a[14:15],v238 offset:10240
ds_read_b64_tr_b16 a[16:17],v239
v_mfma_f32_16x16x32_bf16 v[188:191],a[36:39],a[68:71],v[188:191]
s_add_u32 m0,s58,4096
ds_read_b64_tr_b16 a[18:19],v239 offset:10240
v_mfma_f32_16x16x32_bf16 v[32:35],a[20:23],a[72:75],v[32:35]
buffer_load_dwordx4 v244,s[24:27],s52 offen lds
v_mfma_f32_16x16x32_bf16 v[72:75],a[24:27],a[72:75],v[72:75]
s_add_u32 s52,s52,40960
v_mfma_f32_16x16x32_bf16 v[112:115],a[28:31],a[72:75],v[112:115]
v_mfma_f32_16x16x32_bf16 v[152:155],a[32:35],a[72:75],v[152:155]
v_mfma_f32_16x16x32_bf16 v[192:195],a[36:39],a[72:75],v[192:195]
v_mfma_f32_16x16x32_bf16 v[36:39],a[20:23],a[76:79],v[36:39]
v_mfma_f32_16x16x32_bf16 v[76:79],a[24:27],a[76:79],v[76:79]
v_mfma_f32_16x16x32_bf16 v[116:119],a[28:31],a[76:79],v[116:119]
v_mfma_f32_16x16x32_bf16 v[156:159],a[32:35],a[76:79],v[156:159]
v_mfma_f32_16x16x32_bf16 v[196:199],a[36:39],a[76:79],v[196:199]
s_waitcnt lgkmcnt(0)
v_mfma_f32_16x16x32_bf16 v[0:3],a[0:3],a[40:43],v[0:3]
v_mfma_f32_16x16x32_bf16 v[40:43],a[4:7],a[40:43],v[40:43]
buffer_load_dword a250,v254,s[28:31],s54 offen
v_mfma_f32_16x16x32_bf16 v[80:83],a[8:11],a[40:43],v[80:83]
s_add_u32 m0,s59,20480
ds_read_b128 a[60:63],v247 offset:46080
v_mfma_f32_16x16x32_bf16 v[120:123],a[12:15],a[40:43],v[120:123]
buffer_load_dwordx4 v245,s[20:23],s53 offen sc1 lds
v_mfma_f32_16x16x32_bf16 v[160:163],a[16:19],a[40:43],v[160:163]
s_add_u32 m0,s59,20480
ds_read_b128 a[64:67],v247 offset:47104
v_mfma_f32_16x16x32_bf16 v[4:7],a[0:3],a[44:47],v[4:7]
buffer_load_dwordx4 v245,s[20:23],s53 offen offset:1024 sc1 lds
v_mfma_f32_16x16x32_bf16 v[44:47],a[4:7],a[44:47],v[44:47]
s_add_u32 m0,s59,20480
ds_read_b128 a[68:71],v247 offset:48128
v_mfma_f32_16x16x32_bf16 v[84:87],a[8:11],a[44:47],v[84:87]
buffer_load_dwordx4 v245,s[20:23],s53 offen offset:2048 sc1 lds
v_mfma_f32_16x16x32_bf16 v[124:127],a[12:15],a[44:47],v[124:127]
s_add_u32 m0,s59,20480
ds_read_b128 a[72:75],v247 offset:49152
v_mfma_f32_16x16x32_bf16 v[164:167],a[16:19],a[44:47],v[164:167]
buffer_load_dwordx4 v245,s[20:23],s53 offen offset:3072 sc1 lds
v_mfma_f32_16x16x32_bf16 v[8:11],a[0:3],a[48:51],v[8:11]
s_add_u32 m0,s59,24576
ds_read_b128 a[76:79],v247 offset:50176
v_mfma_f32_16x16x32_bf16 v[48:51],a[4:7],a[48:51],v[48:51]
s_add_u32 s67,s53,4096
v_mfma_f32_16x16x32_bf16 v[88:91],a[8:11],a[48:51],v[88:91]
buffer_load_dwordx4 v245,s[20:23],s67 offen sc1 lds
v_mfma_f32_16x16x32_bf16 v[128:131],a[12:15],a[48:51],v[128:131]
s_add_u32 s53,s53,20480
v_mfma_f32_16x16x32_bf16 v[168:171],a[16:19],a[48:51],v[168:171]
s_add_u32 s54,s54,81920
v_mfma_f32_16x16x32_bf16 v[12:15],a[0:3],a[52:55],v[12:15]
v_mfma_f32_16x16x32_bf16 v[52:55],a[4:7],a[52:55],v[52:55]
v_mfma_f32_16x16x32_bf16 v[92:95],a[8:11],a[52:55],v[92:95]
v_mfma_f32_16x16x32_bf16 v[132:135],a[12:15],a[52:55],v[132:135]
v_mfma_f32_16x16x32_bf16 v[172:175],a[16:19],a[52:55],v[172:175]
v_mfma_f32_16x16x32_bf16 v[16:19],a[0:3],a[56:59],v[16:19]
v_mfma_f32_16x16x32_bf16 v[56:59],a[4:7],a[56:59],v[56:59]
v_mfma_f32_16x16x32_bf16 v[96:99],a[8:11],a[56:59],v[96:99]
v_mfma_f32_16x16x32_bf16 v[136:139],a[12:15],a[56:59],v[136:139]
v_mfma_f32_16x16x32_bf16 v[176:179],a[16:19],a[56:59],v[176:179]
s_waitcnt vmcnt(11) lgkmcnt(0)
s_barrier
v_mfma_f32_16x16x32_bf16 v[20:23],a[0:3],a[60:63],v[20:23]
ds_read_b128 a[40:43],v247 offset:51200
v_mfma_f32_16x16x32_bf16 v[60:63],a[4:7],a[60:63],v[60:63]
ds_read_b128 a[44:47],v247 offset:52224
v_mfma_f32_16x16x32_bf16 v[100:103],a[8:11],a[60:63],v[100:103]
ds_read_b128 a[48:51],v247 offset:53248
v_mfma_f32_16x16x32_bf16 v[140:143],a[12:15],a[60:63],v[140:143]
ds_read_b128 a[52:55],v247 offset:54272
v_mfma_f32_16x16x32_bf16 v[180:183],a[16:19],a[60:63],v[180:183]
ds_read_b128 a[56:59],v247 offset:55296
v_mfma_f32_16x16x32_bf16 v[24:27],a[0:3],a[64:67],v[24:27]
ds_read_b64_tr_b16 a[20:21],v235 offset:20480
v_mfma_f32_16x16x32_bf16 v[64:67],a[4:7],a[64:67],v[64:67]
ds_read_b64_tr_b16 a[22:23],v235 offset:30720
v_mfma_f32_16x16x32_bf16 v[104:107],a[8:11],a[64:67],v[104:107]
ds_read_b64_tr_b16 a[24:25],v236 offset:20480
v_mfma_f32_16x16x32_bf16 v[144:147],a[12:15],a[64:67],v[144:147]
ds_read_b64_tr_b16 a[26:27],v236 offset:30720
v_mfma_f32_16x16x32_bf16 v[184:187],a[16:19],a[64:67],v[184:187]
ds_read_b64_tr_b16 a[28:29],v237 offset:20480
v_mfma_f32_16x16x32_bf16 v[28:31],a[0:3],a[68:71],v[28:31]
ds_read_b64_tr_b16 a[30:31],v237 offset:30720
v_mfma_f32_16x16x32_bf16 v[68:71],a[4:7],a[68:71],v[68:71]
ds_read_b64_tr_b16 a[32:33],v238 offset:20480
v_mfma_f32_16x16x32_bf16 v[108:111],a[8:11],a[68:71],v[108:111]
ds_read_b64_tr_b16 a[34:35],v238 offset:30720
v_mfma_f32_16x16x32_bf16 v[148:151],a[12:15],a[68:71],v[148:151]
ds_read_b64_tr_b16 a[36:37],v239 offset:20480
v_mfma_f32_16x16x32_bf16 v[188:191],a[16:19],a[68:71],v[188:191]
ds_read_b64_tr_b16 a[38:39],v239 offset:30720
v_mfma_f32_16x16x32_bf16 v[32:35],a[0:3],a[72:75],v[32:35]
v_mfma_f32_16x16x32_bf16 v[72:75],a[4:7],a[72:75],v[72:75]
v_mfma_f32_16x16x32_bf16 v[112:115],a[8:11],a[72:75],v[112:115]
v_mfma_f32_16x16x32_bf16 v[152:155],a[12:15],a[72:75],v[152:155]
v_mfma_f32_16x16x32_bf16 v[192:195],a[16:19],a[72:75],v[192:195]
v_mfma_f32_16x16x32_bf16 v[36:39],a[0:3],a[76:79],v[36:39]
v_mfma_f32_16x16x32_bf16 v[76:79],a[4:7],a[76:79],v[76:79]
v_mfma_f32_16x16x32_bf16 v[116:119],a[8:11],a[76:79],v[116:119]
v_mfma_f32_16x16x32_bf16 v[156:159],a[12:15],a[76:79],v[156:159]
v_mfma_f32_16x16x32_bf16 v[196:199],a[16:19],a[76:79],v[196:199]
s_waitcnt lgkmcnt(0)
v_mfma_f32_16x16x32_bf16 v[0:3],a[20:23],a[40:43],v[0:3]
v_mfma_f32_16x16x32_bf16 v[40:43],a[24:27],a[40:43],v[40:43]
ds_read_b128 a[60:63],v247 offset:56320
v_mfma_f32_16x16x32_bf16 v[80:83],a[28:31],a[40:43],v[80:83]
v_mfma_f32_16x16x32_bf16 v[120:123],a[32:35],a[40:43],v[120:123]
ds_read_b128 a[64:67],v247 offset:57344
v_mfma_f32_16x16x32_bf16 v[160:163],a[36:39],a[40:43],v[160:163]
v_mfma_f32_16x16x32_bf16 v[4:7],a[20:23],a[44:47],v[4:7]
ds_read_b128 a[68:71],v247 offset:58368
v_mfma_f32_16x16x32_bf16 v[44:47],a[24:27],a[44:47],v[44:47]
v_mfma_f32_16x16x32_bf16 v[84:87],a[28:31],a[44:47],v[84:87]
ds_read_b128 a[72:75],v247 offset:59392
v_mfma_f32_16x16x32_bf16 v[124:127],a[32:35],a[44:47],v[124:127]
v_mfma_f32_16x16x32_bf16 v[164:167],a[36:39],a[44:47],v[164:167]
s_add_u32 m0,s58,20480
ds_read_b128 a[76:79],v247 offset:60416
v_mfma_f32_16x16x32_bf16 v[8:11],a[20:23],a[48:51],v[8:11]
buffer_load_dwordx4 v240,s[24:27],s52 offen lds
v_mfma_f32_16x16x32_bf16 v[48:51],a[24:27],a[48:51],v[48:51]
v_mfma_f32_16x16x32_bf16 v[88:91],a[28:31],a[48:51],v[88:91]
s_add_u32 m0,s58,20480
v_mfma_f32_16x16x32_bf16 v[128:131],a[32:35],a[48:51],v[128:131]
buffer_load_dwordx4 v241,s[24:27],s52 offen offset:1024 lds
v_mfma_f32_16x16x32_bf16 v[168:171],a[36:39],a[48:51],v[168:171]
v_mfma_f32_16x16x32_bf16 v[12:15],a[20:23],a[52:55],v[12:15]
s_add_u32 m0,s58,20480
v_mfma_f32_16x16x32_bf16 v[52:55],a[24:27],a[52:55],v[52:55]
buffer_load_dwordx4 v242,s[24:27],s52 offen offset:2048 lds
v_mfma_f32_16x16x32_bf16 v[92:95],a[28:31],a[52:55],v[92:95]
v_mfma_f32_16x16x32_bf16 v[132:135],a[32:35],a[52:55],v[132:135]
s_add_u32 m0,s58,20480
v_mfma_f32_16x16x32_bf16 v[172:175],a[36:39],a[52:55],v[172:175]
s_waitcnt vmcnt(35)
buffer_load_dwordx4 v243,s[24:27],s52 offen offset:3072 lds
v_mfma_f32_16x16x32_bf16 v[16:19],a[20:23],a[56:59],v[16:19]
v_mfma_f32_16x16x32_bf16 v[56:59],a[24:27],a[56:59],v[56:59]
s_add_u32 m0,s58,24576
v_mfma_f32_16x16x32_bf16 v[96:99],a[28:31],a[56:59],v[96:99]
buffer_load_dwordx4 v244,s[24:27],s52 offen lds
v_mfma_f32_16x16x32_bf16 v[136:139],a[32:35],a[56:59],v[136:139]
s_add_u32 s52,s52,40960
v_mfma_f32_16x16x32_bf16 v[176:179],a[36:39],a[56:59],v[176:179]
s_waitcnt vmcnt(11) lgkmcnt(0)
s_barrier
v_mfma_f32_16x16x32_bf16 v[20:23],a[20:23],a[60:63],v[20:23]
ds_read_b128 a[40:43],v247
v_mfma_f32_16x16x32_bf16 v[60:63],a[24:27],a[60:63],v[60:63]
ds_read_b128 a[44:47],v247 offset:1024
v_mfma_f32_16x16x32_bf16 v[100:103],a[28:31],a[60:63],v[100:103]
s_add_u32 m0,s58,40960
buffer_store_dword v247,v253,s[8:11],0 offen sc1
v_mfma_f32_16x16x32_bf16 v[140:143],a[32:35],a[60:63],v[140:143]
buffer_load_dwordx4 v240,s[24:27],s52 offen lds
v_mfma_f32_16x16x32_bf16 v[180:183],a[36:39],a[60:63],v[180:183]
ds_read_b128 a[48:51],v247 offset:2048
ds_read_b128 a[52:55],v247 offset:3072
v_mfma_f32_16x16x32_bf16 v[24:27],a[20:23],a[64:67],v[24:27]
s_add_u32 m0,s58,40960
ds_read_b128 a[56:59],v247 offset:4096
ds_read_b64_tr_b16 a[0:1],v230
v_mfma_f32_16x16x32_bf16 v[64:67],a[24:27],a[64:67],v[64:67]
buffer_load_dwordx4 v241,s[24:27],s52 offen offset:1024 lds
v_mfma_f32_16x16x32_bf16 v[104:107],a[28:31],a[64:67],v[104:107]
ds_read_b64_tr_b16 a[2:3],v230 offset:10240
ds_read_b64_tr_b16 a[4:5],v231
v_mfma_f32_16x16x32_bf16 v[144:147],a[32:35],a[64:67],v[144:147]
s_add_u32 m0,s58,40960
ds_read_b64_tr_b16 a[6:7],v231 offset:10240
v_mfma_f32_16x16x32_bf16 v[184:187],a[36:39],a[64:67],v[184:187]
buffer_load_dwordx4 v242,s[24:27],s52 offen offset:2048 lds
v_mfma_f32_16x16x32_bf16 v[28:31],a[20:23],a[68:71],v[28:31]
ds_read_b64_tr_b16 a[8:9],v232
ds_read_b64_tr_b16 a[10:11],v232 offset:10240
v_mfma_f32_16x16x32_bf16 v[68:71],a[24:27],a[68:71],v[68:71]
s_add_u32 m0,s58,40960
ds_read_b64_tr_b16 a[12:13],v233
v_mfma_f32_16x16x32_bf16 v[108:111],a[28:31],a[68:71],v[108:111]
buffer_load_dwordx4 v243,s[24:27],s52 offen offset:3072 lds
v_mfma_f32_16x16x32_bf16 v[148:151],a[32:35],a[68:71],v[148:151]
ds_read_b64_tr_b16 a[14:15],v233 offset:10240
ds_read_b64_tr_b16 a[16:17],v234
v_mfma_f32_16x16x32_bf16 v[188:191],a[36:39],a[68:71],v[188:191]
s_add_u32 m0,s58,45056
ds_read_b64_tr_b16 a[18:19],v234 offset:10240
v_mfma_f32_16x16x32_bf16 v[32:35],a[20:23],a[72:75],v[32:35]
buffer_load_dwordx4 v244,s[24:27],s52 offen lds
v_mfma_f32_16x16x32_bf16 v[72:75],a[24:27],a[72:75],v[72:75]
s_add_u32 s52,s52,40960
v_mfma_f32_16x16x32_bf16 v[112:115],a[28:31],a[72:75],v[112:115]
v_mfma_f32_16x16x32_bf16 v[152:155],a[32:35],a[72:75],v[152:155]
v_mfma_f32_16x16x32_bf16 v[192:195],a[36:39],a[72:75],v[192:195]
v_mfma_f32_16x16x32_bf16 v[36:39],a[20:23],a[76:79],v[36:39]
v_mfma_f32_16x16x32_bf16 v[76:79],a[24:27],a[76:79],v[76:79]
v_mfma_f32_16x16x32_bf16 v[116:119],a[28:31],a[76:79],v[116:119]
v_mfma_f32_16x16x32_bf16 v[156:159],a[32:35],a[76:79],v[156:159]
v_mfma_f32_16x16x32_bf16 v[196:199],a[36:39],a[76:79],v[196:199]
s_waitcnt lgkmcnt(0)
LOLOOP_%=:
v_mfma_f32_16x16x32_bf16 v[0:3],a[0:3],a[40:43],v[0:3]
v_mfma_f32_16x16x32_bf16 v[40:43],a[4:7],a[40:43],v[40:43]
buffer_load_dword a250,v254,s[28:31],s54 offen
v_mfma_f32_16x16x32_bf16 v[80:83],a[8:11],a[40:43],v[80:83]
s_add_u32 m0,s59,40960
ds_read_b128 a[60:63],v247 offset:5120
v_mfma_f32_16x16x32_bf16 v[120:123],a[12:15],a[40:43],v[120:123]
buffer_load_dwordx4 v245,s[20:23],s53 offen sc1 lds
v_mfma_f32_16x16x32_bf16 v[160:163],a[16:19],a[40:43],v[160:163]
s_add_u32 m0,s59,40960
ds_read_b128 a[64:67],v247 offset:6144
v_mfma_f32_16x16x32_bf16 v[4:7],a[0:3],a[44:47],v[4:7]
buffer_load_dwordx4 v245,s[20:23],s53 offen offset:1024 sc1 lds
v_mfma_f32_16x16x32_bf16 v[44:47],a[4:7],a[44:47],v[44:47]
s_add_u32 m0,s59,40960
ds_read_b128 a[68:71],v247 offset:7168
v_mfma_f32_16x16x32_bf16 v[84:87],a[8:11],a[44:47],v[84:87]
buffer_load_dwordx4 v245,s[20:23],s53 offen offset:2048 sc1 lds
v_mfma_f32_16x16x32_bf16 v[124:127],a[12:15],a[44:47],v[124:127]
s_add_u32 m0,s59,40960
ds_read_b128 a[72:75],v247 offset:8192
v_mfma_f32_16x16x32_bf16 v[164:167],a[16:19],a[44:47],v[164:167]
buffer_load_dwordx4 v245,s[20:23],s53 offen offset:3072 sc1 lds
v_mfma_f32_16x16x32_bf16 v[8:11],a[0:3],a[48:51],v[8:11]
s_add_u32 m0,s59,45056
ds_read_b128 a[76:79],v247 offset:9216
v_mfma_f32_16x16x32_bf16 v[48:51],a[4:7],a[48:51],v[48:51]
s_add_u32 s67,s53,4096
v_mfma_f32_16x16x32_bf16 v[88:91],a[8:11],a[48:51],v[88:91]
buffer_load_dwordx4 v245,s[20:23],s67 offen sc1 lds
v_mfma_f32_16x16x32_bf16 v[128:131],a[12:15],a[48:51],v[128:131]
s_add_u32 s53,s53,20480
v_mfma_f32_16x16x32_bf16 v[168:171],a[16:19],a[48:51],v[168:171]
s_add_u32 s54,s54,81920
v_mfma_f32_16x16x32_bf16 v[12:15],a[0:3],a[52:55],v[12:15]
s_cmp_eq_u32 s51,3
v_mfma_f32_16x16x32_bf16 v[52:55],a[4:7],a[52:55],v[52:55]
s_cselect_b32 s66,s50,0
v_mfma_f32_16x16x32_bf16 v[92:95],a[8:11],a[52:55],v[92:95]
s_mul_i32 s74,s66,-1966080
v_mfma_f32_16x16x32_bf16 v[132:135],a[12:15],a[52:55],v[132:135]
s_add_u32 s74,s74,40960
v_mfma_f32_16x16x32_bf16 v[172:175],a[16:19],a[52:55],v[172:175]
s_mul_i32 s75,s66,-491520
v_mfma_f32_16x16x32_bf16 v[16:19],a[0:3],a[56:59],v[16:19]
s_add_u32 s75,s75,20480
v_mfma_f32_16x16x32_bf16 v[56:59],a[4:7],a[56:59],v[56:59]
s_mul_i32 s6,s66,-1966080
v_mfma_f32_16x16x32_bf16 v[96:99],a[8:11],a[56:59],v[96:99]
s_add_u32 s6,s6,81920
v_mfma_f32_16x16x32_bf16 v[136:139],a[12:15],a[56:59],v[136:139]
v_mfma_f32_16x16x32_bf16 v[176:179],a[16:19],a[56:59],v[176:179]
s_waitcnt vmcnt(12) lgkmcnt(0)
s_barrier
v_mfma_f32_16x16x32_bf16 v[20:23],a[0:3],a[60:63],v[20:23]
ds_read_b128 a[40:43],v247 offset:10240
v_mfma_f32_16x16x32_bf16 v[60:63],a[4:7],a[60:63],v[60:63]
ds_read_b128 a[44:47],v247 offset:11264
v_mfma_f32_16x16x32_bf16 v[100:103],a[8:11],a[60:63],v[100:103]
ds_read_b128 a[48:51],v247 offset:12288
v_mfma_f32_16x16x32_bf16 v[140:143],a[12:15],a[60:63],v[140:143]
ds_read_b128 a[52:55],v247 offset:13312
v_mfma_f32_16x16x32_bf16 v[180:183],a[16:19],a[60:63],v[180:183]
ds_read_b128 a[56:59],v247 offset:14336
v_mfma_f32_16x16x32_bf16 v[24:27],a[0:3],a[64:67],v[24:27]
ds_read_b64_tr_b16 a[20:21],v230 offset:20480
v_mfma_f32_16x16x32_bf16 v[64:67],a[4:7],a[64:67],v[64:67]
ds_read_b64_tr_b16 a[22:23],v230 offset:30720
v_mfma_f32_16x16x32_bf16 v[104:107],a[8:11],a[64:67],v[104:107]
ds_read_b64_tr_b16 a[24:25],v231 offset:20480
v_mfma_f32_16x16x32_bf16 v[144:147],a[12:15],a[64:67],v[144:147]
ds_read_b64_tr_b16 a[26:27],v231 offset:30720
v_mfma_f32_16x16x32_bf16 v[184:187],a[16:19],a[64:67],v[184:187]
ds_read_b64_tr_b16 a[28:29],v232 offset:20480
v_mfma_f32_16x16x32_bf16 v[28:31],a[0:3],a[68:71],v[28:31]
ds_read_b64_tr_b16 a[30:31],v232 offset:30720
v_mfma_f32_16x16x32_bf16 v[68:71],a[4:7],a[68:71],v[68:71]
ds_read_b64_tr_b16 a[32:33],v233 offset:20480
v_mfma_f32_16x16x32_bf16 v[108:111],a[8:11],a[68:71],v[108:111]
ds_read_b64_tr_b16 a[34:35],v233 offset:30720
v_mfma_f32_16x16x32_bf16 v[148:151],a[12:15],a[68:71],v[148:151]
ds_read_b64_tr_b16 a[36:37],v234 offset:20480
v_mfma_f32_16x16x32_bf16 v[188:191],a[16:19],a[68:71],v[188:191]
ds_read_b64_tr_b16 a[38:39],v234 offset:30720
v_mfma_f32_16x16x32_bf16 v[32:35],a[0:3],a[72:75],v[32:35]
v_mfma_f32_16x16x32_bf16 v[72:75],a[4:7],a[72:75],v[72:75]
v_mfma_f32_16x16x32_bf16 v[112:115],a[8:11],a[72:75],v[112:115]
v_mfma_f32_16x16x32_bf16 v[152:155],a[12:15],a[72:75],v[152:155]
v_mfma_f32_16x16x32_bf16 v[192:195],a[16:19],a[72:75],v[192:195]
v_mfma_f32_16x16x32_bf16 v[36:39],a[0:3],a[76:79],v[36:39]
v_mfma_f32_16x16x32_bf16 v[76:79],a[4:7],a[76:79],v[76:79]
v_mfma_f32_16x16x32_bf16 v[116:119],a[8:11],a[76:79],v[116:119]
v_mfma_f32_16x16x32_bf16 v[156:159],a[12:15],a[76:79],v[156:159]
v_mfma_f32_16x16x32_bf16 v[196:199],a[16:19],a[76:79],v[196:199]
s_waitcnt lgkmcnt(0)
v_mfma_f32_16x16x32_bf16 v[0:3],a[20:23],a[40:43],v[0:3]
v_mfma_f32_16x16x32_bf16 v[40:43],a[24:27],a[40:43],v[40:43]
ds_read_b128 a[60:63],v247 offset:15360
v_mfma_f32_16x16x32_bf16 v[80:83],a[28:31],a[40:43],v[80:83]
v_mfma_f32_16x16x32_bf16 v[120:123],a[32:35],a[40:43],v[120:123]
ds_read_b128 a[64:67],v247 offset:16384
v_mfma_f32_16x16x32_bf16 v[160:163],a[36:39],a[40:43],v[160:163]
v_mfma_f32_16x16x32_bf16 v[4:7],a[20:23],a[44:47],v[4:7]
ds_read_b128 a[68:71],v247 offset:17408
v_mfma_f32_16x16x32_bf16 v[44:47],a[24:27],a[44:47],v[44:47]
v_mfma_f32_16x16x32_bf16 v[84:87],a[28:31],a[44:47],v[84:87]
ds_read_b128 a[72:75],v247 offset:18432
v_mfma_f32_16x16x32_bf16 v[124:127],a[32:35],a[44:47],v[124:127]
v_mfma_f32_16x16x32_bf16 v[164:167],a[36:39],a[44:47],v[164:167]
s_add_u32 m0,s58,61440
ds_read_b128 a[76:79],v247 offset:19456
v_mfma_f32_16x16x32_bf16 v[8:11],a[20:23],a[48:51],v[8:11]
buffer_load_dwordx4 v240,s[24:27],s52 offen lds
v_mfma_f32_16x16x32_bf16 v[48:51],a[24:27],a[48:51],v[48:51]
v_mfma_f32_16x16x32_bf16 v[88:91],a[28:31],a[48:51],v[88:91]
s_add_u32 m0,s58,61440
v_mfma_f32_16x16x32_bf16 v[128:131],a[32:35],a[48:51],v[128:131]
buffer_load_dwordx4 v241,s[24:27],s52 offen offset:1024 lds
v_mfma_f32_16x16x32_bf16 v[168:171],a[36:39],a[48:51],v[168:171]
v_mfma_f32_16x16x32_bf16 v[12:15],a[20:23],a[52:55],v[12:15]
s_add_u32 m0,s58,61440
v_mfma_f32_16x16x32_bf16 v[52:55],a[24:27],a[52:55],v[52:55]
buffer_load_dwordx4 v242,s[24:27],s52 offen offset:2048 lds
v_mfma_f32_16x16x32_bf16 v[92:95],a[28:31],a[52:55],v[92:95]
v_mfma_f32_16x16x32_bf16 v[132:135],a[32:35],a[52:55],v[132:135]
s_add_u32 m0,s58,61440
v_mfma_f32_16x16x32_bf16 v[172:175],a[36:39],a[52:55],v[172:175]
buffer_load_dwordx4 v243,s[24:27],s52 offen offset:3072 lds
v_mfma_f32_16x16x32_bf16 v[16:19],a[20:23],a[56:59],v[16:19]
v_mfma_f32_16x16x32_bf16 v[56:59],a[24:27],a[56:59],v[56:59]
s_add_u32 m0,s58,65536
v_mfma_f32_16x16x32_bf16 v[96:99],a[28:31],a[56:59],v[96:99]
buffer_load_dwordx4 v244,s[24:27],s52 offen lds
v_mfma_f32_16x16x32_bf16 v[136:139],a[32:35],a[56:59],v[136:139]
s_add_u32 s52,s52,40960
v_mfma_f32_16x16x32_bf16 v[176:179],a[36:39],a[56:59],v[176:179]
s_waitcnt vmcnt(11) lgkmcnt(0)
s_barrier
v_mfma_f32_16x16x32_bf16 v[20:23],a[20:23],a[60:63],v[20:23]
ds_read_b128 a[40:43],v247 offset:20480
v_mfma_f32_16x16x32_bf16 v[60:63],a[24:27],a[60:63],v[60:63]
ds_read_b128 a[44:47],v247 offset:21504
v_mfma_f32_16x16x32_bf16 v[100:103],a[28:31],a[60:63],v[100:103]
s_add_u32 m0,s58,0
ds_read_b128 a[48:51],v247 offset:22528
v_mfma_f32_16x16x32_bf16 v[140:143],a[32:35],a[60:63],v[140:143]
buffer_load_dwordx4 v240,s[24:27],s52 offen lds
v_mfma_f32_16x16x32_bf16 v[180:183],a[36:39],a[60:63],v[180:183]
ds_read_b128 a[52:55],v247 offset:23552
ds_read_b128 a[56:59],v247 offset:24576
v_mfma_f32_16x16x32_bf16 v[24:27],a[20:23],a[64:67],v[24:27]
s_add_u32 m0,s58,0
ds_read_b64_tr_b16 a[0:1],v235
v_mfma_f32_16x16x32_bf16 v[64:67],a[24:27],a[64:67],v[64:67]
buffer_load_dwordx4 v241,s[24:27],s52 offen offset:1024 lds
v_mfma_f32_16x16x32_bf16 v[104:107],a[28:31],a[64:67],v[104:107]
ds_read_b64_tr_b16 a[2:3],v235 offset:10240
ds_read_b64_tr_b16 a[4:5],v236
v_mfma_f32_16x16x32_bf16 v[144:147],a[32:35],a[64:67],v[144:147]
s_add_u32 m0,s58,0
ds_read_b64_tr_b16 a[6:7],v236 offset:10240
v_mfma_f32_16x16x32_bf16 v[184:187],a[36:39],a[64:67],v[184:187]
buffer_load_dwordx4 v242,s[24:27],s52 offen offset:2048 lds
v_mfma_f32_16x16x32_bf16 v[28:31],a[20:23],a[68:71],v[28:31]
ds_read_b64_tr_b16 a[8:9],v237
ds_read_b64_tr_b16 a[10:11],v237 offset:10240
v_mfma_f32_16x16x32_bf16 v[68:71],a[24:27],a[68:71],v[68:71]
s_add_u32 m0,s58,0
ds_read_b64_tr_b16 a[12:13],v238
v_mfma_f32_16x16x32_bf16 v[108:111],a[28:31],a[68:71],v[108:111]
buffer_load_dwordx4 v243,s[24:27],s52 offen offset:3072 lds
v_mfma_f32_16x16x32_bf16 v[148:151],a[32:35],a[68:71],v[148:151]
ds_read_b64_tr_b16 a[14:15],v238 offset:10240
ds_read_b64_tr_b16 a[16:17],v239
v_mfma_f32_16x16x32_bf16 v[188:191],a[36:39],a[68:71],v[188:191]
s_add_u32 m0,s58,4096
ds_read_b64_tr_b16 a[18:19],v239 offset:10240
v_mfma_f32_16x16x32_bf16 v[32:35],a[20:23],a[72:75],v[32:35]
buffer_load_dwordx4 v244,s[24:27],s52 offen lds
v_mfma_f32_16x16x32_bf16 v[72:75],a[24:27],a[72:75],v[72:75]
s_add_u32 s52,s52,40960
v_mfma_f32_16x16x32_bf16 v[112:115],a[28:31],a[72:75],v[112:115]
v_mfma_f32_16x16x32_bf16 v[152:155],a[32:35],a[72:75],v[152:155]
v_mfma_f32_16x16x32_bf16 v[192:195],a[36:39],a[72:75],v[192:195]
v_mfma_f32_16x16x32_bf16 v[36:39],a[20:23],a[76:79],v[36:39]
v_mfma_f32_16x16x32_bf16 v[76:79],a[24:27],a[76:79],v[76:79]
v_mfma_f32_16x16x32_bf16 v[116:119],a[28:31],a[76:79],v[116:119]
v_mfma_f32_16x16x32_bf16 v[156:159],a[32:35],a[76:79],v[156:159]
v_mfma_f32_16x16x32_bf16 v[196:199],a[36:39],a[76:79],v[196:199]
s_waitcnt lgkmcnt(0)
v_mfma_f32_16x16x32_bf16 v[0:3],a[0:3],a[40:43],v[0:3]
v_mfma_f32_16x16x32_bf16 v[40:43],a[4:7],a[40:43],v[40:43]
buffer_load_dword a250,v254,s[28:31],s54 offen
v_mfma_f32_16x16x32_bf16 v[80:83],a[8:11],a[40:43],v[80:83]
s_add_u32 m0,s59,0
ds_read_b128 a[60:63],v247 offset:25600
v_mfma_f32_16x16x32_bf16 v[120:123],a[12:15],a[40:43],v[120:123]
buffer_load_dwordx4 v245,s[20:23],s53 offen sc1 lds
v_mfma_f32_16x16x32_bf16 v[160:163],a[16:19],a[40:43],v[160:163]
s_add_u32 m0,s59,0
ds_read_b128 a[64:67],v247 offset:26624
v_mfma_f32_16x16x32_bf16 v[4:7],a[0:3],a[44:47],v[4:7]
buffer_load_dwordx4 v245,s[20:23],s53 offen offset:1024 sc1 lds
v_mfma_f32_16x16x32_bf16 v[44:47],a[4:7],a[44:47],v[44:47]
s_add_u32 m0,s59,0
ds_read_b128 a[68:71],v247 offset:27648
v_mfma_f32_16x16x32_bf16 v[84:87],a[8:11],a[44:47],v[84:87]
buffer_load_dwordx4 v245,s[20:23],s53 offen offset:2048 sc1 lds
v_mfma_f32_16x16x32_bf16 v[124:127],a[12:15],a[44:47],v[124:127]
s_add_u32 m0,s59,0
ds_read_b128 a[72:75],v247 offset:28672
v_mfma_f32_16x16x32_bf16 v[164:167],a[16:19],a[44:47],v[164:167]
buffer_load_dwordx4 v245,s[20:23],s53 offen offset:3072 sc1 lds
v_mfma_f32_16x16x32_bf16 v[8:11],a[0:3],a[48:51],v[8:11]
s_add_u32 m0,s59,4096
ds_read_b128 a[76:79],v247 offset:29696
v_mfma_f32_16x16x32_bf16 v[48:51],a[4:7],a[48:51],v[48:51]
s_add_u32 s67,s53,4096
v_mfma_f32_16x16x32_bf16 v[88:91],a[8:11],a[48:51],v[88:91]
buffer_load_dwordx4 v245,s[20:23],s67 offen sc1 lds
v_mfma_f32_16x16x32_bf16 v[128:131],a[12:15],a[48:51],v[128:131]
s_add_u32 s53,s53,20480
v_mfma_f32_16x16x32_bf16 v[168:171],a[16:19],a[48:51],v[168:171]
s_add_u32 s54,s54,s6
v_mfma_f32_16x16x32_bf16 v[12:15],a[0:3],a[52:55],v[12:15]
s_add_u32 s69,s69,64
v_mfma_f32_16x16x32_bf16 v[52:55],a[4:7],a[52:55],v[52:55]
s_and_b32 s69,s69,255
v_mfma_f32_16x16x32_bf16 v[92:95],a[8:11],a[52:55],v[92:95]
s_sub_u32 s51,s51,1
v_mfma_f32_16x16x32_bf16 v[132:135],a[12:15],a[52:55],v[132:135]
v_mfma_f32_16x16x32_bf16 v[172:175],a[16:19],a[52:55],v[172:175]
v_mfma_f32_16x16x32_bf16 v[16:19],a[0:3],a[56:59],v[16:19]
v_mfma_f32_16x16x32_bf16 v[56:59],a[4:7],a[56:59],v[56:59]
v_mfma_f32_16x16x32_bf16 v[96:99],a[8:11],a[56:59],v[96:99]
v_mfma_f32_16x16x32_bf16 v[136:139],a[12:15],a[56:59],v[136:139]
v_mfma_f32_16x16x32_bf16 v[176:179],a[16:19],a[56:59],v[176:179]
s_waitcnt vmcnt(11) lgkmcnt(0)
s_barrier
v_mfma_f32_16x16x32_bf16 v[20:23],a[0:3],a[60:63],v[20:23]
ds_read_b128 a[40:43],v247 offset:30720
v_mfma_f32_16x16x32_bf16 v[60:63],a[4:7],a[60:63],v[60:63]
ds_read_b128 a[44:47],v247 offset:31744
v_mfma_f32_16x16x32_bf16 v[100:103],a[8:11],a[60:63],v[100:103]
ds_read_b128 a[48:51],v247 offset:32768
v_mfma_f32_16x16x32_bf16 v[140:143],a[12:15],a[60:63],v[140:143]
ds_read_b128 a[52:55],v247 offset:33792
v_mfma_f32_16x16x32_bf16 v[180:183],a[16:19],a[60:63],v[180:183]
ds_read_b128 a[56:59],v247 offset:34816
v_mfma_f32_16x16x32_bf16 v[24:27],a[0:3],a[64:67],v[24:27]
ds_read_b64_tr_b16 a[20:21],v235 offset:20480
v_mfma_f32_16x16x32_bf16 v[64:67],a[4:7],a[64:67],v[64:67]
ds_read_b64_tr_b16 a[22:23],v235 offset:30720
v_mfma_f32_16x16x32_bf16 v[104:107],a[8:11],a[64:67],v[104:107]
ds_read_b64_tr_b16 a[24:25],v236 offset:20480
v_mfma_f32_16x16x32_bf16 v[144:147],a[12:15],a[64:67],v[144:147]
ds_read_b64_tr_b16 a[26:27],v236 offset:30720
v_mfma_f32_16x16x32_bf16 v[184:187],a[16:19],a[64:67],v[184:187]
ds_read_b64_tr_b16 a[28:29],v237 offset:20480
v_mfma_f32_16x16x32_bf16 v[28:31],a[0:3],a[68:71],v[28:31]
ds_read_b64_tr_b16 a[30:31],v237 offset:30720
v_mfma_f32_16x16x32_bf16 v[68:71],a[4:7],a[68:71],v[68:71]
ds_read_b64_tr_b16 a[32:33],v238 offset:20480
v_mfma_f32_16x16x32_bf16 v[108:111],a[8:11],a[68:71],v[108:111]
ds_read_b64_tr_b16 a[34:35],v238 offset:30720
v_mfma_f32_16x16x32_bf16 v[148:151],a[12:15],a[68:71],v[148:151]
ds_read_b64_tr_b16 a[36:37],v239 offset:20480
v_mfma_f32_16x16x32_bf16 v[188:191],a[16:19],a[68:71],v[188:191]
ds_read_b64_tr_b16 a[38:39],v239 offset:30720
v_mfma_f32_16x16x32_bf16 v[32:35],a[0:3],a[72:75],v[32:35]
v_mfma_f32_16x16x32_bf16 v[72:75],a[4:7],a[72:75],v[72:75]
v_mfma_f32_16x16x32_bf16 v[112:115],a[8:11],a[72:75],v[112:115]
v_mfma_f32_16x16x32_bf16 v[152:155],a[12:15],a[72:75],v[152:155]
v_mfma_f32_16x16x32_bf16 v[192:195],a[16:19],a[72:75],v[192:195]
v_mfma_f32_16x16x32_bf16 v[36:39],a[0:3],a[76:79],v[36:39]
v_mfma_f32_16x16x32_bf16 v[76:79],a[4:7],a[76:79],v[76:79]
v_mfma_f32_16x16x32_bf16 v[116:119],a[8:11],a[76:79],v[116:119]
v_mfma_f32_16x16x32_bf16 v[156:159],a[12:15],a[76:79],v[156:159]
v_mfma_f32_16x16x32_bf16 v[196:199],a[16:19],a[76:79],v[196:199]
s_waitcnt lgkmcnt(0)
v_mfma_f32_16x16x32_bf16 v[0:3],a[20:23],a[40:43],v[0:3]
v_mfma_f32_16x16x32_bf16 v[40:43],a[24:27],a[40:43],v[40:43]
ds_read_b128 a[60:63],v247 offset:35840
v_mfma_f32_16x16x32_bf16 v[80:83],a[28:31],a[40:43],v[80:83]
v_mfma_f32_16x16x32_bf16 v[120:123],a[32:35],a[40:43],v[120:123]
ds_read_b128 a[64:67],v247 offset:36864
v_mfma_f32_16x16x32_bf16 v[160:163],a[36:39],a[40:43],v[160:163]
v_mfma_f32_16x16x32_bf16 v[4:7],a[20:23],a[44:47],v[4:7]
ds_read_b128 a[68:71],v247 offset:37888
v_mfma_f32_16x16x32_bf16 v[44:47],a[24:27],a[44:47],v[44:47]
v_mfma_f32_16x16x32_bf16 v[84:87],a[28:31],a[44:47],v[84:87]
ds_read_b128 a[72:75],v247 offset:38912
v_mfma_f32_16x16x32_bf16 v[124:127],a[32:35],a[44:47],v[124:127]
v_mfma_f32_16x16x32_bf16 v[164:167],a[36:39],a[44:47],v[164:167]
s_add_u32 m0,s58,20480
ds_read_b128 a[76:79],v247 offset:39936
v_mfma_f32_16x16x32_bf16 v[8:11],a[20:23],a[48:51],v[8:11]
buffer_load_dwordx4 v240,s[24:27],s52 offen lds
v_mfma_f32_16x16x32_bf16 v[48:51],a[24:27],a[48:51],v[48:51]
v_mfma_f32_16x16x32_bf16 v[88:91],a[28:31],a[48:51],v[88:91]
s_add_u32 m0,s58,20480
v_mfma_f32_16x16x32_bf16 v[128:131],a[32:35],a[48:51],v[128:131]
buffer_load_dwordx4 v241,s[24:27],s52 offen offset:1024 lds
v_mfma_f32_16x16x32_bf16 v[168:171],a[36:39],a[48:51],v[168:171]
v_mfma_f32_16x16x32_bf16 v[12:15],a[20:23],a[52:55],v[12:15]
s_add_u32 m0,s58,20480
v_mfma_f32_16x16x32_bf16 v[52:55],a[24:27],a[52:55],v[52:55]
buffer_load_dwordx4 v242,s[24:27],s52 offen offset:2048 lds
v_mfma_f32_16x16x32_bf16 v[92:95],a[28:31],a[52:55],v[92:95]
v_mfma_f32_16x16x32_bf16 v[132:135],a[32:35],a[52:55],v[132:135]
s_add_u32 m0,s58,20480
v_mfma_f32_16x16x32_bf16 v[172:175],a[36:39],a[52:55],v[172:175]
buffer_load_dwordx4 v243,s[24:27],s52 offen offset:3072 lds
v_mfma_f32_16x16x32_bf16 v[16:19],a[20:23],a[56:59],v[16:19]
v_mfma_f32_16x16x32_bf16 v[56:59],a[24:27],a[56:59],v[56:59]
s_add_u32 m0,s58,24576
v_mfma_f32_16x16x32_bf16 v[96:99],a[28:31],a[56:59],v[96:99]
buffer_load_dwordx4 v244,s[24:27],s52 offen lds
v_mfma_f32_16x16x32_bf16 v[136:139],a[32:35],a[56:59],v[136:139]
s_add_u32 s52,s52,40960
v_mfma_f32_16x16x32_bf16 v[176:179],a[36:39],a[56:59],v[176:179]
s_waitcnt vmcnt(11) lgkmcnt(0)
s_barrier
v_mfma_f32_16x16x32_bf16 v[20:23],a[20:23],a[60:63],v[20:23]
ds_read_b128 a[40:43],v247 offset:40960
v_mfma_f32_16x16x32_bf16 v[60:63],a[24:27],a[60:63],v[60:63]
ds_read_b128 a[44:47],v247 offset:41984
v_mfma_f32_16x16x32_bf16 v[100:103],a[28:31],a[60:63],v[100:103]
s_add_u32 m0,s58,40960
ds_read_b128 a[48:51],v247 offset:43008
v_mfma_f32_16x16x32_bf16 v[140:143],a[32:35],a[60:63],v[140:143]
buffer_load_dwordx4 v240,s[24:27],s52 offen lds
v_mfma_f32_16x16x32_bf16 v[180:183],a[36:39],a[60:63],v[180:183]
ds_read_b128 a[52:55],v247 offset:44032
ds_read_b128 a[56:59],v247 offset:45056
v_mfma_f32_16x16x32_bf16 v[24:27],a[20:23],a[64:67],v[24:27]
s_add_u32 m0,s58,40960
ds_read_b64_tr_b16 a[0:1],v230
v_mfma_f32_16x16x32_bf16 v[64:67],a[24:27],a[64:67],v[64:67]
buffer_load_dwordx4 v241,s[24:27],s52 offen offset:1024 lds
v_mfma_f32_16x16x32_bf16 v[104:107],a[28:31],a[64:67],v[104:107]
ds_read_b64_tr_b16 a[2:3],v230 offset:10240
ds_read_b64_tr_b16 a[4:5],v231
v_mfma_f32_16x16x32_bf16 v[144:147],a[32:35],a[64:67],v[144:147]
s_add_u32 m0,s58,40960
ds_read_b64_tr_b16 a[6:7],v231 offset:10240
v_mfma_f32_16x16x32_bf16 v[184:187],a[36:39],a[64:67],v[184:187]
buffer_load_dwordx4 v242,s[24:27],s52 offen offset:2048 lds
v_mfma_f32_16x16x32_bf16 v[28:31],a[20:23],a[68:71],v[28:31]
ds_read_b64_tr_b16 a[8:9],v232
ds_read_b64_tr_b16 a[10:11],v232 offset:10240
v_mfma_f32_16x16x32_bf16 v[68:71],a[24:27],a[68:71],v[68:71]
s_add_u32 m0,s58,40960
ds_read_b64_tr_b16 a[12:13],v233
v_mfma_f32_16x16x32_bf16 v[108:111],a[28:31],a[68:71],v[108:111]
buffer_load_dwordx4 v243,s[24:27],s52 offen offset:3072 lds
v_mfma_f32_16x16x32_bf16 v[148:151],a[32:35],a[68:71],v[148:151]
ds_read_b64_tr_b16 a[14:15],v233 offset:10240
ds_read_b64_tr_b16 a[16:17],v234
v_mfma_f32_16x16x32_bf16 v[188:191],a[36:39],a[68:71],v[188:191]
s_add_u32 m0,s58,45056
ds_read_b64_tr_b16 a[18:19],v234 offset:10240
v_mfma_f32_16x16x32_bf16 v[32:35],a[20:23],a[72:75],v[32:35]
buffer_load_dwordx4 v244,s[24:27],s52 offen lds
v_mfma_f32_16x16x32_bf16 v[72:75],a[24:27],a[72:75],v[72:75]
s_add_u32 s52,s52,40960
v_mfma_f32_16x16x32_bf16 v[112:115],a[28:31],a[72:75],v[112:115]
v_mfma_f32_16x16x32_bf16 v[152:155],a[32:35],a[72:75],v[152:155]
v_mfma_f32_16x16x32_bf16 v[192:195],a[36:39],a[72:75],v[192:195]
v_mfma_f32_16x16x32_bf16 v[36:39],a[20:23],a[76:79],v[36:39]
v_mfma_f32_16x16x32_bf16 v[76:79],a[24:27],a[76:79],v[76:79]
v_mfma_f32_16x16x32_bf16 v[116:119],a[28:31],a[76:79],v[116:119]
v_mfma_f32_16x16x32_bf16 v[156:159],a[32:35],a[76:79],v[156:159]
v_mfma_f32_16x16x32_bf16 v[196:199],a[36:39],a[76:79],v[196:199]
s_waitcnt lgkmcnt(0)
v_mfma_f32_16x16x32_bf16 v[0:3],a[0:3],a[40:43],v[0:3]
v_mfma_f32_16x16x32_bf16 v[40:43],a[4:7],a[40:43],v[40:43]
buffer_load_dword a250,v254,s[28:31],s54 offen
v_mfma_f32_16x16x32_bf16 v[80:83],a[8:11],a[40:43],v[80:83]
s_add_u32 m0,s59,20480
ds_read_b128 a[60:63],v247 offset:46080
v_mfma_f32_16x16x32_bf16 v[120:123],a[12:15],a[40:43],v[120:123]
buffer_load_dwordx4 v245,s[20:23],s53 offen sc1 lds
v_mfma_f32_16x16x32_bf16 v[160:163],a[16:19],a[40:43],v[160:163]
s_add_u32 m0,s59,20480
ds_read_b128 a[64:67],v247 offset:47104
v_mfma_f32_16x16x32_bf16 v[4:7],a[0:3],a[44:47],v[4:7]
buffer_load_dwordx4 v245,s[20:23],s53 offen offset:1024 sc1 lds
v_mfma_f32_16x16x32_bf16 v[44:47],a[4:7],a[44:47],v[44:47]
s_add_u32 m0,s59,20480
ds_read_b128 a[68:71],v247 offset:48128
v_mfma_f32_16x16x32_bf16 v[84:87],a[8:11],a[44:47],v[84:87]
buffer_load_dwordx4 v245,s[20:23],s53 offen offset:2048 sc1 lds
v_mfma_f32_16x16x32_bf16 v[124:127],a[12:15],a[44:47],v[124:127]
s_add_u32 m0,s59,20480
ds_read_b128 a[72:75],v247 offset:49152
v_mfma_f32_16x16x32_bf16 v[164:167],a[16:19],a[44:47],v[164:167]
buffer_load_dwordx4 v245,s[20:23],s53 offen offset:3072 sc1 lds
v_mfma_f32_16x16x32_bf16 v[8:11],a[0:3],a[48:51],v[8:11]
s_add_u32 m0,s59,24576
ds_read_b128 a[76:79],v247 offset:50176
v_mfma_f32_16x16x32_bf16 v[48:51],a[4:7],a[48:51],v[48:51]
s_add_u32 s67,s53,4096
v_mfma_f32_16x16x32_bf16 v[88:91],a[8:11],a[48:51],v[88:91]
buffer_load_dwordx4 v245,s[20:23],s67 offen sc1 lds
v_mfma_f32_16x16x32_bf16 v[128:131],a[12:15],a[48:51],v[128:131]
s_add_u32 s53,s53,20480
v_mfma_f32_16x16x32_bf16 v[168:171],a[16:19],a[48:51],v[168:171]
s_add_u32 s54,s54,81920
buffer_load_dword v246,v253,s[12:15],s69 offen sc1
v_mfma_f32_16x16x32_bf16 v[12:15],a[0:3],a[52:55],v[12:15]
v_mfma_f32_16x16x32_bf16 v[52:55],a[4:7],a[52:55],v[52:55]
v_mfma_f32_16x16x32_bf16 v[92:95],a[8:11],a[52:55],v[92:95]
v_mfma_f32_16x16x32_bf16 v[132:135],a[12:15],a[52:55],v[132:135]
v_mfma_f32_16x16x32_bf16 v[172:175],a[16:19],a[52:55],v[172:175]
v_mfma_f32_16x16x32_bf16 v[16:19],a[0:3],a[56:59],v[16:19]
v_mfma_f32_16x16x32_bf16 v[56:59],a[4:7],a[56:59],v[56:59]
v_mfma_f32_16x16x32_bf16 v[96:99],a[8:11],a[56:59],v[96:99]
v_mfma_f32_16x16x32_bf16 v[136:139],a[12:15],a[56:59],v[136:139]
v_mfma_f32_16x16x32_bf16 v[176:179],a[16:19],a[56:59],v[176:179]
s_waitcnt vmcnt(12) lgkmcnt(0)
s_barrier
v_mfma_f32_16x16x32_bf16 v[20:23],a[0:3],a[60:63],v[20:23]
ds_read_b128 a[40:43],v247 offset:51200
v_mfma_f32_16x16x32_bf16 v[60:63],a[4:7],a[60:63],v[60:63]
ds_read_b128 a[44:47],v247 offset:52224
v_mfma_f32_16x16x32_bf16 v[100:103],a[8:11],a[60:63],v[100:103]
ds_read_b128 a[48:51],v247 offset:53248
v_mfma_f32_16x16x32_bf16 v[140:143],a[12:15],a[60:63],v[140:143]
ds_read_b128 a[52:55],v247 offset:54272
v_mfma_f32_16x16x32_bf16 v[180:183],a[16:19],a[60:63],v[180:183]
ds_read_b128 a[56:59],v247 offset:55296
v_mfma_f32_16x16x32_bf16 v[24:27],a[0:3],a[64:67],v[24:27]
ds_read_b64_tr_b16 a[20:21],v230 offset:20480
v_mfma_f32_16x16x32_bf16 v[64:67],a[4:7],a[64:67],v[64:67]
ds_read_b64_tr_b16 a[22:23],v230 offset:30720
v_mfma_f32_16x16x32_bf16 v[104:107],a[8:11],a[64:67],v[104:107]
ds_read_b64_tr_b16 a[24:25],v231 offset:20480
v_mfma_f32_16x16x32_bf16 v[144:147],a[12:15],a[64:67],v[144:147]
ds_read_b64_tr_b16 a[26:27],v231 offset:30720
v_mfma_f32_16x16x32_bf16 v[184:187],a[16:19],a[64:67],v[184:187]
ds_read_b64_tr_b16 a[28:29],v232 offset:20480
v_mfma_f32_16x16x32_bf16 v[28:31],a[0:3],a[68:71],v[28:31]
ds_read_b64_tr_b16 a[30:31],v232 offset:30720
v_mfma_f32_16x16x32_bf16 v[68:71],a[4:7],a[68:71],v[68:71]
ds_read_b64_tr_b16 a[32:33],v233 offset:20480
v_mfma_f32_16x16x32_bf16 v[108:111],a[8:11],a[68:71],v[108:111]
ds_read_b64_tr_b16 a[34:35],v233 offset:30720
v_mfma_f32_16x16x32_bf16 v[148:151],a[12:15],a[68:71],v[148:151]
ds_read_b64_tr_b16 a[36:37],v234 offset:20480
v_mfma_f32_16x16x32_bf16 v[188:191],a[16:19],a[68:71],v[188:191]
ds_read_b64_tr_b16 a[38:39],v234 offset:30720
v_mfma_f32_16x16x32_bf16 v[32:35],a[0:3],a[72:75],v[32:35]
v_mfma_f32_16x16x32_bf16 v[72:75],a[4:7],a[72:75],v[72:75]
v_mfma_f32_16x16x32_bf16 v[112:115],a[8:11],a[72:75],v[112:115]
v_mfma_f32_16x16x32_bf16 v[152:155],a[12:15],a[72:75],v[152:155]
v_mfma_f32_16x16x32_bf16 v[192:195],a[16:19],a[72:75],v[192:195]
v_mfma_f32_16x16x32_bf16 v[36:39],a[0:3],a[76:79],v[36:39]
v_mfma_f32_16x16x32_bf16 v[76:79],a[4:7],a[76:79],v[76:79]
v_mfma_f32_16x16x32_bf16 v[116:119],a[8:11],a[76:79],v[116:119]
v_mfma_f32_16x16x32_bf16 v[156:159],a[12:15],a[76:79],v[156:159]
v_mfma_f32_16x16x32_bf16 v[196:199],a[16:19],a[76:79],v[196:199]
s_waitcnt lgkmcnt(0)
v_mfma_f32_16x16x32_bf16 v[0:3],a[20:23],a[40:43],v[0:3]
v_mfma_f32_16x16x32_bf16 v[40:43],a[24:27],a[40:43],v[40:43]
ds_read_b128 a[60:63],v247 offset:56320
v_mfma_f32_16x16x32_bf16 v[80:83],a[28:31],a[40:43],v[80:83]
v_mfma_f32_16x16x32_bf16 v[120:123],a[32:35],a[40:43],v[120:123]
ds_read_b128 a[64:67],v247 offset:57344
v_mfma_f32_16x16x32_bf16 v[160:163],a[36:39],a[40:43],v[160:163]
v_mfma_f32_16x16x32_bf16 v[4:7],a[20:23],a[44:47],v[4:7]
ds_read_b128 a[68:71],v247 offset:58368
v_mfma_f32_16x16x32_bf16 v[44:47],a[24:27],a[44:47],v[44:47]
v_mfma_f32_16x16x32_bf16 v[84:87],a[28:31],a[44:47],v[84:87]
ds_read_b128 a[72:75],v247 offset:59392
v_mfma_f32_16x16x32_bf16 v[124:127],a[32:35],a[44:47],v[124:127]
v_mfma_f32_16x16x32_bf16 v[164:167],a[36:39],a[44:47],v[164:167]
s_add_u32 m0,s58,61440
ds_read_b128 a[76:79],v247 offset:60416
v_mfma_f32_16x16x32_bf16 v[8:11],a[20:23],a[48:51],v[8:11]
buffer_load_dwordx4 v240,s[24:27],s52 offen lds
v_mfma_f32_16x16x32_bf16 v[48:51],a[24:27],a[48:51],v[48:51]
v_mfma_f32_16x16x32_bf16 v[88:91],a[28:31],a[48:51],v[88:91]
s_add_u32 m0,s58,61440
v_mfma_f32_16x16x32_bf16 v[128:131],a[32:35],a[48:51],v[128:131]
buffer_load_dwordx4 v241,s[24:27],s52 offen offset:1024 lds
v_mfma_f32_16x16x32_bf16 v[168:171],a[36:39],a[48:51],v[168:171]
v_mfma_f32_16x16x32_bf16 v[12:15],a[20:23],a[52:55],v[12:15]
s_add_u32 m0,s58,61440
v_mfma_f32_16x16x32_bf16 v[52:55],a[24:27],a[52:55],v[52:55]
buffer_load_dwordx4 v242,s[24:27],s52 offen offset:2048 lds
v_mfma_f32_16x16x32_bf16 v[92:95],a[28:31],a[52:55],v[92:95]
v_mfma_f32_16x16x32_bf16 v[132:135],a[32:35],a[52:55],v[132:135]
s_add_u32 m0,s58,61440
v_mfma_f32_16x16x32_bf16 v[172:175],a[36:39],a[52:55],v[172:175]
buffer_load_dwordx4 v243,s[24:27],s52 offen offset:3072 lds
v_mfma_f32_16x16x32_bf16 v[16:19],a[20:23],a[56:59],v[16:19]
v_mfma_f32_16x16x32_bf16 v[56:59],a[24:27],a[56:59],v[56:59]
s_add_u32 m0,s58,65536
v_mfma_f32_16x16x32_bf16 v[96:99],a[28:31],a[56:59],v[96:99]
buffer_load_dwordx4 v244,s[24:27],s52 offen lds
v_mfma_f32_16x16x32_bf16 v[136:139],a[32:35],a[56:59],v[136:139]
s_add_u32 s52,s52,40960
v_mfma_f32_16x16x32_bf16 v[176:179],a[36:39],a[56:59],v[176:179]
s_waitcnt vmcnt(12) lgkmcnt(0)
s_barrier
v_mfma_f32_16x16x32_bf16 v[20:23],a[20:23],a[60:63],v[20:23]
ds_read_b128 a[40:43],v247
v_mfma_f32_16x16x32_bf16 v[60:63],a[24:27],a[60:63],v[60:63]
ds_read_b128 a[44:47],v247 offset:1024
v_mfma_f32_16x16x32_bf16 v[100:103],a[28:31],a[60:63],v[100:103]
s_add_u32 m0,s58,0
ds_read_b128 a[48:51],v247 offset:2048
v_mfma_f32_16x16x32_bf16 v[140:143],a[32:35],a[60:63],v[140:143]
buffer_load_dwordx4 v240,s[24:27],s52 offen lds
v_mfma_f32_16x16x32_bf16 v[180:183],a[36:39],a[60:63],v[180:183]
ds_read_b128 a[52:55],v247 offset:3072
ds_read_b128 a[56:59],v247 offset:4096
v_mfma_f32_16x16x32_bf16 v[24:27],a[20:23],a[64:67],v[24:27]
s_add_u32 m0,s58,0
ds_read_b64_tr_b16 a[0:1],v235
v_mfma_f32_16x16x32_bf16 v[64:67],a[24:27],a[64:67],v[64:67]
buffer_load_dwordx4 v241,s[24:27],s52 offen offset:1024 lds
v_mfma_f32_16x16x32_bf16 v[104:107],a[28:31],a[64:67],v[104:107]
ds_read_b64_tr_b16 a[2:3],v235 offset:10240
ds_read_b64_tr_b16 a[4:5],v236
v_mfma_f32_16x16x32_bf16 v[144:147],a[32:35],a[64:67],v[144:147]
s_add_u32 m0,s58,0
ds_read_b64_tr_b16 a[6:7],v236 offset:10240
v_mfma_f32_16x16x32_bf16 v[184:187],a[36:39],a[64:67],v[184:187]
buffer_load_dwordx4 v242,s[24:27],s52 offen offset:2048 lds
v_mfma_f32_16x16x32_bf16 v[28:31],a[20:23],a[68:71],v[28:31]
ds_read_b64_tr_b16 a[8:9],v237
ds_read_b64_tr_b16 a[10:11],v237 offset:10240
v_mfma_f32_16x16x32_bf16 v[68:71],a[24:27],a[68:71],v[68:71]
s_add_u32 m0,s58,0
ds_read_b64_tr_b16 a[12:13],v238
v_mfma_f32_16x16x32_bf16 v[108:111],a[28:31],a[68:71],v[108:111]
buffer_load_dwordx4 v243,s[24:27],s52 offen offset:3072 lds
v_mfma_f32_16x16x32_bf16 v[148:151],a[32:35],a[68:71],v[148:151]
ds_read_b64_tr_b16 a[14:15],v238 offset:10240
ds_read_b64_tr_b16 a[16:17],v239
v_mfma_f32_16x16x32_bf16 v[188:191],a[36:39],a[68:71],v[188:191]
s_add_u32 m0,s58,4096
ds_read_b64_tr_b16 a[18:19],v239 offset:10240
v_mfma_f32_16x16x32_bf16 v[32:35],a[20:23],a[72:75],v[32:35]
buffer_load_dwordx4 v244,s[24:27],s52 offen lds
v_mfma_f32_16x16x32_bf16 v[72:75],a[24:27],a[72:75],v[72:75]
s_add_u32 s52,s52,40960
v_mfma_f32_16x16x32_bf16 v[112:115],a[28:31],a[72:75],v[112:115]
v_mfma_f32_16x16x32_bf16 v[152:155],a[32:35],a[72:75],v[152:155]
v_mfma_f32_16x16x32_bf16 v[192:195],a[36:39],a[72:75],v[192:195]
v_mfma_f32_16x16x32_bf16 v[36:39],a[20:23],a[76:79],v[36:39]
v_mfma_f32_16x16x32_bf16 v[76:79],a[24:27],a[76:79],v[76:79]
v_mfma_f32_16x16x32_bf16 v[116:119],a[28:31],a[76:79],v[116:119]
v_mfma_f32_16x16x32_bf16 v[156:159],a[32:35],a[76:79],v[156:159]
v_mfma_f32_16x16x32_bf16 v[196:199],a[36:39],a[76:79],v[196:199]
s_waitcnt lgkmcnt(0)
v_mfma_f32_16x16x32_bf16 v[0:3],a[0:3],a[40:43],v[0:3]
v_mfma_f32_16x16x32_bf16 v[40:43],a[4:7],a[40:43],v[40:43]
buffer_load_dword a250,v254,s[28:31],s54 offen
v_mfma_f32_16x16x32_bf16 v[80:83],a[8:11],a[40:43],v[80:83]
s_add_u32 m0,s59,40960
ds_read_b128 a[60:63],v247 offset:5120
v_mfma_f32_16x16x32_bf16 v[120:123],a[12:15],a[40:43],v[120:123]
buffer_load_dwordx4 v245,s[20:23],s53 offen sc1 lds
v_mfma_f32_16x16x32_bf16 v[160:163],a[16:19],a[40:43],v[160:163]
s_add_u32 m0,s59,40960
ds_read_b128 a[64:67],v247 offset:6144
v_mfma_f32_16x16x32_bf16 v[4:7],a[0:3],a[44:47],v[4:7]
buffer_load_dwordx4 v245,s[20:23],s53 offen offset:1024 sc1 lds
v_mfma_f32_16x16x32_bf16 v[44:47],a[4:7],a[44:47],v[44:47]
s_add_u32 m0,s59,40960
ds_read_b128 a[68:71],v247 offset:7168
v_mfma_f32_16x16x32_bf16 v[84:87],a[8:11],a[44:47],v[84:87]
buffer_load_dwordx4 v245,s[20:23],s53 offen offset:2048 sc1 lds
v_mfma_f32_16x16x32_bf16 v[124:127],a[12:15],a[44:47],v[124:127]
s_add_u32 m0,s59,40960
ds_read_b128 a[72:75],v247 offset:8192
v_mfma_f32_16x16x32_bf16 v[164:167],a[16:19],a[44:47],v[164:167]
buffer_load_dwordx4 v245,s[20:23],s53 offen offset:3072 sc1 lds
v_mfma_f32_16x16x32_bf16 v[8:11],a[0:3],a[48:51],v[8:11]
s_add_u32 m0,s59,45056
ds_read_b128 a[76:79],v247 offset:9216
v_mfma_f32_16x16x32_bf16 v[48:51],a[4:7],a[48:51],v[48:51]
s_add_u32 s67,s53,4096
v_mfma_f32_16x16x32_bf16 v[88:91],a[8:11],a[48:51],v[88:91]
buffer_load_dwordx4 v245,s[20:23],s67 offen sc1 lds
v_mfma_f32_16x16x32_bf16 v[128:131],a[12:15],a[48:51],v[128:131]
s_add_u32 s53,s53,s75
v_mfma_f32_16x16x32_bf16 v[168:171],a[16:19],a[48:51],v[168:171]
s_add_u32 s54,s54,81920
v_mfma_f32_16x16x32_bf16 v[12:15],a[0:3],a[52:55],v[12:15]
v_mfma_f32_16x16x32_bf16 v[52:55],a[4:7],a[52:55],v[52:55]
v_mfma_f32_16x16x32_bf16 v[92:95],a[8:11],a[52:55],v[92:95]
v_mfma_f32_16x16x32_bf16 v[132:135],a[12:15],a[52:55],v[132:135]
v_mfma_f32_16x16x32_bf16 v[172:175],a[16:19],a[52:55],v[172:175]
v_mfma_f32_16x16x32_bf16 v[16:19],a[0:3],a[56:59],v[16:19]
v_mfma_f32_16x16x32_bf16 v[56:59],a[4:7],a[56:59],v[56:59]
v_mfma_f32_16x16x32_bf16 v[96:99],a[8:11],a[56:59],v[96:99]
v_mfma_f32_16x16x32_bf16 v[136:139],a[12:15],a[56:59],v[136:139]
v_mfma_f32_16x16x32_bf16 v[176:179],a[16:19],a[56:59],v[176:179]
s_waitcnt vmcnt(11) lgkmcnt(0)
s_barrier
v_mfma_f32_16x16x32_bf16 v[20:23],a[0:3],a[60:63],v[20:23]
ds_read_b128 a[40:43],v247 offset:10240
v_mfma_f32_16x16x32_bf16 v[60:63],a[4:7],a[60:63],v[60:63]
ds_read_b128 a[44:47],v247 offset:11264
v_mfma_f32_16x16x32_bf16 v[100:103],a[8:11],a[60:63],v[100:103]
ds_read_b128 a[48:51],v247 offset:12288
v_mfma_f32_16x16x32_bf16 v[140:143],a[12:15],a[60:63],v[140:143]
ds_read_b128 a[52:55],v247 offset:13312
v_mfma_f32_16x16x32_bf16 v[180:183],a[16:19],a[60:63],v[180:183]
ds_read_b128 a[56:59],v247 offset:14336
v_mfma_f32_16x16x32_bf16 v[24:27],a[0:3],a[64:67],v[24:27]
ds_read_b64_tr_b16 a[20:21],v235 offset:20480
v_mfma_f32_16x16x32_bf16 v[64:67],a[4:7],a[64:67],v[64:67]
ds_read_b64_tr_b16 a[22:23],v235 offset:30720
v_mfma_f32_16x16x32_bf16 v[104:107],a[8:11],a[64:67],v[104:107]
ds_read_b64_tr_b16 a[24:25],v236 offset:20480
v_mfma_f32_16x16x32_bf16 v[144:147],a[12:15],a[64:67],v[144:147]
ds_read_b64_tr_b16 a[26:27],v236 offset:30720
v_mfma_f32_16x16x32_bf16 v[184:187],a[16:19],a[64:67],v[184:187]
ds_read_b64_tr_b16 a[28:29],v237 offset:20480
v_mfma_f32_16x16x32_bf16 v[28:31],a[0:3],a[68:71],v[28:31]
ds_read_b64_tr_b16 a[30:31],v237 offset:30720
v_mfma_f32_16x16x32_bf16 v[68:71],a[4:7],a[68:71],v[68:71]
ds_read_b64_tr_b16 a[32:33],v238 offset:20480
v_mfma_f32_16x16x32_bf16 v[108:111],a[8:11],a[68:71],v[108:111]
ds_read_b64_tr_b16 a[34:35],v238 offset:30720
v_mfma_f32_16x16x32_bf16 v[148:151],a[12:15],a[68:71],v[148:151]
ds_read_b64_tr_b16 a[36:37],v239 offset:20480
v_mfma_f32_16x16x32_bf16 v[188:191],a[16:19],a[68:71],v[188:191]
ds_read_b64_tr_b16 a[38:39],v239 offset:30720
v_mfma_f32_16x16x32_bf16 v[32:35],a[0:3],a[72:75],v[32:35]
s_waitcnt vmcnt(16)
v_readfirstlane_b32 s70,v246
v_mfma_f32_16x16x32_bf16 v[72:75],a[4:7],a[72:75],v[72:75]
v_mfma_f32_16x16x32_bf16 v[112:115],a[8:11],a[72:75],v[112:115]
s_cmp_eq_u32 s70,0
s_cbranch_scc1 LOSPIN0_%=
LOBACK0_%=:
v_mfma_f32_16x16x32_bf16 v[152:155],a[12:15],a[72:75],v[152:155]
v_mfma_f32_16x16x32_bf16 v[192:195],a[16:19],a[72:75],v[192:195]
v_mfma_f32_16x16x32_bf16 v[36:39],a[0:3],a[76:79],v[36:39]
v_mfma_f32_16x16x32_bf16 v[76:79],a[4:7],a[76:79],v[76:79]
v_mfma_f32_16x16x32_bf16 v[116:119],a[8:11],a[76:79],v[116:119]
v_mfma_f32_16x16x32_bf16 v[156:159],a[12:15],a[76:79],v[156:159]
v_mfma_f32_16x16x32_bf16 v[196:199],a[16:19],a[76:79],v[196:199]
s_waitcnt lgkmcnt(0)
v_mfma_f32_16x16x32_bf16 v[0:3],a[20:23],a[40:43],v[0:3]
v_mfma_f32_16x16x32_bf16 v[40:43],a[24:27],a[40:43],v[40:43]
ds_read_b128 a[60:63],v247 offset:15360
v_mfma_f32_16x16x32_bf16 v[80:83],a[28:31],a[40:43],v[80:83]
v_mfma_f32_16x16x32_bf16 v[120:123],a[32:35],a[40:43],v[120:123]
ds_read_b128 a[64:67],v247 offset:16384
v_mfma_f32_16x16x32_bf16 v[160:163],a[36:39],a[40:43],v[160:163]
v_mfma_f32_16x16x32_bf16 v[4:7],a[20:23],a[44:47],v[4:7]
ds_read_b128 a[68:71],v247 offset:17408
v_mfma_f32_16x16x32_bf16 v[44:47],a[24:27],a[44:47],v[44:47]
v_mfma_f32_16x16x32_bf16 v[84:87],a[28:31],a[44:47],v[84:87]
ds_read_b128 a[72:75],v247 offset:18432
v_mfma_f32_16x16x32_bf16 v[124:127],a[32:35],a[44:47],v[124:127]
v_mfma_f32_16x16x32_bf16 v[164:167],a[36:39],a[44:47],v[164:167]
s_add_u32 m0,s58,20480
ds_read_b128 a[76:79],v247 offset:19456
v_mfma_f32_16x16x32_bf16 v[8:11],a[20:23],a[48:51],v[8:11]
buffer_load_dwordx4 v240,s[24:27],s52 offen lds
v_mfma_f32_16x16x32_bf16 v[48:51],a[24:27],a[48:51],v[48:51]
v_mfma_f32_16x16x32_bf16 v[88:91],a[28:31],a[48:51],v[88:91]
s_add_u32 m0,s58,20480
v_mfma_f32_16x16x32_bf16 v[128:131],a[32:35],a[48:51],v[128:131]
buffer_load_dwordx4 v241,s[24:27],s52 offen offset:1024 lds
v_mfma_f32_16x16x32_bf16 v[168:171],a[36:39],a[48:51],v[168:171]
v_mfma_f32_16x16x32_bf16 v[12:15],a[20:23],a[52:55],v[12:15]
s_add_u32 m0,s58,20480
v_mfma_f32_16x16x32_bf16 v[52:55],a[24:27],a[52:55],v[52:55]
buffer_load_dwordx4 v242,s[24:27],s52 offen offset:2048 lds
v_mfma_f32_16x16x32_bf16 v[92:95],a[28:31],a[52:55],v[92:95]
v_mfma_f32_16x16x32_bf16 v[132:135],a[32:35],a[52:55],v[132:135]
s_add_u32 m0,s58,20480
v_mfma_f32_16x16x32_bf16 v[172:175],a[36:39],a[52:55],v[172:175]
buffer_load_dwordx4 v243,s[24:27],s52 offen offset:3072 lds
v_mfma_f32_16x16x32_bf16 v[16:19],a[20:23],a[56:59],v[16:19]
v_mfma_f32_16x16x32_bf16 v[56:59],a[24:27],a[56:59],v[56:59]
s_add_u32 m0,s58,24576
v_mfma_f32_16x16x32_bf16 v[96:99],a[28:31],a[56:59],v[96:99]
buffer_load_dwordx4 v244,s[24:27],s52 offen lds
v_mfma_f32_16x16x32_bf16 v[136:139],a[32:35],a[56:59],v[136:139]
s_add_u32 s52,s52,40960
v_mfma_f32_16x16x32_bf16 v[176:179],a[36:39],a[56:59],v[176:179]
s_waitcnt vmcnt(11) lgkmcnt(0)
s_barrier
v_mfma_f32_16x16x32_bf16 v[20:23],a[20:23],a[60:63],v[20:23]
ds_read_b128 a[40:43],v247 offset:20480
v_mfma_f32_16x16x32_bf16 v[60:63],a[24:27],a[60:63],v[60:63]
ds_read_b128 a[44:47],v247 offset:21504
v_mfma_f32_16x16x32_bf16 v[100:103],a[28:31],a[60:63],v[100:103]
s_add_u32 m0,s58,40960
ds_read_b128 a[48:51],v247 offset:22528
v_mfma_f32_16x16x32_bf16 v[140:143],a[32:35],a[60:63],v[140:143]
buffer_load_dwordx4 v240,s[24:27],s52 offen lds
v_mfma_f32_16x16x32_bf16 v[180:183],a[36:39],a[60:63],v[180:183]
ds_read_b128 a[52:55],v247 offset:23552
ds_read_b128 a[56:59],v247 offset:24576
v_mfma_f32_16x16x32_bf16 v[24:27],a[20:23],a[64:67],v[24:27]
s_add_u32 m0,s58,40960
ds_read_b64_tr_b16 a[0:1],v230
v_mfma_f32_16x16x32_bf16 v[64:67],a[24:27],a[64:67],v[64:67]
buffer_load_dwordx4 v241,s[24:27],s52 offen offset:1024 lds
v_mfma_f32_16x16x32_bf16 v[104:107],a[28:31],a[64:67],v[104:107]
ds_read_b64_tr_b16 a[2:3],v230 offset:10240
ds_read_b64_tr_b16 a[4:5],v231
v_mfma_f32_16x16x32_bf16 v[144:147],a[32:35],a[64:67],v[144:147]
s_add_u32 m0,s58,40960
ds_read_b64_tr_b16 a[6:7],v231 offset:10240
v_mfma_f32_16x16x32_bf16 v[184:187],a[36:39],a[64:67],v[184:187]
buffer_load_dwordx4 v242,s[24:27],s52 offen offset:2048 lds
v_mfma_f32_16x16x32_bf16 v[28:31],a[20:23],a[68:71],v[28:31]
ds_read_b64_tr_b16 a[8:9],v232
ds_read_b64_tr_b16 a[10:11],v232 offset:10240
v_mfma_f32_16x16x32_bf16 v[68:71],a[24:27],a[68:71],v[68:71]
s_add_u32 m0,s58,40960
ds_read_b64_tr_b16 a[12:13],v233
v_mfma_f32_16x16x32_bf16 v[108:111],a[28:31],a[68:71],v[108:111]
buffer_load_dwordx4 v243,s[24:27],s52 offen offset:3072 lds
v_mfma_f32_16x16x32_bf16 v[148:151],a[32:35],a[68:71],v[148:151]
ds_read_b64_tr_b16 a[14:15],v233 offset:10240
ds_read_b64_tr_b16 a[16:17],v234
v_mfma_f32_16x16x32_bf16 v[188:191],a[36:39],a[68:71],v[188:191]
s_add_u32 m0,s58,45056
ds_read_b64_tr_b16 a[18:19],v234 offset:10240
v_mfma_f32_16x16x32_bf16 v[32:35],a[20:23],a[72:75],v[32:35]
buffer_load_dwordx4 v244,s[24:27],s52 offen lds
v_mfma_f32_16x16x32_bf16 v[72:75],a[24:27],a[72:75],v[72:75]
s_add_u32 s52,s52,40960
v_mfma_f32_16x16x32_bf16 v[112:115],a[28:31],a[72:75],v[112:115]
v_mfma_f32_16x16x32_bf16 v[152:155],a[32:35],a[72:75],v[152:155]
v_mfma_f32_16x16x32_bf16 v[192:195],a[36:39],a[72:75],v[192:195]
v_mfma_f32_16x16x32_bf16 v[36:39],a[20:23],a[76:79],v[36:39]
v_mfma_f32_16x16x32_bf16 v[76:79],a[24:27],a[76:79],v[76:79]
v_mfma_f32_16x16x32_bf16 v[116:119],a[28:31],a[76:79],v[116:119]
v_mfma_f32_16x16x32_bf16 v[156:159],a[32:35],a[76:79],v[156:159]
v_mfma_f32_16x16x32_bf16 v[196:199],a[36:39],a[76:79],v[196:199]
s_waitcnt lgkmcnt(0)
v_mfma_f32_16x16x32_bf16 v[0:3],a[0:3],a[40:43],v[0:3]
v_mfma_f32_16x16x32_bf16 v[40:43],a[4:7],a[40:43],v[40:43]
buffer_load_dword a250,v254,s[28:31],s54 offen
v_mfma_f32_16x16x32_bf16 v[80:83],a[8:11],a[40:43],v[80:83]
s_add_u32 m0,s59,0
ds_read_b128 a[60:63],v247 offset:25600
v_mfma_f32_16x16x32_bf16 v[120:123],a[12:15],a[40:43],v[120:123]
buffer_load_dwordx4 v245,s[20:23],s53 offen sc1 lds
v_mfma_f32_16x16x32_bf16 v[160:163],a[16:19],a[40:43],v[160:163]
s_add_u32 m0,s59,0
ds_read_b128 a[64:67],v247 offset:26624
v_mfma_f32_16x16x32_bf16 v[4:7],a[0:3],a[44:47],v[4:7]
buffer_load_dwordx4 v245,s[20:23],s53 offen offset:1024 sc1 lds
v_mfma_f32_16x16x32_bf16 v[44:47],a[4:7],a[44:47],v[44:47]
s_add_u32 m0,s59,0
ds_read_b128 a[68:71],v247 offset:27648
v_mfma_f32_16x16x32_bf16 v[84:87],a[8:11],a[44:47],v[84:87]
buffer_load_dwordx4 v245,s[20:23],s53 offen offset:2048 sc1 lds
v_mfma_f32_16x16x32_bf16 v[124:127],a[12:15],a[44:47],v[124:127]
s_add_u32 m0,s59,0
ds_read_b128 a[72:75],v247 offset:28672
v_mfma_f32_16x16x32_bf16 v[164:167],a[16:19],a[44:47],v[164:167]
buffer_load_dwordx4 v245,s[20:23],s53 offen offset:3072 sc1 lds
v_mfma_f32_16x16x32_bf16 v[8:11],a[0:3],a[48:51],v[8:11]
s_add_u32 m0,s59,4096
ds_read_b128 a[76:79],v247 offset:29696
v_mfma_f32_16x16x32_bf16 v[48:51],a[4:7],a[48:51],v[48:51]
s_add_u32 s67,s53,4096
v_mfma_f32_16x16x32_bf16 v[88:91],a[8:11],a[48:51],v[88:91]
buffer_load_dwordx4 v245,s[20:23],s67 offen sc1 lds
v_mfma_f32_16x16x32_bf16 v[128:131],a[12:15],a[48:51],v[128:131]
s_add_u32 s53,s53,20480
v_mfma_f32_16x16x32_bf16 v[168:171],a[16:19],a[48:51],v[168:171]
s_add_u32 s54,s54,81920
v_mfma_f32_16x16x32_bf16 v[12:15],a[0:3],a[52:55],v[12:15]
v_mfma_f32_16x16x32_bf16 v[52:55],a[4:7],a[52:55],v[52:55]
v_mfma_f32_16x16x32_bf16 v[92:95],a[8:11],a[52:55],v[92:95]
v_mfma_f32_16x16x32_bf16 v[132:135],a[12:15],a[52:55],v[132:135]
v_mfma_f32_16x16x32_bf16 v[172:175],a[16:19],a[52:55],v[172:175]
v_mfma_f32_16x16x32_bf16 v[16:19],a[0:3],a[56:59],v[16:19]
v_mfma_f32_16x16x32_bf16 v[56:59],a[4:7],a[56:59],v[56:59]
v_mfma_f32_16x16x32_bf16 v[96:99],a[8:11],a[56:59],v[96:99]
v_mfma_f32_16x16x32_bf16 v[136:139],a[12:15],a[56:59],v[136:139]
v_mfma_f32_16x16x32_bf16 v[176:179],a[16:19],a[56:59],v[176:179]
s_waitcnt vmcnt(11) lgkmcnt(0)
s_barrier
v_mfma_f32_16x16x32_bf16 v[20:23],a[0:3],a[60:63],v[20:23]
ds_read_b128 a[40:43],v247 offset:30720
v_mfma_f32_16x16x32_bf16 v[60:63],a[4:7],a[60:63],v[60:63]
ds_read_b128 a[44:47],v247 offset:31744
v_mfma_f32_16x16x32_bf16 v[100:103],a[8:11],a[60:63],v[100:103]
ds_read_b128 a[48:51],v247 offset:32768
v_mfma_f32_16x16x32_bf16 v[140:143],a[12:15],a[60:63],v[140:143]
ds_read_b128 a[52:55],v247 offset:33792
v_mfma_f32_16x16x32_bf16 v[180:183],a[16:19],a[60:63],v[180:183]
ds_read_b128 a[56:59],v247 offset:34816
v_mfma_f32_16x16x32_bf16 v[24:27],a[0:3],a[64:67],v[24:27]
ds_read_b64_tr_b16 a[20:21],v230 offset:20480
v_mfma_f32_16x16x32_bf16 v[64:67],a[4:7],a[64:67],v[64:67]
ds_read_b64_tr_b16 a[22:23],v230 offset:30720
v_mfma_f32_16x16x32_bf16 v[104:107],a[8:11],a[64:67],v[104:107]
ds_read_b64_tr_b16 a[24:25],v231 offset:20480
v_mfma_f32_16x16x32_bf16 v[144:147],a[12:15],a[64:67],v[144:147]
ds_read_b64_tr_b16 a[26:27],v231 offset:30720
v_mfma_f32_16x16x32_bf16 v[184:187],a[16:19],a[64:67],v[184:187]
ds_read_b64_tr_b16 a[28:29],v232 offset:20480
v_mfma_f32_16x16x32_bf16 v[28:31],a[0:3],a[68:71],v[28:31]
ds_read_b64_tr_b16 a[30:31],v232 offset:30720
v_mfma_f32_16x16x32_bf16 v[68:71],a[4:7],a[68:71],v[68:71]
ds_read_b64_tr_b16 a[32:33],v233 offset:20480
v_mfma_f32_16x16x32_bf16 v[108:111],a[8:11],a[68:71],v[108:111]
ds_read_b64_tr_b16 a[34:35],v233 offset:30720
v_mfma_f32_16x16x32_bf16 v[148:151],a[12:15],a[68:71],v[148:151]
ds_read_b64_tr_b16 a[36:37],v234 offset:20480
v_mfma_f32_16x16x32_bf16 v[188:191],a[16:19],a[68:71],v[188:191]
ds_read_b64_tr_b16 a[38:39],v234 offset:30720
v_mfma_f32_16x16x32_bf16 v[32:35],a[0:3],a[72:75],v[32:35]
v_mfma_f32_16x16x32_bf16 v[72:75],a[4:7],a[72:75],v[72:75]
v_mfma_f32_16x16x32_bf16 v[112:115],a[8:11],a[72:75],v[112:115]
v_mfma_f32_16x16x32_bf16 v[152:155],a[12:15],a[72:75],v[152:155]
v_mfma_f32_16x16x32_bf16 v[192:195],a[16:19],a[72:75],v[192:195]
v_mfma_f32_16x16x32_bf16 v[36:39],a[0:3],a[76:79],v[36:39]
v_mfma_f32_16x16x32_bf16 v[76:79],a[4:7],a[76:79],v[76:79]
v_mfma_f32_16x16x32_bf16 v[116:119],a[8:11],a[76:79],v[116:119]
v_mfma_f32_16x16x32_bf16 v[156:159],a[12:15],a[76:79],v[156:159]
v_mfma_f32_16x16x32_bf16 v[196:199],a[16:19],a[76:79],v[196:199]
s_waitcnt lgkmcnt(0)
v_mfma_f32_16x16x32_bf16 v[0:3],a[20:23],a[40:43],v[0:3]
v_mfma_f32_16x16x32_bf16 v[40:43],a[24:27],a[40:43],v[40:43]
ds_read_b128 a[60:63],v247 offset:35840
v_mfma_f32_16x16x32_bf16 v[80:83],a[28:31],a[40:43],v[80:83]
v_mfma_f32_16x16x32_bf16 v[120:123],a[32:35],a[40:43],v[120:123]
ds_read_b128 a[64:67],v247 offset:36864
v_mfma_f32_16x16x32_bf16 v[160:163],a[36:39],a[40:43],v[160:163]
v_mfma_f32_16x16x32_bf16 v[4:7],a[20:23],a[44:47],v[4:7]
ds_read_b128 a[68:71],v247 offset:37888
v_mfma_f32_16x16x32_bf16 v[44:47],a[24:27],a[44:47],v[44:47]
v_mfma_f32_16x16x32_bf16 v[84:87],a[28:31],a[44:47],v[84:87]
ds_read_b128 a[72:75],v247 offset:38912
v_mfma_f32_16x16x32_bf16 v[124:127],a[32:35],a[44:47],v[124:127]
v_mfma_f32_16x16x32_bf16 v[164:167],a[36:39],a[44:47],v[164:167]
s_add_u32 m0,s58,61440
ds_read_b128 a[76:79],v247 offset:39936
v_mfma_f32_16x16x32_bf16 v[8:11],a[20:23],a[48:51],v[8:11]
buffer_load_dwordx4 v240,s[24:27],s52 offen lds
v_mfma_f32_16x16x32_bf16 v[48:51],a[24:27],a[48:51],v[48:51]
v_mfma_f32_16x16x32_bf16 v[88:91],a[28:31],a[48:51],v[88:91]
s_add_u32 m0,s58,61440
v_mfma_f32_16x16x32_bf16 v[128:131],a[32:35],a[48:51],v[128:131]
buffer_load_dwordx4 v241,s[24:27],s52 offen offset:1024 lds
v_mfma_f32_16x16x32_bf16 v[168:171],a[36:39],a[48:51],v[168:171]
v_mfma_f32_16x16x32_bf16 v[12:15],a[20:23],a[52:55],v[12:15]
s_add_u32 m0,s58,61440
v_mfma_f32_16x16x32_bf16 v[52:55],a[24:27],a[52:55],v[52:55]
buffer_load_dwordx4 v242,s[24:27],s52 offen offset:2048 lds
v_mfma_f32_16x16x32_bf16 v[92:95],a[28:31],a[52:55],v[92:95]
v_mfma_f32_16x16x32_bf16 v[132:135],a[32:35],a[52:55],v[132:135]
s_add_u32 m0,s58,61440
v_mfma_f32_16x16x32_bf16 v[172:175],a[36:39],a[52:55],v[172:175]
buffer_load_dwordx4 v243,s[24:27],s52 offen offset:3072 lds
v_mfma_f32_16x16x32_bf16 v[16:19],a[20:23],a[56:59],v[16:19]
v_mfma_f32_16x16x32_bf16 v[56:59],a[24:27],a[56:59],v[56:59]
s_add_u32 m0,s58,65536
v_mfma_f32_16x16x32_bf16 v[96:99],a[28:31],a[56:59],v[96:99]
buffer_load_dwordx4 v244,s[24:27],s52 offen lds
v_mfma_f32_16x16x32_bf16 v[136:139],a[32:35],a[56:59],v[136:139]
s_add_u32 s52,s52,s74
v_mfma_f32_16x16x32_bf16 v[176:179],a[36:39],a[56:59],v[176:179]
s_waitcnt vmcnt(11) lgkmcnt(0)
s_barrier
v_mfma_f32_16x16x32_bf16 v[20:23],a[20:23],a[60:63],v[20:23]
ds_read_b128 a[40:43],v247 offset:40960
v_mfma_f32_16x16x32_bf16 v[60:63],a[24:27],a[60:63],v[60:63]
ds_read_b128 a[44:47],v247 offset:41984
v_mfma_f32_16x16x32_bf16 v[100:103],a[28:31],a[60:63],v[100:103]
s_add_u32 m0,s58,0
ds_read_b128 a[48:51],v247 offset:43008
v_mfma_f32_16x16x32_bf16 v[140:143],a[32:35],a[60:63],v[140:143]
buffer_load_dwordx4 v240,s[24:27],s52 offen lds
v_mfma_f32_16x16x32_bf16 v[180:183],a[36:39],a[60:63],v[180:183]
ds_read_b128 a[52:55],v247 offset:44032
ds_read_b128 a[56:59],v247 offset:45056
v_mfma_f32_16x16x32_bf16 v[24:27],a[20:23],a[64:67],v[24:27]
s_add_u32 m0,s58,0
ds_read_b64_tr_b16 a[0:1],v235
v_mfma_f32_16x16x32_bf16 v[64:67],a[24:27],a[64:67],v[64:67]
buffer_load_dwordx4 v241,s[24:27],s52 offen offset:1024 lds
v_mfma_f32_16x16x32_bf16 v[104:107],a[28:31],a[64:67],v[104:107]
ds_read_b64_tr_b16 a[2:3],v235 offset:10240
ds_read_b64_tr_b16 a[4:5],v236
v_mfma_f32_16x16x32_bf16 v[144:147],a[32:35],a[64:67],v[144:147]
s_add_u32 m0,s58,0
ds_read_b64_tr_b16 a[6:7],v236 offset:10240
v_mfma_f32_16x16x32_bf16 v[184:187],a[36:39],a[64:67],v[184:187]
buffer_load_dwordx4 v242,s[24:27],s52 offen offset:2048 lds
v_mfma_f32_16x16x32_bf16 v[28:31],a[20:23],a[68:71],v[28:31]
ds_read_b64_tr_b16 a[8:9],v237
ds_read_b64_tr_b16 a[10:11],v237 offset:10240
v_mfma_f32_16x16x32_bf16 v[68:71],a[24:27],a[68:71],v[68:71]
s_add_u32 m0,s58,0
ds_read_b64_tr_b16 a[12:13],v238
v_mfma_f32_16x16x32_bf16 v[108:111],a[28:31],a[68:71],v[108:111]
buffer_load_dwordx4 v243,s[24:27],s52 offen offset:3072 lds
v_mfma_f32_16x16x32_bf16 v[148:151],a[32:35],a[68:71],v[148:151]
ds_read_b64_tr_b16 a[14:15],v238 offset:10240
ds_read_b64_tr_b16 a[16:17],v239
v_mfma_f32_16x16x32_bf16 v[188:191],a[36:39],a[68:71],v[188:191]
s_add_u32 m0,s58,4096
ds_read_b64_tr_b16 a[18:19],v239 offset:10240
v_mfma_f32_16x16x32_bf16 v[32:35],a[20:23],a[72:75],v[32:35]
buffer_load_dwordx4 v244,s[24:27],s52 offen lds
v_mfma_f32_16x16x32_bf16 v[72:75],a[24:27],a[72:75],v[72:75]
s_add_u32 s52,s52,40960
v_mfma_f32_16x16x32_bf16 v[112:115],a[28:31],a[72:75],v[112:115]
v_mfma_f32_16x16x32_bf16 v[152:155],a[32:35],a[72:75],v[152:155]
v_mfma_f32_16x16x32_bf16 v[192:195],a[36:39],a[72:75],v[192:195]
v_mfma_f32_16x16x32_bf16 v[36:39],a[20:23],a[76:79],v[36:39]
v_mfma_f32_16x16x32_bf16 v[76:79],a[24:27],a[76:79],v[76:79]
v_mfma_f32_16x16x32_bf16 v[116:119],a[28:31],a[76:79],v[116:119]
v_mfma_f32_16x16x32_bf16 v[156:159],a[32:35],a[76:79],v[156:159]
v_mfma_f32_16x16x32_bf16 v[196:199],a[36:39],a[76:79],v[196:199]
s_waitcnt lgkmcnt(0)
v_mfma_f32_16x16x32_bf16 v[0:3],a[0:3],a[40:43],v[0:3]
v_mfma_f32_16x16x32_bf16 v[40:43],a[4:7],a[40:43],v[40:43]
buffer_load_dword a250,v254,s[28:31],s54 offen
v_mfma_f32_16x16x32_bf16 v[80:83],a[8:11],a[40:43],v[80:83]
s_add_u32 m0,s59,20480
ds_read_b128 a[60:63],v247 offset:46080
v_mfma_f32_16x16x32_bf16 v[120:123],a[12:15],a[40:43],v[120:123]
buffer_load_dwordx4 v245,s[20:23],s53 offen sc1 lds
v_mfma_f32_16x16x32_bf16 v[160:163],a[16:19],a[40:43],v[160:163]
s_add_u32 m0,s59,20480
ds_read_b128 a[64:67],v247 offset:47104
v_mfma_f32_16x16x32_bf16 v[4:7],a[0:3],a[44:47],v[4:7]
buffer_load_dwordx4 v245,s[20:23],s53 offen offset:1024 sc1 lds
v_mfma_f32_16x16x32_bf16 v[44:47],a[4:7],a[44:47],v[44:47]
s_add_u32 m0,s59,20480
ds_read_b128 a[68:71],v247 offset:48128
v_mfma_f32_16x16x32_bf16 v[84:87],a[8:11],a[44:47],v[84:87]
buffer_load_dwordx4 v245,s[20:23],s53 offen offset:2048 sc1 lds
v_mfma_f32_16x16x32_bf16 v[124:127],a[12:15],a[44:47],v[124:127]
s_add_u32 m0,s59,20480
ds_read_b128 a[72:75],v247 offset:49152
v_mfma_f32_16x16x32_bf16 v[164:167],a[16:19],a[44:47],v[164:167]
buffer_load_dwordx4 v245,s[20:23],s53 offen offset:3072 sc1 lds
v_mfma_f32_16x16x32_bf16 v[8:11],a[0:3],a[48:51],v[8:11]
s_add_u32 m0,s59,24576
ds_read_b128 a[76:79],v247 offset:50176
v_mfma_f32_16x16x32_bf16 v[48:51],a[4:7],a[48:51],v[48:51]
s_add_u32 s67,s53,4096
v_mfma_f32_16x16x32_bf16 v[88:91],a[8:11],a[48:51],v[88:91]
buffer_load_dwordx4 v245,s[20:23],s67 offen sc1 lds
v_mfma_f32_16x16x32_bf16 v[128:131],a[12:15],a[48:51],v[128:131]
s_add_u32 s53,s53,20480
v_mfma_f32_16x16x32_bf16 v[168:171],a[16:19],a[48:51],v[168:171]
s_add_u32 s54,s54,81920
v_mfma_f32_16x16x32_bf16 v[12:15],a[0:3],a[52:55],v[12:15]
v_mfma_f32_16x16x32_bf16 v[52:55],a[4:7],a[52:55],v[52:55]
v_mfma_f32_16x16x32_bf16 v[92:95],a[8:11],a[52:55],v[92:95]
v_mfma_f32_16x16x32_bf16 v[132:135],a[12:15],a[52:55],v[132:135]
v_mfma_f32_16x16x32_bf16 v[172:175],a[16:19],a[52:55],v[172:175]
v_mfma_f32_16x16x32_bf16 v[16:19],a[0:3],a[56:59],v[16:19]
v_mfma_f32_16x16x32_bf16 v[56:59],a[4:7],a[56:59],v[56:59]
v_mfma_f32_16x16x32_bf16 v[96:99],a[8:11],a[56:59],v[96:99]
v_mfma_f32_16x16x32_bf16 v[136:139],a[12:15],a[56:59],v[136:139]
v_mfma_f32_16x16x32_bf16 v[176:179],a[16:19],a[56:59],v[176:179]
s_waitcnt vmcnt(11) lgkmcnt(0)
s_barrier
v_mfma_f32_16x16x32_bf16 v[20:23],a[0:3],a[60:63],v[20:23]
ds_read_b128 a[40:43],v247 offset:51200
v_mfma_f32_16x16x32_bf16 v[60:63],a[4:7],a[60:63],v[60:63]
ds_read_b128 a[44:47],v247 offset:52224
v_mfma_f32_16x16x32_bf16 v[100:103],a[8:11],a[60:63],v[100:103]
ds_read_b128 a[48:51],v247 offset:53248
v_mfma_f32_16x16x32_bf16 v[140:143],a[12:15],a[60:63],v[140:143]
ds_read_b128 a[52:55],v247 offset:54272
v_mfma_f32_16x16x32_bf16 v[180:183],a[16:19],a[60:63],v[180:183]
ds_read_b128 a[56:59],v247 offset:55296
v_mfma_f32_16x16x32_bf16 v[24:27],a[0:3],a[64:67],v[24:27]
ds_read_b64_tr_b16 a[20:21],v235 offset:20480
v_mfma_f32_16x16x32_bf16 v[64:67],a[4:7],a[64:67],v[64:67]
ds_read_b64_tr_b16 a[22:23],v235 offset:30720
v_mfma_f32_16x16x32_bf16 v[104:107],a[8:11],a[64:67],v[104:107]
ds_read_b64_tr_b16 a[24:25],v236 offset:20480
v_mfma_f32_16x16x32_bf16 v[144:147],a[12:15],a[64:67],v[144:147]
ds_read_b64_tr_b16 a[26:27],v236 offset:30720
v_mfma_f32_16x16x32_bf16 v[184:187],a[16:19],a[64:67],v[184:187]
ds_read_b64_tr_b16 a[28:29],v237 offset:20480
v_mfma_f32_16x16x32_bf16 v[28:31],a[0:3],a[68:71],v[28:31]
ds_read_b64_tr_b16 a[30:31],v237 offset:30720
v_mfma_f32_16x16x32_bf16 v[68:71],a[4:7],a[68:71],v[68:71]
ds_read_b64_tr_b16 a[32:33],v238 offset:20480
v_mfma_f32_16x16x32_bf16 v[108:111],a[8:11],a[68:71],v[108:111]
ds_read_b64_tr_b16 a[34:35],v238 offset:30720
v_mfma_f32_16x16x32_bf16 v[148:151],a[12:15],a[68:71],v[148:151]
ds_read_b64_tr_b16 a[36:37],v239 offset:20480
v_mfma_f32_16x16x32_bf16 v[188:191],a[16:19],a[68:71],v[188:191]
ds_read_b64_tr_b16 a[38:39],v239 offset:30720
v_mfma_f32_16x16x32_bf16 v[32:35],a[0:3],a[72:75],v[32:35]
v_mfma_f32_16x16x32_bf16 v[72:75],a[4:7],a[72:75],v[72:75]
v_mfma_f32_16x16x32_bf16 v[112:115],a[8:11],a[72:75],v[112:115]
v_mfma_f32_16x16x32_bf16 v[152:155],a[12:15],a[72:75],v[152:155]
v_mfma_f32_16x16x32_bf16 v[192:195],a[16:19],a[72:75],v[192:195]
v_mfma_f32_16x16x32_bf16 v[36:39],a[0:3],a[76:79],v[36:39]
v_mfma_f32_16x16x32_bf16 v[76:79],a[4:7],a[76:79],v[76:79]
v_mfma_f32_16x16x32_bf16 v[116:119],a[8:11],a[76:79],v[116:119]
v_mfma_f32_16x16x32_bf16 v[156:159],a[12:15],a[76:79],v[156:159]
v_mfma_f32_16x16x32_bf16 v[196:199],a[16:19],a[76:79],v[196:199]
s_waitcnt lgkmcnt(0)
v_mfma_f32_16x16x32_bf16 v[0:3],a[20:23],a[40:43],v[0:3]
v_mfma_f32_16x16x32_bf16 v[40:43],a[24:27],a[40:43],v[40:43]
ds_read_b128 a[60:63],v247 offset:56320
v_mfma_f32_16x16x32_bf16 v[80:83],a[28:31],a[40:43],v[80:83]
v_mfma_f32_16x16x32_bf16 v[120:123],a[32:35],a[40:43],v[120:123]
ds_read_b128 a[64:67],v247 offset:57344
v_mfma_f32_16x16x32_bf16 v[160:163],a[36:39],a[40:43],v[160:163]
v_mfma_f32_16x16x32_bf16 v[4:7],a[20:23],a[44:47],v[4:7]
ds_read_b128 a[68:71],v247 offset:58368
v_mfma_f32_16x16x32_bf16 v[44:47],a[24:27],a[44:47],v[44:47]
v_mfma_f32_16x16x32_bf16 v[84:87],a[28:31],a[44:47],v[84:87]
ds_read_b128 a[72:75],v247 offset:59392
v_mfma_f32_16x16x32_bf16 v[124:127],a[32:35],a[44:47],v[124:127]
v_mfma_f32_16x16x32_bf16 v[164:167],a[36:39],a[44:47],v[164:167]
s_add_u32 m0,s58,20480
ds_read_b128 a[76:79],v247 offset:60416
v_mfma_f32_16x16x32_bf16 v[8:11],a[20:23],a[48:51],v[8:11]
buffer_load_dwordx4 v240,s[24:27],s52 offen lds
v_mfma_f32_16x16x32_bf16 v[48:51],a[24:27],a[48:51],v[48:51]
v_mfma_f32_16x16x32_bf16 v[88:91],a[28:31],a[48:51],v[88:91]
s_add_u32 m0,s58,20480
v_mfma_f32_16x16x32_bf16 v[128:131],a[32:35],a[48:51],v[128:131]
buffer_load_dwordx4 v241,s[24:27],s52 offen offset:1024 lds
v_mfma_f32_16x16x32_bf16 v[168:171],a[36:39],a[48:51],v[168:171]
v_mfma_f32_16x16x32_bf16 v[12:15],a[20:23],a[52:55],v[12:15]
s_add_u32 m0,s58,20480
v_mfma_f32_16x16x32_bf16 v[52:55],a[24:27],a[52:55],v[52:55]
buffer_load_dwordx4 v242,s[24:27],s52 offen offset:2048 lds
v_mfma_f32_16x16x32_bf16 v[92:95],a[28:31],a[52:55],v[92:95]
v_mfma_f32_16x16x32_bf16 v[132:135],a[32:35],a[52:55],v[132:135]
s_add_u32 m0,s58,20480
v_mfma_f32_16x16x32_bf16 v[172:175],a[36:39],a[52:55],v[172:175]
buffer_load_dwordx4 v243,s[24:27],s52 offen offset:3072 lds
v_mfma_f32_16x16x32_bf16 v[16:19],a[20:23],a[56:59],v[16:19]
v_mfma_f32_16x16x32_bf16 v[56:59],a[24:27],a[56:59],v[56:59]
s_add_u32 m0,s58,24576
v_mfma_f32_16x16x32_bf16 v[96:99],a[28:31],a[56:59],v[96:99]
buffer_load_dwordx4 v244,s[24:27],s52 offen lds
v_mfma_f32_16x16x32_bf16 v[136:139],a[32:35],a[56:59],v[136:139]
s_add_u32 s52,s52,40960
v_mfma_f32_16x16x32_bf16 v[176:179],a[36:39],a[56:59],v[176:179]
s_waitcnt vmcnt(11) lgkmcnt(0)
s_barrier
v_mfma_f32_16x16x32_bf16 v[20:23],a[20:23],a[60:63],v[20:23]
ds_read_b128 a[40:43],v247
v_mfma_f32_16x16x32_bf16 v[60:63],a[24:27],a[60:63],v[60:63]
ds_read_b128 a[44:47],v247 offset:1024
v_mfma_f32_16x16x32_bf16 v[100:103],a[28:31],a[60:63],v[100:103]
s_add_u32 m0,s58,40960
ds_read_b128 a[48:51],v247 offset:2048
v_mfma_f32_16x16x32_bf16 v[140:143],a[32:35],a[60:63],v[140:143]
buffer_load_dwordx4 v240,s[24:27],s52 offen lds
v_mfma_f32_16x16x32_bf16 v[180:183],a[36:39],a[60:63],v[180:183]
ds_read_b128 a[52:55],v247 offset:3072
ds_read_b128 a[56:59],v247 offset:4096
v_mfma_f32_16x16x32_bf16 v[24:27],a[20:23],a[64:67],v[24:27]
s_add_u32 m0,s58,40960
ds_read_b64_tr_b16 a[0:1],v230
v_mfma_f32_16x16x32_bf16 v[64:67],a[24:27],a[64:67],v[64:67]
buffer_load_dwordx4 v241,s[24:27],s52 offen offset:1024 lds
v_mfma_f32_16x16x32_bf16 v[104:107],a[28:31],a[64:67],v[104:107]
ds_read_b64_tr_b16 a[2:3],v230 offset:10240
ds_read_b64_tr_b16 a[4:5],v231
v_mfma_f32_16x16x32_bf16 v[144:147],a[32:35],a[64:67],v[144:147]
s_add_u32 m0,s58,40960
ds_read_b64_tr_b16 a[6:7],v231 offset:10240
v_mfma_f32_16x16x32_bf16 v[184:187],a[36:39],a[64:67],v[184:187]
buffer_load_dwordx4 v242,s[24:27],s52 offen offset:2048 lds
v_mfma_f32_16x16x32_bf16 v[28:31],a[20:23],a[68:71],v[28:31]
ds_read_b64_tr_b16 a[8:9],v232
ds_read_b64_tr_b16 a[10:11],v232 offset:10240
v_mfma_f32_16x16x32_bf16 v[68:71],a[24:27],a[68:71],v[68:71]
s_add_u32 m0,s58,40960
ds_read_b64_tr_b16 a[12:13],v233
v_mfma_f32_16x16x32_bf16 v[108:111],a[28:31],a[68:71],v[108:111]
buffer_load_dwordx4 v243,s[24:27],s52 offen offset:3072 lds
v_mfma_f32_16x16x32_bf16 v[148:151],a[32:35],a[68:71],v[148:151]
ds_read_b64_tr_b16 a[14:15],v233 offset:10240
ds_read_b64_tr_b16 a[16:17],v234
v_mfma_f32_16x16x32_bf16 v[188:191],a[36:39],a[68:71],v[188:191]
s_add_u32 m0,s58,45056
ds_read_b64_tr_b16 a[18:19],v234 offset:10240
v_mfma_f32_16x16x32_bf16 v[32:35],a[20:23],a[72:75],v[32:35]
buffer_load_dwordx4 v244,s[24:27],s52 offen lds
v_mfma_f32_16x16x32_bf16 v[72:75],a[24:27],a[72:75],v[72:75]
s_add_u32 s52,s52,40960
v_mfma_f32_16x16x32_bf16 v[112:115],a[28:31],a[72:75],v[112:115]
v_mfma_f32_16x16x32_bf16 v[152:155],a[32:35],a[72:75],v[152:155]
v_mfma_f32_16x16x32_bf16 v[192:195],a[36:39],a[72:75],v[192:195]
v_mfma_f32_16x16x32_bf16 v[36:39],a[20:23],a[76:79],v[36:39]
v_mfma_f32_16x16x32_bf16 v[76:79],a[24:27],a[76:79],v[76:79]
v_mfma_f32_16x16x32_bf16 v[116:119],a[28:31],a[76:79],v[116:119]
v_mfma_f32_16x16x32_bf16 v[156:159],a[32:35],a[76:79],v[156:159]
v_mfma_f32_16x16x32_bf16 v[196:199],a[36:39],a[76:79],v[196:199]
s_waitcnt lgkmcnt(0)
s_cmp_eq_u32 s51,0
s_cbranch_scc0 LOLOOP_%=
s_cmp_eq_u32 s33,0
s_cbranch_scc1 LFIN0_%=
s_cmp_eq_u32 s33,1
s_cbranch_scc1 LFIN1_%=
s_cmp_eq_u32 s33,2
s_cbranch_scc1 LFIN2_%=
s_lshl_b32 s64,s50,4
s_add_u32 s64,s64,s48
s_lshl_b32 s64,s64,1
s_add_u32 s64,s64,s34
s_mul_i32 s65,s64,458752
s_mul_i32 s66,s16,28672
s_add_u32 s65,s65,s66
s_add_u32 s65,s65,62980096
s_add_u32 s44,s98,s65
s_addc_u32 s45,s99,0
s_mov_b32 s46,372736
s_lshl_b32 s65,s64,8
s_add_u32 s65,s65,32768
s_add_u32 s12,s98,s65
s_addc_u32 s13,s99,0
s_mov_b32 s14,196
s_mov_b64 s[8:9],s[12:13]
s_cmp_eq_u32 s16,0
s_cselect_b32 s10,196,0
v_cvt_pk_bf16_f32 v0,v0,v1
v_cvt_pk_bf16_f32 v1,v2,v3
v_cvt_pk_bf16_f32 v2,v16,v17
v_cvt_pk_bf16_f32 v3,v18,v19
s_mov_b32 s73,344064
buffer_store_dwordx4 v[0:3],v253,s[44:47],s73 offen sc1
v_cvt_pk_bf16_f32 v32,v32,v33
v_cvt_pk_bf16_f32 v33,v34,v35
v_cvt_pk_bf16_f32 v34,v48,v49
v_cvt_pk_bf16_f32 v35,v50,v51
s_mov_b32 s73,345088
buffer_store_dwordx4 v[32:35],v253,s[44:47],s73 offen sc1
v_cvt_pk_bf16_f32 v64,v64,v65
v_cvt_pk_bf16_f32 v65,v66,v67
v_cvt_pk_bf16_f32 v66,v80,v81
v_cvt_pk_bf16_f32 v67,v82,v83
s_mov_b32 s73,346112
buffer_store_dwordx4 v[64:67],v253,s[44:47],s73 offen sc1
v_cvt_pk_bf16_f32 v96,v96,v97
v_cvt_pk_bf16_f32 v97,v98,v99
v_cvt_pk_bf16_f32 v98,v112,v113
v_cvt_pk_bf16_f32 v99,v114,v115
s_mov_b32 s73,347136
buffer_store_dwordx4 v[96:99],v253,s[44:47],s73 offen sc1
v_cvt_pk_bf16_f32 v128,v128,v129
v_cvt_pk_bf16_f32 v129,v130,v131
v_cvt_pk_bf16_f32 v130,v144,v145
v_cvt_pk_bf16_f32 v131,v146,v147
s_mov_b32 s73,348160
buffer_store_dwordx4 v[128:131],v253,s[44:47],s73 offen sc1
v_cvt_pk_bf16_f32 v160,v160,v161
v_cvt_pk_bf16_f32 v161,v162,v163
v_cvt_pk_bf16_f32 v162,v176,v177
v_cvt_pk_bf16_f32 v163,v178,v179
s_mov_b32 s73,349184
buffer_store_dwordx4 v[160:163],v253,s[44:47],s73 offen sc1
v_cvt_pk_bf16_f32 v192,v192,v193
v_cvt_pk_bf16_f32 v193,v194,v195
s_mov_b32 s73,350208
buffer_store_dwordx4 v[192:195],v253,s[44:47],s73 offen sc1
v_cvt_pk_bf16_f32 v4,v4,v5
v_cvt_pk_bf16_f32 v5,v6,v7
v_cvt_pk_bf16_f32 v6,v20,v21
v_cvt_pk_bf16_f32 v7,v22,v23
s_mov_b32 s73,351232
buffer_store_dwordx4 v[4:7],v253,s[44:47],s73 offen sc1
v_cvt_pk_bf16_f32 v36,v36,v37
v_cvt_pk_bf16_f32 v37,v38,v39
v_cvt_pk_bf16_f32 v38,v52,v53
v_cvt_pk_bf16_f32 v39,v54,v55
s_mov_b32 s73,352256
buffer_store_dwordx4 v[36:39],v253,s[44:47],s73 offen sc1
v_cvt_pk_bf16_f32 v68,v68,v69
v_cvt_pk_bf16_f32 v69,v70,v71
v_cvt_pk_bf16_f32 v70,v84,v85
v_cvt_pk_bf16_f32 v71,v86,v87
s_mov_b32 s73,353280
buffer_store_dwordx4 v[68:71],v253,s[44:47],s73 offen sc1
v_cvt_pk_bf16_f32 v100,v100,v101
v_cvt_pk_bf16_f32 v101,v102,v103
v_cvt_pk_bf16_f32 v102,v116,v117
v_cvt_pk_bf16_f32 v103,v118,v119
s_mov_b32 s73,354304
buffer_store_dwordx4 v[100:103],v253,s[44:47],s73 offen sc1
v_cvt_pk_bf16_f32 v132,v132,v133
v_cvt_pk_bf16_f32 v133,v134,v135
v_cvt_pk_bf16_f32 v134,v148,v149
v_cvt_pk_bf16_f32 v135,v150,v151
s_mov_b32 s73,355328
buffer_store_dwordx4 v[132:135],v253,s[44:47],s73 offen sc1
v_cvt_pk_bf16_f32 v164,v164,v165
v_cvt_pk_bf16_f32 v165,v166,v167
v_cvt_pk_bf16_f32 v166,v180,v181
v_cvt_pk_bf16_f32 v167,v182,v183
s_mov_b32 s73,356352
buffer_store_dwordx4 v[164:167],v253,s[44:47],s73 offen sc1
v_cvt_pk_bf16_f32 v196,v196,v197
v_cvt_pk_bf16_f32 v197,v198,v199
s_mov_b32 s73,357376
buffer_store_dwordx4 v[196:199],v253,s[44:47],s73 offen sc1
v_cvt_pk_bf16_f32 v8,v8,v9
v_cvt_pk_bf16_f32 v9,v10,v11
v_cvt_pk_bf16_f32 v10,v24,v25
v_cvt_pk_bf16_f32 v11,v26,v27
s_mov_b32 s73,358400
buffer_store_dwordx4 v[8:11],v253,s[44:47],s73 offen sc1
v_cvt_pk_bf16_f32 v40,v40,v41
v_cvt_pk_bf16_f32 v41,v42,v43
v_cvt_pk_bf16_f32 v42,v56,v57
v_cvt_pk_bf16_f32 v43,v58,v59
s_mov_b32 s73,359424
buffer_store_dwordx4 v[40:43],v253,s[44:47],s73 offen sc1
v_cvt_pk_bf16_f32 v72,v72,v73
v_cvt_pk_bf16_f32 v73,v74,v75
v_cvt_pk_bf16_f32 v74,v88,v89
v_cvt_pk_bf16_f32 v75,v90,v91
s_mov_b32 s73,360448
buffer_store_dwordx4 v[72:75],v253,s[44:47],s73 offen sc1
v_cvt_pk_bf16_f32 v104,v104,v105
v_cvt_pk_bf16_f32 v105,v106,v107
v_cvt_pk_bf16_f32 v106,v120,v121
v_cvt_pk_bf16_f32 v107,v122,v123
s_mov_b32 s73,361472
buffer_store_dwordx4 v[104:107],v253,s[44:47],s73 offen sc1
v_cvt_pk_bf16_f32 v136,v136,v137
v_cvt_pk_bf16_f32 v137,v138,v139
v_cvt_pk_bf16_f32 v138,v152,v153
v_cvt_pk_bf16_f32 v139,v154,v155
s_mov_b32 s73,362496
buffer_store_dwordx4 v[136:139],v253,s[44:47],s73 offen sc1
v_cvt_pk_bf16_f32 v168,v168,v169
v_cvt_pk_bf16_f32 v169,v170,v171
v_cvt_pk_bf16_f32 v170,v184,v185
v_cvt_pk_bf16_f32 v171,v186,v187
s_mov_b32 s73,363520
buffer_store_dwordx4 v[168:171],v253,s[44:47],s73 offen sc1
v_mbcnt_lo_u32_b32 v254,-1,0
v_mbcnt_hi_u32_b32 v254,-1,v254
v_and_b32 v255,15,v254
v_mul_u32_u24 v255,2560,v255
v_lshrrev_b32 v254,4,v254
v_lshlrev_b32 v254,3,v254
v_add_u32 v248,v254,v255
s_mul_i32 s64,s48,409600
s_mul_i32 s65,s50,1280
s_add_u32 s64,s64,s65
s_mul_i32 s65,s34,640
s_add_u32 s64,s64,s65
s_mul_i32 s65,s16,160
s_add_u32 s64,s64,s65
s_add_u32 s36,s96,s64
s_addc_u32 s37,s97,0
s_mov_b32 s38,407200
s_mov_b32 s67,4294901760
s_waitcnt vmcnt(0)
s_barrier
buffer_store_dword v247,v253,s[8:11],0 offen offset:192 sc1
s_mov_b32 s71,65536
LFS3_%=:
buffer_load_dword v246,v253,s[12:15],0 offen sc1
s_waitcnt vmcnt(0)
v_cmp_ne_u32 vcc,0,v246
s_sub_u32 s71,s71,1
s_cmp_eq_u32 s71,0
s_cbranch_scc1 LFS3_%=X
s_nop 0
s_and_b32 s70,vcc_lo,273
s_cmp_eq_u32 s70,273
s_cbranch_scc0 LFS3_%=
LFS3_%=X:
s_mov_b32 s73,21504
buffer_load_dwordx4 v[16:19],v253,s[44:47],s73 offen sc1
s_mov_b32 s73,136192
buffer_load_dwordx4 v[48:51],v253,s[44:47],s73 offen sc1
s_mov_b32 s73,250880
buffer_load_dwordx4 v[80:83],v253,s[44:47],s73 offen sc1
s_mov_b32 s73,22528
buffer_load_dwordx4 v[112:115],v253,s[44:47],s73 offen sc1
s_mov_b32 s73,137216
buffer_load_dwordx4 v[144:147],v253,s[44:47],s73 offen sc1
s_mov_b32 s73,251904
buffer_load_dwordx4 v[176:179],v253,s[44:47],s73 offen sc1
s_mov_b32 s73,23552
buffer_load_dwordx4 v[20:23],v253,s[44:47],s73 offen sc1
s_mov_b32 s73,138240
buffer_load_dwordx4 v[52:55],v253,s[44:47],s73 offen sc1
s_mov_b32 s73,252928
buffer_load_dwordx4 v[84:87],v253,s[44:47],s73 offen sc1
s_mov_b32 s73,24576
buffer_load_dwordx4 v[116:119],v253,s[44:47],s73 offen sc1
s_mov_b32 s73,139264
buffer_load_dwordx4 v[148:151],v253,s[44:47],s73 offen sc1
s_mov_b32 s73,253952
buffer_load_dwordx4 v[180:183],v253,s[44:47],s73 offen sc1
s_mov_b32 s73,25600
buffer_load_dwordx4 v[24:27],v253,s[44:47],s73 offen sc1
s_mov_b32 s73,140288
buffer_load_dwordx4 v[56:59],v253,s[44:47],s73 offen sc1
s_mov_b32 s73,254976
buffer_load_dwordx4 v[88:91],v253,s[44:47],s73 offen sc1
s_mov_b32 s73,26624
buffer_load_dwordx4 v[120:123],v253,s[44:47],s73 offen sc1
s_mov_b32 s73,141312
buffer_load_dwordx4 v[152:155],v253,s[44:47],s73 offen sc1
s_mov_b32 s73,256000
buffer_load_dwordx4 v[184:187],v253,s[44:47],s73 offen sc1
s_waitcnt vmcnt(15)
v_lshlrev_b32 v224,16,v16
v_and_b32 v225,s67,v16
v_pk_add_f32 v[12:13],v[12:13],v[224:225]
v_lshlrev_b32 v226,16,v17
v_and_b32 v227,s67,v17
v_pk_add_f32 v[14:15],v[14:15],v[226:227]
v_lshlrev_b32 v224,16,v48
v_and_b32 v225,s67,v48
v_pk_add_f32 v[12:13],v[12:13],v[224:225]
v_lshlrev_b32 v226,16,v49
v_and_b32 v227,s67,v49
v_pk_add_f32 v[14:15],v[14:15],v[226:227]
v_lshlrev_b32 v224,16,v80
v_and_b32 v225,s67,v80
v_pk_add_f32 v[12:13],v[12:13],v[224:225]
v_lshlrev_b32 v226,16,v81
v_and_b32 v227,s67,v81
v_pk_add_f32 v[14:15],v[14:15],v[226:227]
v_cvt_pk_bf16_f32 v12,v12,v13
v_cvt_pk_bf16_f32 v13,v14,v15
s_mov_b32 s73,122880
buffer_store_dwordx2 v[12:13],v248,s[36:39],s73 offen
v_lshlrev_b32 v224,16,v18
v_and_b32 v225,s67,v18
v_pk_add_f32 v[28:29],v[28:29],v[224:225]
v_lshlrev_b32 v226,16,v19
v_and_b32 v227,s67,v19
v_pk_add_f32 v[30:31],v[30:31],v[226:227]
v_lshlrev_b32 v224,16,v50
v_and_b32 v225,s67,v50
v_pk_add_f32 v[28:29],v[28:29],v[224:225]
v_lshlrev_b32 v226,16,v51
v_and_b32 v227,s67,v51
v_pk_add_f32 v[30:31],v[30:31],v[226:227]
v_lshlrev_b32 v224,16,v82
v_and_b32 v225,s67,v82
v_pk_add_f32 v[28:29],v[28:29],v[224:225]
v_lshlrev_b32 v226,16,v83
v_and_b32 v227,s67,v83
v_pk_add_f32 v[30:31],v[30:31],v[226:227]
v_cvt_pk_bf16_f32 v28,v28,v29
v_cvt_pk_bf16_f32 v29,v30,v31
s_mov_b32 s73,286720
buffer_store_dwordx2 v[28:29],v248,s[36:39],s73 offen
s_waitcnt vmcnt(14)
v_lshlrev_b32 v224,16,v112
v_and_b32 v225,s67,v112
v_pk_add_f32 v[44:45],v[44:45],v[224:225]
v_lshlrev_b32 v226,16,v113
v_and_b32 v227,s67,v113
v_pk_add_f32 v[46:47],v[46:47],v[226:227]
v_lshlrev_b32 v224,16,v144
v_and_b32 v225,s67,v144
v_pk_add_f32 v[44:45],v[44:45],v[224:225]
v_lshlrev_b32 v226,16,v145
v_and_b32 v227,s67,v145
v_pk_add_f32 v[46:47],v[46:47],v[226:227]
v_lshlrev_b32 v224,16,v176
v_and_b32 v225,s67,v176
v_pk_add_f32 v[44:45],v[44:45],v[224:225]
v_lshlrev_b32 v226,16,v177
v_and_b32 v227,s67,v177
v_pk_add_f32 v[46:47],v[46:47],v[226:227]
v_cvt_pk_bf16_f32 v44,v44,v45
v_cvt_pk_bf16_f32 v45,v46,v47
s_mov_b32 s73,40992
buffer_store_dwordx2 v[44:45],v248,s[36:39],s73 offen
v_lshlrev_b32 v224,16,v114
v_and_b32 v225,s67,v114
v_pk_add_f32 v[60:61],v[60:61],v[224:225]
v_lshlrev_b32 v226,16,v115
v_and_b32 v227,s67,v115
v_pk_add_f32 v[62:63],v[62:63],v[226:227]
v_lshlrev_b32 v224,16,v146
v_and_b32 v225,s67,v146
v_pk_add_f32 v[60:61],v[60:61],v[224:225]
v_lshlrev_b32 v226,16,v147
v_and_b32 v227,s67,v147
v_pk_add_f32 v[62:63],v[62:63],v[226:227]
v_lshlrev_b32 v224,16,v178
v_and_b32 v225,s67,v178
v_pk_add_f32 v[60:61],v[60:61],v[224:225]
v_lshlrev_b32 v226,16,v179
v_and_b32 v227,s67,v179
v_pk_add_f32 v[62:63],v[62:63],v[226:227]
v_cvt_pk_bf16_f32 v60,v60,v61
v_cvt_pk_bf16_f32 v61,v62,v63
s_mov_b32 s73,204832
buffer_store_dwordx2 v[60:61],v248,s[36:39],s73 offen
s_waitcnt vmcnt(13)
v_lshlrev_b32 v224,16,v20
v_and_b32 v225,s67,v20
v_pk_add_f32 v[76:77],v[76:77],v[224:225]
v_lshlrev_b32 v226,16,v21
v_and_b32 v227,s67,v21
v_pk_add_f32 v[78:79],v[78:79],v[226:227]
v_lshlrev_b32 v224,16,v52
v_and_b32 v225,s67,v52
v_pk_add_f32 v[76:77],v[76:77],v[224:225]
v_lshlrev_b32 v226,16,v53
v_and_b32 v227,s67,v53
v_pk_add_f32 v[78:79],v[78:79],v[226:227]
v_lshlrev_b32 v224,16,v84
v_and_b32 v225,s67,v84
v_pk_add_f32 v[76:77],v[76:77],v[224:225]
v_lshlrev_b32 v226,16,v85
v_and_b32 v227,s67,v85
v_pk_add_f32 v[78:79],v[78:79],v[226:227]
v_cvt_pk_bf16_f32 v76,v76,v77
v_cvt_pk_bf16_f32 v77,v78,v79
s_mov_b32 s73,368672
buffer_store_dwordx2 v[76:77],v248,s[36:39],s73 offen
v_lshlrev_b32 v224,16,v22
v_and_b32 v225,s67,v22
v_pk_add_f32 v[92:93],v[92:93],v[224:225]
v_lshlrev_b32 v226,16,v23
v_and_b32 v227,s67,v23
v_pk_add_f32 v[94:95],v[94:95],v[226:227]
v_lshlrev_b32 v224,16,v54
v_and_b32 v225,s67,v54
v_pk_add_f32 v[92:93],v[92:93],v[224:225]
v_lshlrev_b32 v226,16,v55
v_and_b32 v227,s67,v55
v_pk_add_f32 v[94:95],v[94:95],v[226:227]
v_lshlrev_b32 v224,16,v86
v_and_b32 v225,s67,v86
v_pk_add_f32 v[92:93],v[92:93],v[224:225]
v_lshlrev_b32 v226,16,v87
v_and_b32 v227,s67,v87
v_pk_add_f32 v[94:95],v[94:95],v[226:227]
v_cvt_pk_bf16_f32 v92,v92,v93
v_cvt_pk_bf16_f32 v93,v94,v95
s_mov_b32 s73,122944
buffer_store_dwordx2 v[92:93],v248,s[36:39],s73 offen
s_waitcnt vmcnt(12)
v_lshlrev_b32 v224,16,v116
v_and_b32 v225,s67,v116
v_pk_add_f32 v[108:109],v[108:109],v[224:225]
v_lshlrev_b32 v226,16,v117
v_and_b32 v227,s67,v117
v_pk_add_f32 v[110:111],v[110:111],v[226:227]
v_lshlrev_b32 v224,16,v148
v_and_b32 v225,s67,v148
v_pk_add_f32 v[108:109],v[108:109],v[224:225]
v_lshlrev_b32 v226,16,v149
v_and_b32 v227,s67,v149
v_pk_add_f32 v[110:111],v[110:111],v[226:227]
v_lshlrev_b32 v224,16,v180
v_and_b32 v225,s67,v180
v_pk_add_f32 v[108:109],v[108:109],v[224:225]
v_lshlrev_b32 v226,16,v181
v_and_b32 v227,s67,v181
v_pk_add_f32 v[110:111],v[110:111],v[226:227]
v_cvt_pk_bf16_f32 v108,v108,v109
v_cvt_pk_bf16_f32 v109,v110,v111
s_mov_b32 s73,286784
buffer_store_dwordx2 v[108:109],v248,s[36:39],s73 offen
v_lshlrev_b32 v224,16,v118
v_and_b32 v225,s67,v118
v_pk_add_f32 v[124:125],v[124:125],v[224:225]
v_lshlrev_b32 v226,16,v119
v_and_b32 v227,s67,v119
v_pk_add_f32 v[126:127],v[126:127],v[226:227]
v_lshlrev_b32 v224,16,v150
v_and_b32 v225,s67,v150
v_pk_add_f32 v[124:125],v[124:125],v[224:225]
v_lshlrev_b32 v226,16,v151
v_and_b32 v227,s67,v151
v_pk_add_f32 v[126:127],v[126:127],v[226:227]
v_lshlrev_b32 v224,16,v182
v_and_b32 v225,s67,v182
v_pk_add_f32 v[124:125],v[124:125],v[224:225]
v_lshlrev_b32 v226,16,v183
v_and_b32 v227,s67,v183
v_pk_add_f32 v[126:127],v[126:127],v[226:227]
v_cvt_pk_bf16_f32 v124,v124,v125
v_cvt_pk_bf16_f32 v125,v126,v127
s_mov_b32 s73,41056
buffer_store_dwordx2 v[124:125],v248,s[36:39],s73 offen
s_waitcnt vmcnt(11)
v_lshlrev_b32 v224,16,v24
v_and_b32 v225,s67,v24
v_pk_add_f32 v[140:141],v[140:141],v[224:225]
v_lshlrev_b32 v226,16,v25
v_and_b32 v227,s67,v25
v_pk_add_f32 v[142:143],v[142:143],v[226:227]
v_lshlrev_b32 v224,16,v56
v_and_b32 v225,s67,v56
v_pk_add_f32 v[140:141],v[140:141],v[224:225]
v_lshlrev_b32 v226,16,v57
v_and_b32 v227,s67,v57
v_pk_add_f32 v[142:143],v[142:143],v[226:227]
v_lshlrev_b32 v224,16,v88
v_and_b32 v225,s67,v88
v_pk_add_f32 v[140:141],v[140:141],v[224:225]
v_lshlrev_b32 v226,16,v89
v_and_b32 v227,s67,v89
v_pk_add_f32 v[142:143],v[142:143],v[226:227]
v_cvt_pk_bf16_f32 v140,v140,v141
v_cvt_pk_bf16_f32 v141,v142,v143
s_mov_b32 s73,204896
buffer_store_dwordx2 v[140:141],v248,s[36:39],s73 offen
v_lshlrev_b32 v224,16,v26
v_and_b32 v225,s67,v26
v_pk_add_f32 v[156:157],v[156:157],v[224:225]
v_lshlrev_b32 v226,16,v27
v_and_b32 v227,s67,v27
v_pk_add_f32 v[158:159],v[158:159],v[226:227]
v_lshlrev_b32 v224,16,v58
v_and_b32 v225,s67,v58
v_pk_add_f32 v[156:157],v[156:157],v[224:225]
v_lshlrev_b32 v226,16,v59
v_and_b32 v227,s67,v59
v_pk_add_f32 v[158:159],v[158:159],v[226:227]
v_lshlrev_b32 v224,16,v90
v_and_b32 v225,s67,v90
v_pk_add_f32 v[156:157],v[156:157],v[224:225]
v_lshlrev_b32 v226,16,v91
v_and_b32 v227,s67,v91
v_pk_add_f32 v[158:159],v[158:159],v[226:227]
v_cvt_pk_bf16_f32 v156,v156,v157
v_cvt_pk_bf16_f32 v157,v158,v159
s_mov_b32 s73,368736
buffer_store_dwordx2 v[156:157],v248,s[36:39],s73 offen
s_waitcnt vmcnt(10)
v_lshlrev_b32 v224,16,v120
v_and_b32 v225,s67,v120
v_pk_add_f32 v[172:173],v[172:173],v[224:225]
v_lshlrev_b32 v226,16,v121
v_and_b32 v227,s67,v121
v_pk_add_f32 v[174:175],v[174:175],v[226:227]
v_lshlrev_b32 v224,16,v152
v_and_b32 v225,s67,v152
v_pk_add_f32 v[172:173],v[172:173],v[224:225]
v_lshlrev_b32 v226,16,v153
v_and_b32 v227,s67,v153
v_pk_add_f32 v[174:175],v[174:175],v[226:227]
v_lshlrev_b32 v224,16,v184
v_and_b32 v225,s67,v184
v_pk_add_f32 v[172:173],v[172:173],v[224:225]
v_lshlrev_b32 v226,16,v185
v_and_b32 v227,s67,v185
v_pk_add_f32 v[174:175],v[174:175],v[226:227]
v_cvt_pk_bf16_f32 v172,v172,v173
v_cvt_pk_bf16_f32 v173,v174,v175
s_mov_b32 s73,123008
buffer_store_dwordx2 v[172:173],v248,s[36:39],s73 offen
v_lshlrev_b32 v224,16,v122
v_and_b32 v225,s67,v122
v_pk_add_f32 v[188:189],v[188:189],v[224:225]
v_lshlrev_b32 v226,16,v123
v_and_b32 v227,s67,v123
v_pk_add_f32 v[190:191],v[190:191],v[226:227]
v_lshlrev_b32 v224,16,v154
v_and_b32 v225,s67,v154
v_pk_add_f32 v[188:189],v[188:189],v[224:225]
v_lshlrev_b32 v226,16,v155
v_and_b32 v227,s67,v155
v_pk_add_f32 v[190:191],v[190:191],v[226:227]
v_lshlrev_b32 v224,16,v186
v_and_b32 v225,s67,v186
v_pk_add_f32 v[188:189],v[188:189],v[224:225]
v_lshlrev_b32 v226,16,v187
v_and_b32 v227,s67,v187
v_pk_add_f32 v[190:191],v[190:191],v[226:227]
v_cvt_pk_bf16_f32 v188,v188,v189
v_cvt_pk_bf16_f32 v189,v190,v191
s_mov_b32 s73,286848
buffer_store_dwordx2 v[188:189],v248,s[36:39],s73 offen
s_branch LEND_%=
LFIN0_%=:
s_lshl_b32 s64,s50,4
s_add_u32 s64,s64,s48
s_lshl_b32 s64,s64,1
s_add_u32 s64,s64,s34
s_mul_i32 s65,s64,458752
s_mul_i32 s66,s16,28672
s_add_u32 s65,s65,s66
s_add_u32 s65,s65,62980096
s_add_u32 s44,s98,s65
s_addc_u32 s45,s99,0
s_mov_b32 s46,372736
s_lshl_b32 s65,s64,8
s_add_u32 s65,s65,32768
s_add_u32 s12,s98,s65
s_addc_u32 s13,s99,0
s_mov_b32 s14,196
s_mov_b64 s[8:9],s[12:13]
s_cmp_eq_u32 s16,0
s_cselect_b32 s10,4,0
v_cvt_pk_bf16_f32 v4,v4,v5
v_cvt_pk_bf16_f32 v5,v6,v7
v_cvt_pk_bf16_f32 v6,v20,v21
v_cvt_pk_bf16_f32 v7,v22,v23
s_mov_b32 s73,7168
buffer_store_dwordx4 v[4:7],v253,s[44:47],s73 offen sc1
v_cvt_pk_bf16_f32 v36,v36,v37
v_cvt_pk_bf16_f32 v37,v38,v39
v_cvt_pk_bf16_f32 v38,v52,v53
v_cvt_pk_bf16_f32 v39,v54,v55
s_mov_b32 s73,8192
buffer_store_dwordx4 v[36:39],v253,s[44:47],s73 offen sc1
v_cvt_pk_bf16_f32 v68,v68,v69
v_cvt_pk_bf16_f32 v69,v70,v71
v_cvt_pk_bf16_f32 v70,v84,v85
v_cvt_pk_bf16_f32 v71,v86,v87
s_mov_b32 s73,9216
buffer_store_dwordx4 v[68:71],v253,s[44:47],s73 offen sc1
v_cvt_pk_bf16_f32 v100,v100,v101
v_cvt_pk_bf16_f32 v101,v102,v103
v_cvt_pk_bf16_f32 v102,v116,v117
v_cvt_pk_bf16_f32 v103,v118,v119
s_mov_b32 s73,10240
buffer_store_dwordx4 v[100:103],v253,s[44:47],s73 offen sc1
v_cvt_pk_bf16_f32 v132,v132,v133
v_cvt_pk_bf16_f32 v133,v134,v135
v_cvt_pk_bf16_f32 v134,v148,v149
v_cvt_pk_bf16_f32 v135,v150,v151
s_mov_b32 s73,11264
buffer_store_dwordx4 v[132:135],v253,s[44:47],s73 offen sc1
v_cvt_pk_bf16_f32 v164,v164,v165
v_cvt_pk_bf16_f32 v165,v166,v167
v_cvt_pk_bf16_f32 v166,v180,v181
v_cvt_pk_bf16_f32 v167,v182,v183
s_mov_b32 s73,12288
buffer_store_dwordx4 v[164:167],v253,s[44:47],s73 offen sc1
v_cvt_pk_bf16_f32 v196,v196,v197
v_cvt_pk_bf16_f32 v197,v198,v199
s_mov_b32 s73,13312
buffer_store_dwordx4 v[196:199],v253,s[44:47],s73 offen sc1
v_cvt_pk_bf16_f32 v8,v8,v9
v_cvt_pk_bf16_f32 v9,v10,v11
v_cvt_pk_bf16_f32 v10,v24,v25
v_cvt_pk_bf16_f32 v11,v26,v27
s_mov_b32 s73,14336
buffer_store_dwordx4 v[8:11],v253,s[44:47],s73 offen sc1
v_cvt_pk_bf16_f32 v40,v40,v41
v_cvt_pk_bf16_f32 v41,v42,v43
v_cvt_pk_bf16_f32 v42,v56,v57
v_cvt_pk_bf16_f32 v43,v58,v59
s_mov_b32 s73,15360
buffer_store_dwordx4 v[40:43],v253,s[44:47],s73 offen sc1
v_cvt_pk_bf16_f32 v72,v72,v73
v_cvt_pk_bf16_f32 v73,v74,v75
v_cvt_pk_bf16_f32 v74,v88,v89
v_cvt_pk_bf16_f32 v75,v90,v91
s_mov_b32 s73,16384
buffer_store_dwordx4 v[72:75],v253,s[44:47],s73 offen sc1
v_cvt_pk_bf16_f32 v104,v104,v105
v_cvt_pk_bf16_f32 v105,v106,v107
v_cvt_pk_bf16_f32 v106,v120,v121
v_cvt_pk_bf16_f32 v107,v122,v123
s_mov_b32 s73,17408
buffer_store_dwordx4 v[104:107],v253,s[44:47],s73 offen sc1
v_cvt_pk_bf16_f32 v136,v136,v137
v_cvt_pk_bf16_f32 v137,v138,v139
v_cvt_pk_bf16_f32 v138,v152,v153
v_cvt_pk_bf16_f32 v139,v154,v155
s_mov_b32 s73,18432
buffer_store_dwordx4 v[136:139],v253,s[44:47],s73 offen sc1
v_cvt_pk_bf16_f32 v168,v168,v169
v_cvt_pk_bf16_f32 v169,v170,v171
v_cvt_pk_bf16_f32 v170,v184,v185
v_cvt_pk_bf16_f32 v171,v186,v187
s_mov_b32 s73,19456
buffer_store_dwordx4 v[168:171],v253,s[44:47],s73 offen sc1
v_cvt_pk_bf16_f32 v12,v12,v13
v_cvt_pk_bf16_f32 v13,v14,v15
v_cvt_pk_bf16_f32 v14,v28,v29
v_cvt_pk_bf16_f32 v15,v30,v31
s_mov_b32 s73,21504
buffer_store_dwordx4 v[12:15],v253,s[44:47],s73 offen sc1
v_cvt_pk_bf16_f32 v44,v44,v45
v_cvt_pk_bf16_f32 v45,v46,v47
v_cvt_pk_bf16_f32 v46,v60,v61
v_cvt_pk_bf16_f32 v47,v62,v63
s_mov_b32 s73,22528
buffer_store_dwordx4 v[44:47],v253,s[44:47],s73 offen sc1
v_cvt_pk_bf16_f32 v76,v76,v77
v_cvt_pk_bf16_f32 v77,v78,v79
v_cvt_pk_bf16_f32 v78,v92,v93
v_cvt_pk_bf16_f32 v79,v94,v95
s_mov_b32 s73,23552
buffer_store_dwordx4 v[76:79],v253,s[44:47],s73 offen sc1
v_cvt_pk_bf16_f32 v108,v108,v109
v_cvt_pk_bf16_f32 v109,v110,v111
v_cvt_pk_bf16_f32 v110,v124,v125
v_cvt_pk_bf16_f32 v111,v126,v127
s_mov_b32 s73,24576
buffer_store_dwordx4 v[108:111],v253,s[44:47],s73 offen sc1
v_cvt_pk_bf16_f32 v140,v140,v141
v_cvt_pk_bf16_f32 v141,v142,v143
v_cvt_pk_bf16_f32 v142,v156,v157
v_cvt_pk_bf16_f32 v143,v158,v159
s_mov_b32 s73,25600
buffer_store_dwordx4 v[140:143],v253,s[44:47],s73 offen sc1
v_cvt_pk_bf16_f32 v172,v172,v173
v_cvt_pk_bf16_f32 v173,v174,v175
v_cvt_pk_bf16_f32 v174,v188,v189
v_cvt_pk_bf16_f32 v175,v190,v191
s_mov_b32 s73,26624
buffer_store_dwordx4 v[172:175],v253,s[44:47],s73 offen sc1
v_mbcnt_lo_u32_b32 v254,-1,0
v_mbcnt_hi_u32_b32 v254,-1,v254
v_and_b32 v255,15,v254
v_mul_u32_u24 v255,2560,v255
v_lshrrev_b32 v254,4,v254
v_lshlrev_b32 v254,3,v254
v_add_u32 v248,v254,v255
s_mul_i32 s64,s48,409600
s_mul_i32 s65,s50,1280
s_add_u32 s64,s64,s65
s_mul_i32 s65,s34,640
s_add_u32 s64,s64,s65
s_mul_i32 s65,s16,160
s_add_u32 s64,s64,s65
s_add_u32 s36,s96,s64
s_addc_u32 s37,s97,0
s_mov_b32 s38,407200
s_mov_b32 s67,4294901760
s_waitcnt vmcnt(0)
s_barrier
buffer_store_dword v247,v253,s[8:11],0 offen sc1
s_mov_b32 s71,65536
LFS0_%=:
buffer_load_dword v246,v253,s[12:15],0 offen sc1
s_waitcnt vmcnt(0)
v_cmp_ne_u32 vcc,0,v246
s_sub_u32 s71,s71,1
s_cmp_eq_u32 s71,0
s_cbranch_scc1 LFS0_%=X
s_nop 0
s_and_b32 s70,vcc_lo,4368
s_cmp_eq_u32 s70,4368
s_cbranch_scc0 LFS0_%=
LFS0_%=X:
s_mov_b32 s73,114688
buffer_load_dwordx4 v[20:23],v253,s[44:47],s73 offen sc1
s_mov_b32 s73,229376
buffer_load_dwordx4 v[52:55],v253,s[44:47],s73 offen sc1
s_mov_b32 s73,344064
buffer_load_dwordx4 v[84:87],v253,s[44:47],s73 offen sc1
s_mov_b32 s73,115712
buffer_load_dwordx4 v[116:119],v253,s[44:47],s73 offen sc1
s_mov_b32 s73,230400
buffer_load_dwordx4 v[148:151],v253,s[44:47],s73 offen sc1
s_mov_b32 s73,345088
buffer_load_dwordx4 v[180:183],v253,s[44:47],s73 offen sc1
s_mov_b32 s73,116736
buffer_load_dwordx4 v[24:27],v253,s[44:47],s73 offen sc1
s_mov_b32 s73,231424
buffer_load_dwordx4 v[56:59],v253,s[44:47],s73 offen sc1
s_mov_b32 s73,346112
buffer_load_dwordx4 v[88:91],v253,s[44:47],s73 offen sc1
s_mov_b32 s73,117760
buffer_load_dwordx4 v[120:123],v253,s[44:47],s73 offen sc1
s_mov_b32 s73,232448
buffer_load_dwordx4 v[152:155],v253,s[44:47],s73 offen sc1
s_mov_b32 s73,347136
buffer_load_dwordx4 v[184:187],v253,s[44:47],s73 offen sc1
s_mov_b32 s73,118784
buffer_load_dwordx4 v[28:31],v253,s[44:47],s73 offen sc1
s_mov_b32 s73,233472
buffer_load_dwordx4 v[60:63],v253,s[44:47],s73 offen sc1
s_mov_b32 s73,348160
buffer_load_dwordx4 v[92:95],v253,s[44:47],s73 offen sc1
s_mov_b32 s73,119808
buffer_load_dwordx4 v[124:127],v253,s[44:47],s73 offen sc1
s_mov_b32 s73,234496
buffer_load_dwordx4 v[156:159],v253,s[44:47],s73 offen sc1
s_mov_b32 s73,349184
buffer_load_dwordx4 v[188:191],v253,s[44:47],s73 offen sc1
s_mov_b32 s73,120832
buffer_load_dwordx4 v[200:203],v253,s[44:47],s73 offen sc1
s_mov_b32 s73,235520
buffer_load_dwordx4 v[204:207],v253,s[44:47],s73 offen sc1
s_mov_b32 s73,350208
buffer_load_dwordx4 v[208:211],v253,s[44:47],s73 offen sc1
s_waitcnt vmcnt(18)
v_lshlrev_b32 v224,16,v20
v_and_b32 v225,s67,v20
v_pk_add_f32 v[0:1],v[0:1],v[224:225]
v_lshlrev_b32 v226,16,v21
v_and_b32 v227,s67,v21
v_pk_add_f32 v[2:3],v[2:3],v[226:227]
v_lshlrev_b32 v224,16,v52
v_and_b32 v225,s67,v52
v_pk_add_f32 v[0:1],v[0:1],v[224:225]
v_lshlrev_b32 v226,16,v53
v_and_b32 v227,s67,v53
v_pk_add_f32 v[2:3],v[2:3],v[226:227]
v_lshlrev_b32 v224,16,v84
v_and_b32 v225,s67,v84
v_pk_add_f32 v[0:1],v[0:1],v[224:225]
v_lshlrev_b32 v226,16,v85
v_and_b32 v227,s67,v85
v_pk_add_f32 v[2:3],v[2:3],v[226:227]
v_cvt_pk_bf16_f32 v0,v0,v1
v_cvt_pk_bf16_f32 v1,v2,v3
s_mov_b32 s73,0
buffer_store_dwordx2 v[0:1],v248,s[36:39],s73 offen
v_lshlrev_b32 v224,16,v22
v_and_b32 v225,s67,v22
v_pk_add_f32 v[16:17],v[16:17],v[224:225]
v_lshlrev_b32 v226,16,v23
v_and_b32 v227,s67,v23
v_pk_add_f32 v[18:19],v[18:19],v[226:227]
v_lshlrev_b32 v224,16,v54
v_and_b32 v225,s67,v54
v_pk_add_f32 v[16:17],v[16:17],v[224:225]
v_lshlrev_b32 v226,16,v55
v_and_b32 v227,s67,v55
v_pk_add_f32 v[18:19],v[18:19],v[226:227]
v_lshlrev_b32 v224,16,v86
v_and_b32 v225,s67,v86
v_pk_add_f32 v[16:17],v[16:17],v[224:225]
v_lshlrev_b32 v226,16,v87
v_and_b32 v227,s67,v87
v_pk_add_f32 v[18:19],v[18:19],v[226:227]
v_cvt_pk_bf16_f32 v16,v16,v17
v_cvt_pk_bf16_f32 v17,v18,v19
s_mov_b32 s73,163840
buffer_store_dwordx2 v[16:17],v248,s[36:39],s73 offen
s_waitcnt vmcnt(17)
v_lshlrev_b32 v224,16,v116
v_and_b32 v225,s67,v116
v_pk_add_f32 v[32:33],v[32:33],v[224:225]
v_lshlrev_b32 v226,16,v117
v_and_b32 v227,s67,v117
v_pk_add_f32 v[34:35],v[34:35],v[226:227]
v_lshlrev_b32 v224,16,v148
v_and_b32 v225,s67,v148
v_pk_add_f32 v[32:33],v[32:33],v[224:225]
v_lshlrev_b32 v226,16,v149
v_and_b32 v227,s67,v149
v_pk_add_f32 v[34:35],v[34:35],v[226:227]
v_lshlrev_b32 v224,16,v180
v_and_b32 v225,s67,v180
v_pk_add_f32 v[32:33],v[32:33],v[224:225]
v_lshlrev_b32 v226,16,v181
v_and_b32 v227,s67,v181
v_pk_add_f32 v[34:35],v[34:35],v[226:227]
v_cvt_pk_bf16_f32 v32,v32,v33
v_cvt_pk_bf16_f32 v33,v34,v35
s_mov_b32 s73,327680
buffer_store_dwordx2 v[32:33],v248,s[36:39],s73 offen
v_lshlrev_b32 v224,16,v118
v_and_b32 v225,s67,v118
v_pk_add_f32 v[48:49],v[48:49],v[224:225]
v_lshlrev_b32 v226,16,v119
v_and_b32 v227,s67,v119
v_pk_add_f32 v[50:51],v[50:51],v[226:227]
v_lshlrev_b32 v224,16,v150
v_and_b32 v225,s67,v150
v_pk_add_f32 v[48:49],v[48:49],v[224:225]
v_lshlrev_b32 v226,16,v151
v_and_b32 v227,s67,v151
v_pk_add_f32 v[50:51],v[50:51],v[226:227]
v_lshlrev_b32 v224,16,v182
v_and_b32 v225,s67,v182
v_pk_add_f32 v[48:49],v[48:49],v[224:225]
v_lshlrev_b32 v226,16,v183
v_and_b32 v227,s67,v183
v_pk_add_f32 v[50:51],v[50:51],v[226:227]
v_cvt_pk_bf16_f32 v48,v48,v49
v_cvt_pk_bf16_f32 v49,v50,v51
s_mov_b32 s73,81952
buffer_store_dwordx2 v[48:49],v248,s[36:39],s73 offen
s_waitcnt vmcnt(16)
v_lshlrev_b32 v224,16,v24
v_and_b32 v225,s67,v24
v_pk_add_f32 v[64:65],v[64:65],v[224:225]
v_lshlrev_b32 v226,16,v25
v_and_b32 v227,s67,v25
v_pk_add_f32 v[66:67],v[66:67],v[226:227]
v_lshlrev_b32 v224,16,v56
v_and_b32 v225,s67,v56
v_pk_add_f32 v[64:65],v[64:65],v[224:225]
v_lshlrev_b32 v226,16,v57
v_and_b32 v227,s67,v57
v_pk_add_f32 v[66:67],v[66:67],v[226:227]
v_lshlrev_b32 v224,16,v88
v_and_b32 v225,s67,v88
v_pk_add_f32 v[64:65],v[64:65],v[224:225]
v_lshlrev_b32 v226,16,v89
v_and_b32 v227,s67,v89
v_pk_add_f32 v[66:67],v[66:67],v[226:227]
v_cvt_pk_bf16_f32 v64,v64,v65
v_cvt_pk_bf16_f32 v65,v66,v67
s_mov_b32 s73,245792
buffer_store_dwordx2 v[64:65],v248,s[36:39],s73 offen
v_lshlrev_b32 v224,16,v26
v_and_b32 v225,s67,v26
v_pk_add_f32 v[80:81],v[80:81],v[224:225]
v_lshlrev_b32 v226,16,v27
v_and_b32 v227,s67,v27
v_pk_add_f32 v[82:83],v[82:83],v[226:227]
v_lshlrev_b32 v224,16,v58
v_and_b32 v225,s67,v58
v_pk_add_f32 v[80:81],v[80:81],v[224:225]
v_lshlrev_b32 v226,16,v59
v_and_b32 v227,s67,v59
v_pk_add_f32 v[82:83],v[82:83],v[226:227]
v_lshlrev_b32 v224,16,v90
v_and_b32 v225,s67,v90
v_pk_add_f32 v[80:81],v[80:81],v[224:225]
v_lshlrev_b32 v226,16,v91
v_and_b32 v227,s67,v91
v_pk_add_f32 v[82:83],v[82:83],v[226:227]
v_cvt_pk_bf16_f32 v80,v80,v81
v_cvt_pk_bf16_f32 v81,v82,v83
s_mov_b32 s73,64
buffer_store_dwordx2 v[80:81],v248,s[36:39],s73 offen
s_waitcnt vmcnt(15)
v_lshlrev_b32 v224,16,v120
v_and_b32 v225,s67,v120
v_pk_add_f32 v[96:97],v[96:97],v[224:225]
v_lshlrev_b32 v226,16,v121
v_and_b32 v227,s67,v121
v_pk_add_f32 v[98:99],v[98:99],v[226:227]
v_lshlrev_b32 v224,16,v152
v_and_b32 v225,s67,v152
v_pk_add_f32 v[96:97],v[96:97],v[224:225]
v_lshlrev_b32 v226,16,v153
v_and_b32 v227,s67,v153
v_pk_add_f32 v[98:99],v[98:99],v[226:227]
v_lshlrev_b32 v224,16,v184
v_and_b32 v225,s67,v184
v_pk_add_f32 v[96:97],v[96:97],v[224:225]
v_lshlrev_b32 v226,16,v185
v_and_b32 v227,s67,v185
v_pk_add_f32 v[98:99],v[98:99],v[226:227]
v_cvt_pk_bf16_f32 v96,v96,v97
v_cvt_pk_bf16_f32 v97,v98,v99
s_mov_b32 s73,163904
buffer_store_dwordx2 v[96:97],v248,s[36:39],s73 offen
v_lshlrev_b32 v224,16,v122
v_and_b32 v225,s67,v122
v_pk_add_f32 v[112:113],v[112:113],v[224:225]
v_lshlrev_b32 v226,16,v123
v_and_b32 v227,s67,v123
v_pk_add_f32 v[114:115],v[114:115],v[226:227]
v_lshlrev_b32 v224,16,v154
v_and_b32 v225,s67,v154
v_pk_add_f32 v[112:113],v[112:113],v[224:225]
v_lshlrev_b32 v226,16,v155
v_and_b32 v227,s67,v155
v_pk_add_f32 v[114:115],v[114:115],v[226:227]
v_lshlrev_b32 v224,16,v186
v_and_b32 v225,s67,v186
v_pk_add_f32 v[112:113],v[112:113],v[224:225]
v_lshlrev_b32 v226,16,v187
v_and_b32 v227,s67,v187
v_pk_add_f32 v[114:115],v[114:115],v[226:227]
v_cvt_pk_bf16_f32 v112,v112,v113
v_cvt_pk_bf16_f32 v113,v114,v115
s_mov_b32 s73,327744
buffer_store_dwordx2 v[112:113],v248,s[36:39],s73 offen
s_waitcnt vmcnt(14)
v_lshlrev_b32 v224,16,v28
v_and_b32 v225,s67,v28
v_pk_add_f32 v[128:129],v[128:129],v[224:225]
v_lshlrev_b32 v226,16,v29
v_and_b32 v227,s67,v29
v_pk_add_f32 v[130:131],v[130:131],v[226:227]
v_lshlrev_b32 v224,16,v60
v_and_b32 v225,s67,v60
v_pk_add_f32 v[128:129],v[128:129],v[224:225]
v_lshlrev_b32 v226,16,v61
v_and_b32 v227,s67,v61
v_pk_add_f32 v[130:131],v[130:131],v[226:227]
v_lshlrev_b32 v224,16,v92
v_and_b32 v225,s67,v92
v_pk_add_f32 v[128:129],v[128:129],v[224:225]
v_lshlrev_b32 v226,16,v93
v_and_b32 v227,s67,v93
v_pk_add_f32 v[130:131],v[130:131],v[226:227]
v_cvt_pk_bf16_f32 v128,v128,v129
v_cvt_pk_bf16_f32 v129,v130,v131
s_mov_b32 s73,82016
buffer_store_dwordx2 v[128:129],v248,s[36:39],s73 offen
v_lshlrev_b32 v224,16,v30
v_and_b32 v225,s67,v30
v_pk_add_f32 v[144:145],v[144:145],v[224:225]
v_lshlrev_b32 v226,16,v31
v_and_b32 v227,s67,v31
v_pk_add_f32 v[146:147],v[146:147],v[226:227]
v_lshlrev_b32 v224,16,v62
v_and_b32 v225,s67,v62
v_pk_add_f32 v[144:145],v[144:145],v[224:225]
v_lshlrev_b32 v226,16,v63
v_and_b32 v227,s67,v63
v_pk_add_f32 v[146:147],v[146:147],v[226:227]
v_lshlrev_b32 v224,16,v94
v_and_b32 v225,s67,v94
v_pk_add_f32 v[144:145],v[144:145],v[224:225]
v_lshlrev_b32 v226,16,v95
v_and_b32 v227,s67,v95
v_pk_add_f32 v[146:147],v[146:147],v[226:227]
v_cvt_pk_bf16_f32 v144,v144,v145
v_cvt_pk_bf16_f32 v145,v146,v147
s_mov_b32 s73,245856
buffer_store_dwordx2 v[144:145],v248,s[36:39],s73 offen
s_waitcnt vmcnt(13)
v_lshlrev_b32 v224,16,v124
v_and_b32 v225,s67,v124
v_pk_add_f32 v[160:161],v[160:161],v[224:225]
v_lshlrev_b32 v226,16,v125
v_and_b32 v227,s67,v125
v_pk_add_f32 v[162:163],v[162:163],v[226:227]
v_lshlrev_b32 v224,16,v156
v_and_b32 v225,s67,v156
v_pk_add_f32 v[160:161],v[160:161],v[224:225]
v_lshlrev_b32 v226,16,v157
v_and_b32 v227,s67,v157
v_pk_add_f32 v[162:163],v[162:163],v[226:227]
v_lshlrev_b32 v224,16,v188
v_and_b32 v225,s67,v188
v_pk_add_f32 v[160:161],v[160:161],v[224:225]
v_lshlrev_b32 v226,16,v189
v_and_b32 v227,s67,v189
v_pk_add_f32 v[162:163],v[162:163],v[226:227]
v_cvt_pk_bf16_f32 v160,v160,v161
v_cvt_pk_bf16_f32 v161,v162,v163
s_mov_b32 s73,128
buffer_store_dwordx2 v[160:161],v248,s[36:39],s73 offen
v_lshlrev_b32 v224,16,v126
v_and_b32 v225,s67,v126
v_pk_add_f32 v[176:177],v[176:177],v[224:225]
v_lshlrev_b32 v226,16,v127
v_and_b32 v227,s67,v127
v_pk_add_f32 v[178:179],v[178:179],v[226:227]
v_lshlrev_b32 v224,16,v158
v_and_b32 v225,s67,v158
v_pk_add_f32 v[176:177],v[176:177],v[224:225]
v_lshlrev_b32 v226,16,v159
v_and_b32 v227,s67,v159
v_pk_add_f32 v[178:179],v[178:179],v[226:227]
v_lshlrev_b32 v224,16,v190
v_and_b32 v225,s67,v190
v_pk_add_f32 v[176:177],v[176:177],v[224:225]
v_lshlrev_b32 v226,16,v191
v_and_b32 v227,s67,v191
v_pk_add_f32 v[178:179],v[178:179],v[226:227]
v_cvt_pk_bf16_f32 v176,v176,v177
v_cvt_pk_bf16_f32 v177,v178,v179
s_mov_b32 s73,163968
buffer_store_dwordx2 v[176:177],v248,s[36:39],s73 offen
s_waitcnt vmcnt(12)
v_lshlrev_b32 v224,16,v200
v_and_b32 v225,s67,v200
v_pk_add_f32 v[192:193],v[192:193],v[224:225]
v_lshlrev_b32 v226,16,v201
v_and_b32 v227,s67,v201
v_pk_add_f32 v[194:195],v[194:195],v[226:227]
v_lshlrev_b32 v224,16,v204
v_and_b32 v225,s67,v204
v_pk_add_f32 v[192:193],v[192:193],v[224:225]
v_lshlrev_b32 v226,16,v205
v_and_b32 v227,s67,v205
v_pk_add_f32 v[194:195],v[194:195],v[226:227]
v_lshlrev_b32 v224,16,v208
v_and_b32 v225,s67,v208
v_pk_add_f32 v[192:193],v[192:193],v[224:225]
v_lshlrev_b32 v226,16,v209
v_and_b32 v227,s67,v209
v_pk_add_f32 v[194:195],v[194:195],v[226:227]
v_cvt_pk_bf16_f32 v192,v192,v193
v_cvt_pk_bf16_f32 v193,v194,v195
s_mov_b32 s73,327808
buffer_store_dwordx2 v[192:193],v248,s[36:39],s73 offen
s_branch LEND_%=
LFIN1_%=:
s_lshl_b32 s64,s50,4
s_add_u32 s64,s64,s48
s_lshl_b32 s64,s64,1
s_add_u32 s64,s64,s34
s_mul_i32 s65,s64,458752
s_mul_i32 s66,s16,28672
s_add_u32 s65,s65,s66
s_add_u32 s65,s65,62980096
s_add_u32 s44,s98,s65
s_addc_u32 s45,s99,0
s_mov_b32 s46,372736
s_lshl_b32 s65,s64,8
s_add_u32 s65,s65,32768
s_add_u32 s12,s98,s65
s_addc_u32 s13,s99,0
s_mov_b32 s14,196
s_mov_b64 s[8:9],s[12:13]
s_cmp_eq_u32 s16,0
s_cselect_b32 s10,68,0
v_cvt_pk_bf16_f32 v0,v0,v1
v_cvt_pk_bf16_f32 v1,v2,v3
v_cvt_pk_bf16_f32 v2,v16,v17
v_cvt_pk_bf16_f32 v3,v18,v19
s_mov_b32 s73,114688
buffer_store_dwordx4 v[0:3],v253,s[44:47],s73 offen sc1
v_cvt_pk_bf16_f32 v32,v32,v33
v_cvt_pk_bf16_f32 v33,v34,v35
v_cvt_pk_bf16_f32 v34,v48,v49
v_cvt_pk_bf16_f32 v35,v50,v51
s_mov_b32 s73,115712
buffer_store_dwordx4 v[32:35],v253,s[44:47],s73 offen sc1
v_cvt_pk_bf16_f32 v64,v64,v65
v_cvt_pk_bf16_f32 v65,v66,v67
v_cvt_pk_bf16_f32 v66,v80,v81
v_cvt_pk_bf16_f32 v67,v82,v83
s_mov_b32 s73,116736
buffer_store_dwordx4 v[64:67],v253,s[44:47],s73 offen sc1
v_cvt_pk_bf16_f32 v96,v96,v97
v_cvt_pk_bf16_f32 v97,v98,v99
v_cvt_pk_bf16_f32 v98,v112,v113
v_cvt_pk_bf16_f32 v99,v114,v115
s_mov_b32 s73,117760
buffer_store_dwordx4 v[96:99],v253,s[44:47],s73 offen sc1
v_cvt_pk_bf16_f32 v128,v128,v129
v_cvt_pk_bf16_f32 v129,v130,v131
v_cvt_pk_bf16_f32 v130,v144,v145
v_cvt_pk_bf16_f32 v131,v146,v147
s_mov_b32 s73,118784
buffer_store_dwordx4 v[128:131],v253,s[44:47],s73 offen sc1
v_cvt_pk_bf16_f32 v160,v160,v161
v_cvt_pk_bf16_f32 v161,v162,v163
v_cvt_pk_bf16_f32 v162,v176,v177
v_cvt_pk_bf16_f32 v163,v178,v179
s_mov_b32 s73,119808
buffer_store_dwordx4 v[160:163],v253,s[44:47],s73 offen sc1
v_cvt_pk_bf16_f32 v192,v192,v193
v_cvt_pk_bf16_f32 v193,v194,v195
s_mov_b32 s73,120832
buffer_store_dwordx4 v[192:195],v253,s[44:47],s73 offen sc1
v_cvt_pk_bf16_f32 v8,v8,v9
v_cvt_pk_bf16_f32 v9,v10,v11
v_cvt_pk_bf16_f32 v10,v24,v25
v_cvt_pk_bf16_f32 v11,v26,v27
s_mov_b32 s73,129024
buffer_store_dwordx4 v[8:11],v253,s[44:47],s73 offen sc1
v_cvt_pk_bf16_f32 v40,v40,v41
v_cvt_pk_bf16_f32 v41,v42,v43
v_cvt_pk_bf16_f32 v42,v56,v57
v_cvt_pk_bf16_f32 v43,v58,v59
s_mov_b32 s73,130048
buffer_store_dwordx4 v[40:43],v253,s[44:47],s73 offen sc1
v_cvt_pk_bf16_f32 v72,v72,v73
v_cvt_pk_bf16_f32 v73,v74,v75
v_cvt_pk_bf16_f32 v74,v88,v89
v_cvt_pk_bf16_f32 v75,v90,v91
s_mov_b32 s73,131072
buffer_store_dwordx4 v[72:75],v253,s[44:47],s73 offen sc1
v_cvt_pk_bf16_f32 v104,v104,v105
v_cvt_pk_bf16_f32 v105,v106,v107
v_cvt_pk_bf16_f32 v106,v120,v121
v_cvt_pk_bf16_f32 v107,v122,v123
s_mov_b32 s73,132096
buffer_store_dwordx4 v[104:107],v253,s[44:47],s73 offen sc1
v_cvt_pk_bf16_f32 v136,v136,v137
v_cvt_pk_bf16_f32 v137,v138,v139
v_cvt_pk_bf16_f32 v138,v152,v153
v_cvt_pk_bf16_f32 v139,v154,v155
s_mov_b32 s73,133120
buffer_store_dwordx4 v[136:139],v253,s[44:47],s73 offen sc1
v_cvt_pk_bf16_f32 v168,v168,v169
v_cvt_pk_bf16_f32 v169,v170,v171
v_cvt_pk_bf16_f32 v170,v184,v185
v_cvt_pk_bf16_f32 v171,v186,v187
s_mov_b32 s73,134144
buffer_store_dwordx4 v[168:171],v253,s[44:47],s73 offen sc1
v_cvt_pk_bf16_f32 v12,v12,v13
v_cvt_pk_bf16_f32 v13,v14,v15
v_cvt_pk_bf16_f32 v14,v28,v29
v_cvt_pk_bf16_f32 v15,v30,v31
s_mov_b32 s73,136192
buffer_store_dwordx4 v[12:15],v253,s[44:47],s73 offen sc1
v_cvt_pk_bf16_f32 v44,v44,v45
v_cvt_pk_bf16_f32 v45,v46,v47
v_cvt_pk_bf16_f32 v46,v60,v61
v_cvt_pk_bf16_f32 v47,v62,v63
s_mov_b32 s73,137216
buffer_store_dwordx4 v[44:47],v253,s[44:47],s73 offen sc1
v_cvt_pk_bf16_f32 v76,v76,v77
v_cvt_pk_bf16_f32 v77,v78,v79
v_cvt_pk_bf16_f32 v78,v92,v93
v_cvt_pk_bf16_f32 v79,v94,v95
s_mov_b32 s73,138240
buffer_store_dwordx4 v[76:79],v253,s[44:47],s73 offen sc1
v_cvt_pk_bf16_f32 v108,v108,v109
v_cvt_pk_bf16_f32 v109,v110,v111
v_cvt_pk_bf16_f32 v110,v124,v125
v_cvt_pk_bf16_f32 v111,v126,v127
s_mov_b32 s73,139264
buffer_store_dwordx4 v[108:111],v253,s[44:47],s73 offen sc1
v_cvt_pk_bf16_f32 v140,v140,v141
v_cvt_pk_bf16_f32 v141,v142,v143
v_cvt_pk_bf16_f32 v142,v156,v157
v_cvt_pk_bf16_f32 v143,v158,v159
s_mov_b32 s73,140288
buffer_store_dwordx4 v[140:143],v253,s[44:47],s73 offen sc1
v_cvt_pk_bf16_f32 v172,v172,v173
v_cvt_pk_bf16_f32 v173,v174,v175
v_cvt_pk_bf16_f32 v174,v188,v189
v_cvt_pk_bf16_f32 v175,v190,v191
s_mov_b32 s73,141312
buffer_store_dwordx4 v[172:175],v253,s[44:47],s73 offen sc1
v_mbcnt_lo_u32_b32 v254,-1,0
v_mbcnt_hi_u32_b32 v254,-1,v254
v_and_b32 v255,15,v254
v_mul_u32_u24 v255,2560,v255
v_lshrrev_b32 v254,4,v254
v_lshlrev_b32 v254,3,v254
v_add_u32 v248,v254,v255
s_mul_i32 s64,s48,409600
s_mul_i32 s65,s50,1280
s_add_u32 s64,s64,s65
s_mul_i32 s65,s34,640
s_add_u32 s64,s64,s65
s_mul_i32 s65,s16,160
s_add_u32 s64,s64,s65
s_add_u32 s36,s96,s64
s_addc_u32 s37,s97,0
s_mov_b32 s38,407200
s_mov_b32 s67,4294901760
s_waitcnt vmcnt(0)
s_barrier
buffer_store_dword v247,v253,s[8:11],0 offen offset:64 sc1
s_mov_b32 s71,65536
LFS1_%=:
buffer_load_dword v246,v253,s[12:15],0 offen sc1
s_waitcnt vmcnt(0)
v_cmp_ne_u32 vcc,0,v246
s_sub_u32 s71,s71,1
s_cmp_eq_u32 s71,0
s_cbranch_scc1 LFS1_%=X
s_nop 0
s_and_b32 s70,vcc_lo,4353
s_cmp_eq_u32 s70,4353
s_cbranch_scc0 LFS1_%=
LFS1_%=X:
s_mov_b32 s73,7168
buffer_load_dwordx4 v[16:19],v253,s[44:47],s73 offen sc1
s_mov_b32 s73,236544
buffer_load_dwordx4 v[48:51],v253,s[44:47],s73 offen sc1
s_mov_b32 s73,351232
buffer_load_dwordx4 v[80:83],v253,s[44:47],s73 offen sc1
s_mov_b32 s73,8192
buffer_load_dwordx4 v[112:115],v253,s[44:47],s73 offen sc1
s_mov_b32 s73,237568
buffer_load_dwordx4 v[144:147],v253,s[44:47],s73 offen sc1
s_mov_b32 s73,352256
buffer_load_dwordx4 v[176:179],v253,s[44:47],s73 offen sc1
s_mov_b32 s73,9216
buffer_load_dwordx4 v[24:27],v253,s[44:47],s73 offen sc1
s_mov_b32 s73,238592
buffer_load_dwordx4 v[56:59],v253,s[44:47],s73 offen sc1
s_mov_b32 s73,353280
buffer_load_dwordx4 v[88:91],v253,s[44:47],s73 offen sc1
s_mov_b32 s73,10240
buffer_load_dwordx4 v[120:123],v253,s[44:47],s73 offen sc1
s_mov_b32 s73,239616
buffer_load_dwordx4 v[152:155],v253,s[44:47],s73 offen sc1
s_mov_b32 s73,354304
buffer_load_dwordx4 v[184:187],v253,s[44:47],s73 offen sc1
s_mov_b32 s73,11264
buffer_load_dwordx4 v[28:31],v253,s[44:47],s73 offen sc1
s_mov_b32 s73,240640
buffer_load_dwordx4 v[60:63],v253,s[44:47],s73 offen sc1
s_mov_b32 s73,355328
buffer_load_dwordx4 v[92:95],v253,s[44:47],s73 offen sc1
s_mov_b32 s73,12288
buffer_load_dwordx4 v[124:127],v253,s[44:47],s73 offen sc1
s_mov_b32 s73,241664
buffer_load_dwordx4 v[156:159],v253,s[44:47],s73 offen sc1
s_mov_b32 s73,356352
buffer_load_dwordx4 v[188:191],v253,s[44:47],s73 offen sc1
s_mov_b32 s73,13312
buffer_load_dwordx4 v[200:203],v253,s[44:47],s73 offen sc1
s_mov_b32 s73,242688
buffer_load_dwordx4 v[204:207],v253,s[44:47],s73 offen sc1
s_mov_b32 s73,357376
buffer_load_dwordx4 v[208:211],v253,s[44:47],s73 offen sc1
s_waitcnt vmcnt(18)
v_lshlrev_b32 v224,16,v16
v_and_b32 v225,s67,v16
v_pk_add_f32 v[4:5],v[4:5],v[224:225]
v_lshlrev_b32 v226,16,v17
v_and_b32 v227,s67,v17
v_pk_add_f32 v[6:7],v[6:7],v[226:227]
v_lshlrev_b32 v224,16,v48
v_and_b32 v225,s67,v48
v_pk_add_f32 v[4:5],v[4:5],v[224:225]
v_lshlrev_b32 v226,16,v49
v_and_b32 v227,s67,v49
v_pk_add_f32 v[6:7],v[6:7],v[226:227]
v_lshlrev_b32 v224,16,v80
v_and_b32 v225,s67,v80
v_pk_add_f32 v[4:5],v[4:5],v[224:225]
v_lshlrev_b32 v226,16,v81
v_and_b32 v227,s67,v81
v_pk_add_f32 v[6:7],v[6:7],v[226:227]
v_cvt_pk_bf16_f32 v4,v4,v5
v_cvt_pk_bf16_f32 v5,v6,v7
s_mov_b32 s73,40960
buffer_store_dwordx2 v[4:5],v248,s[36:39],s73 offen
v_lshlrev_b32 v224,16,v18
v_and_b32 v225,s67,v18
v_pk_add_f32 v[20:21],v[20:21],v[224:225]
v_lshlrev_b32 v226,16,v19
v_and_b32 v227,s67,v19
v_pk_add_f32 v[22:23],v[22:23],v[226:227]
v_lshlrev_b32 v224,16,v50
v_and_b32 v225,s67,v50
v_pk_add_f32 v[20:21],v[20:21],v[224:225]
v_lshlrev_b32 v226,16,v51
v_and_b32 v227,s67,v51
v_pk_add_f32 v[22:23],v[22:23],v[226:227]
v_lshlrev_b32 v224,16,v82
v_and_b32 v225,s67,v82
v_pk_add_f32 v[20:21],v[20:21],v[224:225]
v_lshlrev_b32 v226,16,v83
v_and_b32 v227,s67,v83
v_pk_add_f32 v[22:23],v[22:23],v[226:227]
v_cvt_pk_bf16_f32 v20,v20,v21
v_cvt_pk_bf16_f32 v21,v22,v23
s_mov_b32 s73,204800
buffer_store_dwordx2 v[20:21],v248,s[36:39],s73 offen
s_waitcnt vmcnt(17)
v_lshlrev_b32 v224,16,v112
v_and_b32 v225,s67,v112
v_pk_add_f32 v[36:37],v[36:37],v[224:225]
v_lshlrev_b32 v226,16,v113
v_and_b32 v227,s67,v113
v_pk_add_f32 v[38:39],v[38:39],v[226:227]
v_lshlrev_b32 v224,16,v144
v_and_b32 v225,s67,v144
v_pk_add_f32 v[36:37],v[36:37],v[224:225]
v_lshlrev_b32 v226,16,v145
v_and_b32 v227,s67,v145
v_pk_add_f32 v[38:39],v[38:39],v[226:227]
v_lshlrev_b32 v224,16,v176
v_and_b32 v225,s67,v176
v_pk_add_f32 v[36:37],v[36:37],v[224:225]
v_lshlrev_b32 v226,16,v177
v_and_b32 v227,s67,v177
v_pk_add_f32 v[38:39],v[38:39],v[226:227]
v_cvt_pk_bf16_f32 v36,v36,v37
v_cvt_pk_bf16_f32 v37,v38,v39
s_mov_b32 s73,368640
buffer_store_dwordx2 v[36:37],v248,s[36:39],s73 offen
v_lshlrev_b32 v224,16,v114
v_and_b32 v225,s67,v114
v_pk_add_f32 v[52:53],v[52:53],v[224:225]
v_lshlrev_b32 v226,16,v115
v_and_b32 v227,s67,v115
v_pk_add_f32 v[54:55],v[54:55],v[226:227]
v_lshlrev_b32 v224,16,v146
v_and_b32 v225,s67,v146
v_pk_add_f32 v[52:53],v[52:53],v[224:225]
v_lshlrev_b32 v226,16,v147
v_and_b32 v227,s67,v147
v_pk_add_f32 v[54:55],v[54:55],v[226:227]
v_lshlrev_b32 v224,16,v178
v_and_b32 v225,s67,v178
v_pk_add_f32 v[52:53],v[52:53],v[224:225]
v_lshlrev_b32 v226,16,v179
v_and_b32 v227,s67,v179
v_pk_add_f32 v[54:55],v[54:55],v[226:227]
v_cvt_pk_bf16_f32 v52,v52,v53
v_cvt_pk_bf16_f32 v53,v54,v55
s_mov_b32 s73,122912
buffer_store_dwordx2 v[52:53],v248,s[36:39],s73 offen
s_waitcnt vmcnt(16)
v_lshlrev_b32 v224,16,v24
v_and_b32 v225,s67,v24
v_pk_add_f32 v[68:69],v[68:69],v[224:225]
v_lshlrev_b32 v226,16,v25
v_and_b32 v227,s67,v25
v_pk_add_f32 v[70:71],v[70:71],v[226:227]
v_lshlrev_b32 v224,16,v56
v_and_b32 v225,s67,v56
v_pk_add_f32 v[68:69],v[68:69],v[224:225]
v_lshlrev_b32 v226,16,v57
v_and_b32 v227,s67,v57
v_pk_add_f32 v[70:71],v[70:71],v[226:227]
v_lshlrev_b32 v224,16,v88
v_and_b32 v225,s67,v88
v_pk_add_f32 v[68:69],v[68:69],v[224:225]
v_lshlrev_b32 v226,16,v89
v_and_b32 v227,s67,v89
v_pk_add_f32 v[70:71],v[70:71],v[226:227]
v_cvt_pk_bf16_f32 v68,v68,v69
v_cvt_pk_bf16_f32 v69,v70,v71
s_mov_b32 s73,286752
buffer_store_dwordx2 v[68:69],v248,s[36:39],s73 offen
v_lshlrev_b32 v224,16,v26
v_and_b32 v225,s67,v26
v_pk_add_f32 v[84:85],v[84:85],v[224:225]
v_lshlrev_b32 v226,16,v27
v_and_b32 v227,s67,v27
v_pk_add_f32 v[86:87],v[86:87],v[226:227]
v_lshlrev_b32 v224,16,v58
v_and_b32 v225,s67,v58
v_pk_add_f32 v[84:85],v[84:85],v[224:225]
v_lshlrev_b32 v226,16,v59
v_and_b32 v227,s67,v59
v_pk_add_f32 v[86:87],v[86:87],v[226:227]
v_lshlrev_b32 v224,16,v90
v_and_b32 v225,s67,v90
v_pk_add_f32 v[84:85],v[84:85],v[224:225]
v_lshlrev_b32 v226,16,v91
v_and_b32 v227,s67,v91
v_pk_add_f32 v[86:87],v[86:87],v[226:227]
v_cvt_pk_bf16_f32 v84,v84,v85
v_cvt_pk_bf16_f32 v85,v86,v87
s_mov_b32 s73,41024
buffer_store_dwordx2 v[84:85],v248,s[36:39],s73 offen
s_waitcnt vmcnt(15)
v_lshlrev_b32 v224,16,v120
v_and_b32 v225,s67,v120
v_pk_add_f32 v[100:101],v[100:101],v[224:225]
v_lshlrev_b32 v226,16,v121
v_and_b32 v227,s67,v121
v_pk_add_f32 v[102:103],v[102:103],v[226:227]
v_lshlrev_b32 v224,16,v152
v_and_b32 v225,s67,v152
v_pk_add_f32 v[100:101],v[100:101],v[224:225]
v_lshlrev_b32 v226,16,v153
v_and_b32 v227,s67,v153
v_pk_add_f32 v[102:103],v[102:103],v[226:227]
v_lshlrev_b32 v224,16,v184
v_and_b32 v225,s67,v184
v_pk_add_f32 v[100:101],v[100:101],v[224:225]
v_lshlrev_b32 v226,16,v185
v_and_b32 v227,s67,v185
v_pk_add_f32 v[102:103],v[102:103],v[226:227]
v_cvt_pk_bf16_f32 v100,v100,v101
v_cvt_pk_bf16_f32 v101,v102,v103
s_mov_b32 s73,204864
buffer_store_dwordx2 v[100:101],v248,s[36:39],s73 offen
v_lshlrev_b32 v224,16,v122
v_and_b32 v225,s67,v122
v_pk_add_f32 v[116:117],v[116:117],v[224:225]
v_lshlrev_b32 v226,16,v123
v_and_b32 v227,s67,v123
v_pk_add_f32 v[118:119],v[118:119],v[226:227]
v_lshlrev_b32 v224,16,v154
v_and_b32 v225,s67,v154
v_pk_add_f32 v[116:117],v[116:117],v[224:225]
v_lshlrev_b32 v226,16,v155
v_and_b32 v227,s67,v155
v_pk_add_f32 v[118:119],v[118:119],v[226:227]
v_lshlrev_b32 v224,16,v186
v_and_b32 v225,s67,v186
v_pk_add_f32 v[116:117],v[116:117],v[224:225]
v_lshlrev_b32 v226,16,v187
v_and_b32 v227,s67,v187
v_pk_add_f32 v[118:119],v[118:119],v[226:227]
v_cvt_pk_bf16_f32 v116,v116,v117
v_cvt_pk_bf16_f32 v117,v118,v119
s_mov_b32 s73,368704
buffer_store_dwordx2 v[116:117],v248,s[36:39],s73 offen
s_waitcnt vmcnt(14)
v_lshlrev_b32 v224,16,v28
v_and_b32 v225,s67,v28
v_pk_add_f32 v[132:133],v[132:133],v[224:225]
v_lshlrev_b32 v226,16,v29
v_and_b32 v227,s67,v29
v_pk_add_f32 v[134:135],v[134:135],v[226:227]
v_lshlrev_b32 v224,16,v60
v_and_b32 v225,s67,v60
v_pk_add_f32 v[132:133],v[132:133],v[224:225]
v_lshlrev_b32 v226,16,v61
v_and_b32 v227,s67,v61
v_pk_add_f32 v[134:135],v[134:135],v[226:227]
v_lshlrev_b32 v224,16,v92
v_and_b32 v225,s67,v92
v_pk_add_f32 v[132:133],v[132:133],v[224:225]
v_lshlrev_b32 v226,16,v93
v_and_b32 v227,s67,v93
v_pk_add_f32 v[134:135],v[134:135],v[226:227]
v_cvt_pk_bf16_f32 v132,v132,v133
v_cvt_pk_bf16_f32 v133,v134,v135
s_mov_b32 s73,122976
buffer_store_dwordx2 v[132:133],v248,s[36:39],s73 offen
v_lshlrev_b32 v224,16,v30
v_and_b32 v225,s67,v30
v_pk_add_f32 v[148:149],v[148:149],v[224:225]
v_lshlrev_b32 v226,16,v31
v_and_b32 v227,s67,v31
v_pk_add_f32 v[150:151],v[150:151],v[226:227]
v_lshlrev_b32 v224,16,v62
v_and_b32 v225,s67,v62
v_pk_add_f32 v[148:149],v[148:149],v[224:225]
v_lshlrev_b32 v226,16,v63
v_and_b32 v227,s67,v63
v_pk_add_f32 v[150:151],v[150:151],v[226:227]
v_lshlrev_b32 v224,16,v94
v_and_b32 v225,s67,v94
v_pk_add_f32 v[148:149],v[148:149],v[224:225]
v_lshlrev_b32 v226,16,v95
v_and_b32 v227,s67,v95
v_pk_add_f32 v[150:151],v[150:151],v[226:227]
v_cvt_pk_bf16_f32 v148,v148,v149
v_cvt_pk_bf16_f32 v149,v150,v151
s_mov_b32 s73,286816
buffer_store_dwordx2 v[148:149],v248,s[36:39],s73 offen
s_waitcnt vmcnt(13)
v_lshlrev_b32 v224,16,v124
v_and_b32 v225,s67,v124
v_pk_add_f32 v[164:165],v[164:165],v[224:225]
v_lshlrev_b32 v226,16,v125
v_and_b32 v227,s67,v125
v_pk_add_f32 v[166:167],v[166:167],v[226:227]
v_lshlrev_b32 v224,16,v156
v_and_b32 v225,s67,v156
v_pk_add_f32 v[164:165],v[164:165],v[224:225]
v_lshlrev_b32 v226,16,v157
v_and_b32 v227,s67,v157
v_pk_add_f32 v[166:167],v[166:167],v[226:227]
v_lshlrev_b32 v224,16,v188
v_and_b32 v225,s67,v188
v_pk_add_f32 v[164:165],v[164:165],v[224:225]
v_lshlrev_b32 v226,16,v189
v_and_b32 v227,s67,v189
v_pk_add_f32 v[166:167],v[166:167],v[226:227]
v_cvt_pk_bf16_f32 v164,v164,v165
v_cvt_pk_bf16_f32 v165,v166,v167
s_mov_b32 s73,41088
buffer_store_dwordx2 v[164:165],v248,s[36:39],s73 offen
v_lshlrev_b32 v224,16,v126
v_and_b32 v225,s67,v126
v_pk_add_f32 v[180:181],v[180:181],v[224:225]
v_lshlrev_b32 v226,16,v127
v_and_b32 v227,s67,v127
v_pk_add_f32 v[182:183],v[182:183],v[226:227]
v_lshlrev_b32 v224,16,v158
v_and_b32 v225,s67,v158
v_pk_add_f32 v[180:181],v[180:181],v[224:225]
v_lshlrev_b32 v226,16,v159
v_and_b32 v227,s67,v159
v_pk_add_f32 v[182:183],v[182:183],v[226:227]
v_lshlrev_b32 v224,16,v190
v_and_b32 v225,s67,v190
v_pk_add_f32 v[180:181],v[180:181],v[224:225]
v_lshlrev_b32 v226,16,v191
v_and_b32 v227,s67,v191
v_pk_add_f32 v[182:183],v[182:183],v[226:227]
v_cvt_pk_bf16_f32 v180,v180,v181
v_cvt_pk_bf16_f32 v181,v182,v183
s_mov_b32 s73,204928
buffer_store_dwordx2 v[180:181],v248,s[36:39],s73 offen
s_waitcnt vmcnt(12)
v_lshlrev_b32 v224,16,v200
v_and_b32 v225,s67,v200
v_pk_add_f32 v[196:197],v[196:197],v[224:225]
v_lshlrev_b32 v226,16,v201
v_and_b32 v227,s67,v201
v_pk_add_f32 v[198:199],v[198:199],v[226:227]
v_lshlrev_b32 v224,16,v204
v_and_b32 v225,s67,v204
v_pk_add_f32 v[196:197],v[196:197],v[224:225]
v_lshlrev_b32 v226,16,v205
v_and_b32 v227,s67,v205
v_pk_add_f32 v[198:199],v[198:199],v[226:227]
v_lshlrev_b32 v224,16,v208
v_and_b32 v225,s67,v208
v_pk_add_f32 v[196:197],v[196:197],v[224:225]
v_lshlrev_b32 v226,16,v209
v_and_b32 v227,s67,v209
v_pk_add_f32 v[198:199],v[198:199],v[226:227]
v_cvt_pk_bf16_f32 v196,v196,v197
v_cvt_pk_bf16_f32 v197,v198,v199
s_mov_b32 s73,368768
buffer_store_dwordx2 v[196:197],v248,s[36:39],s73 offen
s_branch LEND_%=
LFIN2_%=:
s_lshl_b32 s64,s50,4
s_add_u32 s64,s64,s48
s_lshl_b32 s64,s64,1
s_add_u32 s64,s64,s34
s_mul_i32 s65,s64,458752
s_mul_i32 s66,s16,28672
s_add_u32 s65,s65,s66
s_add_u32 s65,s65,62980096
s_add_u32 s44,s98,s65
s_addc_u32 s45,s99,0
s_mov_b32 s46,372736
s_lshl_b32 s65,s64,8
s_add_u32 s65,s65,32768
s_add_u32 s12,s98,s65
s_addc_u32 s13,s99,0
s_mov_b32 s14,196
s_mov_b64 s[8:9],s[12:13]
s_cmp_eq_u32 s16,0
s_cselect_b32 s10,132,0
v_cvt_pk_bf16_f32 v0,v0,v1
v_cvt_pk_bf16_f32 v1,v2,v3
v_cvt_pk_bf16_f32 v2,v16,v17
v_cvt_pk_bf16_f32 v3,v18,v19
s_mov_b32 s73,229376
buffer_store_dwordx4 v[0:3],v253,s[44:47],s73 offen sc1
v_cvt_pk_bf16_f32 v32,v32,v33
v_cvt_pk_bf16_f32 v33,v34,v35
v_cvt_pk_bf16_f32 v34,v48,v49
v_cvt_pk_bf16_f32 v35,v50,v51
s_mov_b32 s73,230400
buffer_store_dwordx4 v[32:35],v253,s[44:47],s73 offen sc1
v_cvt_pk_bf16_f32 v64,v64,v65
v_cvt_pk_bf16_f32 v65,v66,v67
v_cvt_pk_bf16_f32 v66,v80,v81
v_cvt_pk_bf16_f32 v67,v82,v83
s_mov_b32 s73,231424
buffer_store_dwordx4 v[64:67],v253,s[44:47],s73 offen sc1
v_cvt_pk_bf16_f32 v96,v96,v97
v_cvt_pk_bf16_f32 v97,v98,v99
v_cvt_pk_bf16_f32 v98,v112,v113
v_cvt_pk_bf16_f32 v99,v114,v115
s_mov_b32 s73,232448
buffer_store_dwordx4 v[96:99],v253,s[44:47],s73 offen sc1
v_cvt_pk_bf16_f32 v128,v128,v129
v_cvt_pk_bf16_f32 v129,v130,v131
v_cvt_pk_bf16_f32 v130,v144,v145
v_cvt_pk_bf16_f32 v131,v146,v147
s_mov_b32 s73,233472
buffer_store_dwordx4 v[128:131],v253,s[44:47],s73 offen sc1
v_cvt_pk_bf16_f32 v160,v160,v161
v_cvt_pk_bf16_f32 v161,v162,v163
v_cvt_pk_bf16_f32 v162,v176,v177
v_cvt_pk_bf16_f32 v163,v178,v179
s_mov_b32 s73,234496
buffer_store_dwordx4 v[160:163],v253,s[44:47],s73 offen sc1
v_cvt_pk_bf16_f32 v192,v192,v193
v_cvt_pk_bf16_f32 v193,v194,v195
s_mov_b32 s73,235520
buffer_store_dwordx4 v[192:195],v253,s[44:47],s73 offen sc1
v_cvt_pk_bf16_f32 v4,v4,v5
v_cvt_pk_bf16_f32 v5,v6,v7
v_cvt_pk_bf16_f32 v6,v20,v21
v_cvt_pk_bf16_f32 v7,v22,v23
s_mov_b32 s73,236544
buffer_store_dwordx4 v[4:7],v253,s[44:47],s73 offen sc1
v_cvt_pk_bf16_f32 v36,v36,v37
v_cvt_pk_bf16_f32 v37,v38,v39
v_cvt_pk_bf16_f32 v38,v52,v53
v_cvt_pk_bf16_f32 v39,v54,v55
s_mov_b32 s73,237568
buffer_store_dwordx4 v[36:39],v253,s[44:47],s73 offen sc1
v_cvt_pk_bf16_f32 v68,v68,v69
v_cvt_pk_bf16_f32 v69,v70,v71
v_cvt_pk_bf16_f32 v70,v84,v85
v_cvt_pk_bf16_f32 v71,v86,v87
s_mov_b32 s73,238592
buffer_store_dwordx4 v[68:71],v253,s[44:47],s73 offen sc1
v_cvt_pk_bf16_f32 v100,v100,v101
v_cvt_pk_bf16_f32 v101,v102,v103
v_cvt_pk_bf16_f32 v102,v116,v117
v_cvt_pk_bf16_f32 v103,v118,v119
s_mov_b32 s73,239616
buffer_store_dwordx4 v[100:103],v253,s[44:47],s73 offen sc1
v_cvt_pk_bf16_f32 v132,v132,v133
v_cvt_pk_bf16_f32 v133,v134,v135
v_cvt_pk_bf16_f32 v134,v148,v149
v_cvt_pk_bf16_f32 v135,v150,v151
s_mov_b32 s73,240640
buffer_store_dwordx4 v[132:135],v253,s[44:47],s73 offen sc1
v_cvt_pk_bf16_f32 v164,v164,v165
v_cvt_pk_bf16_f32 v165,v166,v167
v_cvt_pk_bf16_f32 v166,v180,v181
v_cvt_pk_bf16_f32 v167,v182,v183
s_mov_b32 s73,241664
buffer_store_dwordx4 v[164:167],v253,s[44:47],s73 offen sc1
v_cvt_pk_bf16_f32 v196,v196,v197
v_cvt_pk_bf16_f32 v197,v198,v199
s_mov_b32 s73,242688
buffer_store_dwordx4 v[196:199],v253,s[44:47],s73 offen sc1
v_cvt_pk_bf16_f32 v12,v12,v13
v_cvt_pk_bf16_f32 v13,v14,v15
v_cvt_pk_bf16_f32 v14,v28,v29
v_cvt_pk_bf16_f32 v15,v30,v31
s_mov_b32 s73,250880
buffer_store_dwordx4 v[12:15],v253,s[44:47],s73 offen sc1
v_cvt_pk_bf16_f32 v44,v44,v45
v_cvt_pk_bf16_f32 v45,v46,v47
v_cvt_pk_bf16_f32 v46,v60,v61
v_cvt_pk_bf16_f32 v47,v62,v63
s_mov_b32 s73,251904
buffer_store_dwordx4 v[44:47],v253,s[44:47],s73 offen sc1
v_cvt_pk_bf16_f32 v76,v76,v77
v_cvt_pk_bf16_f32 v77,v78,v79
v_cvt_pk_bf16_f32 v78,v92,v93
v_cvt_pk_bf16_f32 v79,v94,v95
s_mov_b32 s73,252928
buffer_store_dwordx4 v[76:79],v253,s[44:47],s73 offen sc1
v_cvt_pk_bf16_f32 v108,v108,v109
v_cvt_pk_bf16_f32 v109,v110,v111
v_cvt_pk_bf16_f32 v110,v124,v125
v_cvt_pk_bf16_f32 v111,v126,v127
s_mov_b32 s73,253952
buffer_store_dwordx4 v[108:111],v253,s[44:47],s73 offen sc1
v_cvt_pk_bf16_f32 v140,v140,v141
v_cvt_pk_bf16_f32 v141,v142,v143
v_cvt_pk_bf16_f32 v142,v156,v157
v_cvt_pk_bf16_f32 v143,v158,v159
s_mov_b32 s73,254976
buffer_store_dwordx4 v[140:143],v253,s[44:47],s73 offen sc1
v_cvt_pk_bf16_f32 v172,v172,v173
v_cvt_pk_bf16_f32 v173,v174,v175
v_cvt_pk_bf16_f32 v174,v188,v189
v_cvt_pk_bf16_f32 v175,v190,v191
s_mov_b32 s73,256000
buffer_store_dwordx4 v[172:175],v253,s[44:47],s73 offen sc1
v_mbcnt_lo_u32_b32 v254,-1,0
v_mbcnt_hi_u32_b32 v254,-1,v254
v_and_b32 v255,15,v254
v_mul_u32_u24 v255,2560,v255
v_lshrrev_b32 v254,4,v254
v_lshlrev_b32 v254,3,v254
v_add_u32 v248,v254,v255
s_mul_i32 s64,s48,409600
s_mul_i32 s65,s50,1280
s_add_u32 s64,s64,s65
s_mul_i32 s65,s34,640
s_add_u32 s64,s64,s65
s_mul_i32 s65,s16,160
s_add_u32 s64,s64,s65
s_add_u32 s36,s96,s64
s_addc_u32 s37,s97,0
s_mov_b32 s38,407200
s_mov_b32 s67,4294901760
s_waitcnt vmcnt(0)
s_barrier
buffer_store_dword v247,v253,s[8:11],0 offen offset:128 sc1
s_mov_b32 s71,65536
LFS2_%=:
buffer_load_dword v246,v253,s[12:15],0 offen sc1
s_waitcnt vmcnt(0)
v_cmp_ne_u32 vcc,0,v246
s_sub_u32 s71,s71,1
s_cmp_eq_u32 s71,0
s_cbranch_scc1 LFS2_%=X
s_nop 0
s_and_b32 s70,vcc_lo,4113
s_cmp_eq_u32 s70,4113
s_cbranch_scc0 LFS2_%=
LFS2_%=X:
s_mov_b32 s73,14336
buffer_load_dwordx4 v[16:19],v253,s[44:47],s73 offen sc1
s_mov_b32 s73,129024
buffer_load_dwordx4 v[48:51],v253,s[44:47],s73 offen sc1
s_mov_b32 s73,358400
buffer_load_dwordx4 v[80:83],v253,s[44:47],s73 offen sc1
s_mov_b32 s73,15360
buffer_load_dwordx4 v[112:115],v253,s[44:47],s73 offen sc1
s_mov_b32 s73,130048
buffer_load_dwordx4 v[144:147],v253,s[44:47],s73 offen sc1
s_mov_b32 s73,359424
buffer_load_dwordx4 v[176:179],v253,s[44:47],s73 offen sc1
s_mov_b32 s73,16384
buffer_load_dwordx4 v[20:23],v253,s[44:47],s73 offen sc1
s_mov_b32 s73,131072
buffer_load_dwordx4 v[52:55],v253,s[44:47],s73 offen sc1
s_mov_b32 s73,360448
buffer_load_dwordx4 v[84:87],v253,s[44:47],s73 offen sc1
s_mov_b32 s73,17408
buffer_load_dwordx4 v[116:119],v253,s[44:47],s73 offen sc1
s_mov_b32 s73,132096
buffer_load_dwordx4 v[148:151],v253,s[44:47],s73 offen sc1
s_mov_b32 s73,361472
buffer_load_dwordx4 v[180:183],v253,s[44:47],s73 offen sc1
s_mov_b32 s73,18432
buffer_load_dwordx4 v[28:31],v253,s[44:47],s73 offen sc1
s_mov_b32 s73,133120
buffer_load_dwordx4 v[60:63],v253,s[44:47],s73 offen sc1
s_mov_b32 s73,362496
buffer_load_dwordx4 v[92:95],v253,s[44:47],s73 offen sc1
s_mov_b32 s73,19456
buffer_load_dwordx4 v[124:127],v253,s[44:47],s73 offen sc1
s_mov_b32 s73,134144
buffer_load_dwordx4 v[156:159],v253,s[44:47],s73 offen sc1
s_mov_b32 s73,363520
buffer_load_dwordx4 v[188:191],v253,s[44:47],s73 offen sc1
s_waitcnt vmcnt(15)
v_lshlrev_b32 v224,16,v16
v_and_b32 v225,s67,v16
v_pk_add_f32 v[8:9],v[8:9],v[224:225]
v_lshlrev_b32 v226,16,v17
v_and_b32 v227,s67,v17
v_pk_add_f32 v[10:11],v[10:11],v[226:227]
v_lshlrev_b32 v224,16,v48
v_and_b32 v225,s67,v48
v_pk_add_f32 v[8:9],v[8:9],v[224:225]
v_lshlrev_b32 v226,16,v49
v_and_b32 v227,s67,v49
v_pk_add_f32 v[10:11],v[10:11],v[226:227]
v_lshlrev_b32 v224,16,v80
v_and_b32 v225,s67,v80
v_pk_add_f32 v[8:9],v[8:9],v[224:225]
v_lshlrev_b32 v226,16,v81
v_and_b32 v227,s67,v81
v_pk_add_f32 v[10:11],v[10:11],v[226:227]
v_cvt_pk_bf16_f32 v8,v8,v9
v_cvt_pk_bf16_f32 v9,v10,v11
s_mov_b32 s73,81920
buffer_store_dwordx2 v[8:9],v248,s[36:39],s73 offen
v_lshlrev_b32 v224,16,v18
v_and_b32 v225,s67,v18
v_pk_add_f32 v[24:25],v[24:25],v[224:225]
v_lshlrev_b32 v226,16,v19
v_and_b32 v227,s67,v19
v_pk_add_f32 v[26:27],v[26:27],v[226:227]
v_lshlrev_b32 v224,16,v50
v_and_b32 v225,s67,v50
v_pk_add_f32 v[24:25],v[24:25],v[224:225]
v_lshlrev_b32 v226,16,v51
v_and_b32 v227,s67,v51
v_pk_add_f32 v[26:27],v[26:27],v[226:227]
v_lshlrev_b32 v224,16,v82
v_and_b32 v225,s67,v82
v_pk_add_f32 v[24:25],v[24:25],v[224:225]
v_lshlrev_b32 v226,16,v83
v_and_b32 v227,s67,v83
v_pk_add_f32 v[26:27],v[26:27],v[226:227]
v_cvt_pk_bf16_f32 v24,v24,v25
v_cvt_pk_bf16_f32 v25,v26,v27
s_mov_b32 s73,245760
buffer_store_dwordx2 v[24:25],v248,s[36:39],s73 offen
s_waitcnt vmcnt(14)
v_lshlrev_b32 v224,16,v112
v_and_b32 v225,s67,v112
v_pk_add_f32 v[40:41],v[40:41],v[224:225]
v_lshlrev_b32 v226,16,v113
v_and_b32 v227,s67,v113
v_pk_add_f32 v[42:43],v[42:43],v[226:227]
v_lshlrev_b32 v224,16,v144
v_and_b32 v225,s67,v144
v_pk_add_f32 v[40:41],v[40:41],v[224:225]
v_lshlrev_b32 v226,16,v145
v_and_b32 v227,s67,v145
v_pk_add_f32 v[42:43],v[42:43],v[226:227]
v_lshlrev_b32 v224,16,v176
v_and_b32 v225,s67,v176
v_pk_add_f32 v[40:41],v[40:41],v[224:225]
v_lshlrev_b32 v226,16,v177
v_and_b32 v227,s67,v177
v_pk_add_f32 v[42:43],v[42:43],v[226:227]
v_cvt_pk_bf16_f32 v40,v40,v41
v_cvt_pk_bf16_f32 v41,v42,v43
s_mov_b32 s73,32
buffer_store_dwordx2 v[40:41],v248,s[36:39],s73 offen
v_lshlrev_b32 v224,16,v114
v_and_b32 v225,s67,v114
v_pk_add_f32 v[56:57],v[56:57],v[224:225]
v_lshlrev_b32 v226,16,v115
v_and_b32 v227,s67,v115
v_pk_add_f32 v[58:59],v[58:59],v[226:227]
v_lshlrev_b32 v224,16,v146
v_and_b32 v225,s67,v146
v_pk_add_f32 v[56:57],v[56:57],v[224:225]
v_lshlrev_b32 v226,16,v147
v_and_b32 v227,s67,v147
v_pk_add_f32 v[58:59],v[58:59],v[226:227]
v_lshlrev_b32 v224,16,v178
v_and_b32 v225,s67,v178
v_pk_add_f32 v[56:57],v[56:57],v[224:225]
v_lshlrev_b32 v226,16,v179
v_and_b32 v227,s67,v179
v_pk_add_f32 v[58:59],v[58:59],v[226:227]
v_cvt_pk_bf16_f32 v56,v56,v57
v_cvt_pk_bf16_f32 v57,v58,v59
s_mov_b32 s73,163872
buffer_store_dwordx2 v[56:57],v248,s[36:39],s73 offen
s_waitcnt vmcnt(13)
v_lshlrev_b32 v224,16,v20
v_and_b32 v225,s67,v20
v_pk_add_f32 v[72:73],v[72:73],v[224:225]
v_lshlrev_b32 v226,16,v21
v_and_b32 v227,s67,v21
v_pk_add_f32 v[74:75],v[74:75],v[226:227]
v_lshlrev_b32 v224,16,v52
v_and_b32 v225,s67,v52
v_pk_add_f32 v[72:73],v[72:73],v[224:225]
v_lshlrev_b32 v226,16,v53
v_and_b32 v227,s67,v53
v_pk_add_f32 v[74:75],v[74:75],v[226:227]
v_lshlrev_b32 v224,16,v84
v_and_b32 v225,s67,v84
v_pk_add_f32 v[72:73],v[72:73],v[224:225]
v_lshlrev_b32 v226,16,v85
v_and_b32 v227,s67,v85
v_pk_add_f32 v[74:75],v[74:75],v[226:227]
v_cvt_pk_bf16_f32 v72,v72,v73
v_cvt_pk_bf16_f32 v73,v74,v75
s_mov_b32 s73,327712
buffer_store_dwordx2 v[72:73],v248,s[36:39],s73 offen
v_lshlrev_b32 v224,16,v22
v_and_b32 v225,s67,v22
v_pk_add_f32 v[88:89],v[88:89],v[224:225]
v_lshlrev_b32 v226,16,v23
v_and_b32 v227,s67,v23
v_pk_add_f32 v[90:91],v[90:91],v[226:227]
v_lshlrev_b32 v224,16,v54
v_and_b32 v225,s67,v54
v_pk_add_f32 v[88:89],v[88:89],v[224:225]
v_lshlrev_b32 v226,16,v55
v_and_b32 v227,s67,v55
v_pk_add_f32 v[90:91],v[90:91],v[226:227]
v_lshlrev_b32 v224,16,v86
v_and_b32 v225,s67,v86
v_pk_add_f32 v[88:89],v[88:89],v[224:225]
v_lshlrev_b32 v226,16,v87
v_and_b32 v227,s67,v87
v_pk_add_f32 v[90:91],v[90:91],v[226:227]
v_cvt_pk_bf16_f32 v88,v88,v89
v_cvt_pk_bf16_f32 v89,v90,v91
s_mov_b32 s73,81984
buffer_store_dwordx2 v[88:89],v248,s[36:39],s73 offen
s_waitcnt vmcnt(12)
v_lshlrev_b32 v224,16,v116
v_and_b32 v225,s67,v116
v_pk_add_f32 v[104:105],v[104:105],v[224:225]
v_lshlrev_b32 v226,16,v117
v_and_b32 v227,s67,v117
v_pk_add_f32 v[106:107],v[106:107],v[226:227]
v_lshlrev_b32 v224,16,v148
v_and_b32 v225,s67,v148
v_pk_add_f32 v[104:105],v[104:105],v[224:225]
v_lshlrev_b32 v226,16,v149
v_and_b32 v227,s67,v149
v_pk_add_f32 v[106:107],v[106:107],v[226:227]
v_lshlrev_b32 v224,16,v180
v_and_b32 v225,s67,v180
v_pk_add_f32 v[104:105],v[104:105],v[224:225]
v_lshlrev_b32 v226,16,v181
v_and_b32 v227,s67,v181
v_pk_add_f32 v[106:107],v[106:107],v[226:227]
v_cvt_pk_bf16_f32 v104,v104,v105
v_cvt_pk_bf16_f32 v105,v106,v107
s_mov_b32 s73,245824
buffer_store_dwordx2 v[104:105],v248,s[36:39],s73 offen
v_lshlrev_b32 v224,16,v118
v_and_b32 v225,s67,v118
v_pk_add_f32 v[120:121],v[120:121],v[224:225]
v_lshlrev_b32 v226,16,v119
v_and_b32 v227,s67,v119
v_pk_add_f32 v[122:123],v[122:123],v[226:227]
v_lshlrev_b32 v224,16,v150
v_and_b32 v225,s67,v150
v_pk_add_f32 v[120:121],v[120:121],v[224:225]
v_lshlrev_b32 v226,16,v151
v_and_b32 v227,s67,v151
v_pk_add_f32 v[122:123],v[122:123],v[226:227]
v_lshlrev_b32 v224,16,v182
v_and_b32 v225,s67,v182
v_pk_add_f32 v[120:121],v[120:121],v[224:225]
v_lshlrev_b32 v226,16,v183
v_and_b32 v227,s67,v183
v_pk_add_f32 v[122:123],v[122:123],v[226:227]
v_cvt_pk_bf16_f32 v120,v120,v121
v_cvt_pk_bf16_f32 v121,v122,v123
s_mov_b32 s73,96
buffer_store_dwordx2 v[120:121],v248,s[36:39],s73 offen
s_waitcnt vmcnt(11)
v_lshlrev_b32 v224,16,v28
v_and_b32 v225,s67,v28
v_pk_add_f32 v[136:137],v[136:137],v[224:225]
v_lshlrev_b32 v226,16,v29
v_and_b32 v227,s67,v29
v_pk_add_f32 v[138:139],v[138:139],v[226:227]
v_lshlrev_b32 v224,16,v60
v_and_b32 v225,s67,v60
v_pk_add_f32 v[136:137],v[136:137],v[224:225]
v_lshlrev_b32 v226,16,v61
v_and_b32 v227,s67,v61
v_pk_add_f32 v[138:139],v[138:139],v[226:227]
v_lshlrev_b32 v224,16,v92
v_and_b32 v225,s67,v92
v_pk_add_f32 v[136:137],v[136:137],v[224:225]
v_lshlrev_b32 v226,16,v93
v_and_b32 v227,s67,v93
v_pk_add_f32 v[138:139],v[138:139],v[226:227]
v_cvt_pk_bf16_f32 v136,v136,v137
v_cvt_pk_bf16_f32 v137,v138,v139
s_mov_b32 s73,163936
buffer_store_dwordx2 v[136:137],v248,s[36:39],s73 offen
v_lshlrev_b32 v224,16,v30
v_and_b32 v225,s67,v30
v_pk_add_f32 v[152:153],v[152:153],v[224:225]
v_lshlrev_b32 v226,16,v31
v_and_b32 v227,s67,v31
v_pk_add_f32 v[154:155],v[154:155],v[226:227]
v_lshlrev_b32 v224,16,v62
v_and_b32 v225,s67,v62
v_pk_add_f32 v[152:153],v[152:153],v[224:225]
v_lshlrev_b32 v226,16,v63
v_and_b32 v227,s67,v63
v_pk_add_f32 v[154:155],v[154:155],v[226:227]
v_lshlrev_b32 v224,16,v94
v_and_b32 v225,s67,v94
v_pk_add_f32 v[152:153],v[152:153],v[224:225]
v_lshlrev_b32 v226,16,v95
v_and_b32 v227,s67,v95
v_pk_add_f32 v[154:155],v[154:155],v[226:227]
v_cvt_pk_bf16_f32 v152,v152,v153
v_cvt_pk_bf16_f32 v153,v154,v155
s_mov_b32 s73,327776
buffer_store_dwordx2 v[152:153],v248,s[36:39],s73 offen
s_waitcnt vmcnt(10)
v_lshlrev_b32 v224,16,v124
v_and_b32 v225,s67,v124
v_pk_add_f32 v[168:169],v[168:169],v[224:225]
v_lshlrev_b32 v226,16,v125
v_and_b32 v227,s67,v125
v_pk_add_f32 v[170:171],v[170:171],v[226:227]
v_lshlrev_b32 v224,16,v156
v_and_b32 v225,s67,v156
v_pk_add_f32 v[168:169],v[168:169],v[224:225]
v_lshlrev_b32 v226,16,v157
v_and_b32 v227,s67,v157
v_pk_add_f32 v[170:171],v[170:171],v[226:227]
v_lshlrev_b32 v224,16,v188
v_and_b32 v225,s67,v188
v_pk_add_f32 v[168:169],v[168:169],v[224:225]
v_lshlrev_b32 v226,16,v189
v_and_b32 v227,s67,v189
v_pk_add_f32 v[170:171],v[170:171],v[226:227]
v_cvt_pk_bf16_f32 v168,v168,v169
v_cvt_pk_bf16_f32 v169,v170,v171
s_mov_b32 s73,82048
buffer_store_dwordx2 v[168:169],v248,s[36:39],s73 offen
v_lshlrev_b32 v224,16,v126
v_and_b32 v225,s67,v126
v_pk_add_f32 v[184:185],v[184:185],v[224:225]
v_lshlrev_b32 v226,16,v127
v_and_b32 v227,s67,v127
v_pk_add_f32 v[186:187],v[186:187],v[226:227]
v_lshlrev_b32 v224,16,v158
v_and_b32 v225,s67,v158
v_pk_add_f32 v[184:185],v[184:185],v[224:225]
v_lshlrev_b32 v226,16,v159
v_and_b32 v227,s67,v159
v_pk_add_f32 v[186:187],v[186:187],v[226:227]
v_lshlrev_b32 v224,16,v190
v_and_b32 v225,s67,v190
v_pk_add_f32 v[184:185],v[184:185],v[224:225]
v_lshlrev_b32 v226,16,v191
v_and_b32 v227,s67,v191
v_pk_add_f32 v[186:187],v[186:187],v[226:227]
v_cvt_pk_bf16_f32 v184,v184,v185
v_cvt_pk_bf16_f32 v185,v186,v187
s_mov_b32 s73,245888
buffer_store_dwordx2 v[184:185],v248,s[36:39],s73 offen
s_branch LEND_%=
LOSPIN0_%=:
s_cmp_eq_u32 s51,0
s_cbranch_scc1 LOBACK0_%=
s_mov_b32 s71,65536
LOSPIN0_%=L:
s_sleep 2
buffer_load_dword v246,v253,s[12:15],s69 offen sc1
s_waitcnt vmcnt(0)
v_readfirstlane_b32 s70,v246
s_sub_u32 s71,s71,1
s_cmp_eq_u32 s71,0
s_cbranch_scc1 LOSPIN0_%=X
s_cmp_eq_u32 s70,0
s_cbranch_scc1 LOSPIN0_%=L
LOSPIN0_%=X:
s_branch LOBACK0_%=
LOSPIN1_%=:
s_cmp_eq_u32 s51,0
s_cbranch_scc1 LOBACK1_%=
s_mov_b32 s71,65536
LOSPIN1_%=L:
s_sleep 2
buffer_load_dword v246,v253,s[12:15],s69 offen sc1
s_waitcnt vmcnt(0)
v_readfirstlane_b32 s70,v246
s_sub_u32 s71,s71,1
s_cmp_eq_u32 s71,0
s_cbranch_scc1 LOSPIN1_%=X
s_cmp_eq_u32 s70,0
s_cbranch_scc1 LOSPIN1_%=L
LOSPIN1_%=X:
s_branch LOBACK1_%=
LSLOW_%=:
s_lshl_b32 s68,s48,5
s_lshl_b32 s69,s49,1
s_add_u32 s68,s68,s69
s_add_u32 s68,s68,s18
s_mul_i32 s69,s68,196608
s_cmp_eq_u32 s62,0
s_cselect_b64 s[28:29],s[78:79],s[88:89]
s_add_u32 s28,s28,s69
s_addc_u32 s29,s29,0
s_cmp_eq_u32 s62,0
s_cselect_b64 s[40:41],s[80:81],s[90:91]
s_add_u32 s40,s40,s69
s_addc_u32 s41,s41,0
s_mov_b32 s30,196608
s_mov_b32 s42,196608
s_mul_i32 s54,s19,6144
s_mov_b32 s55,s54
v_mov_b32 v200,0xff800000
v_mov_b32 v201,0xff800000
v_mov_b32 v202,0xff800000
v_mov_b32 v203,0xff800000
v_mov_b32 v204,0xff800000
s_mov_b32 s51,16
LSLOOP_%=:
s_add_u32 m0,s56,0
s_nop 0
buffer_load_dwordx4 v250,s[28:31],s54 offen nt lds
buffer_load_dwordx4 v251,s[28:31],s54 offen offset:1024 nt lds
buffer_load_dwordx4 v252,s[28:31],s54 offen offset:2048 nt lds
buffer_load_dwordx4 v250,s[28:31],s54 offen offset:3072 nt lds
s_add_u32 m0,s56,4096
s_add_u32 s68,s54,4096
buffer_load_dwordx4 v251,s[28:31],s68 offen nt lds
buffer_load_dwordx4 v252,s[28:31],s68 offen offset:1024 nt lds
s_add_u32 s54,s54,12288
s_waitcnt vmcnt(0)
s_barrier
ds_read_b128 a[200:203],v240
ds_read_b128 a[204:207],v241
ds_read_b128 a[208:211],v240 offset:128
ds_read_b128 a[212:215],v241 offset:128
ds_read_b128 a[216:219],v240 offset:256
ds_read_b128 a[220:223],v241 offset:256
ds_read_b128 a[224:227],v240 offset:6144
ds_read_b128 a[228:231],v241 offset:6144
ds_read_b128 a[232:235],v240 offset:6272
ds_read_b128 a[236:239],v241 offset:6272
ds_read_b128 a[240:243],v240 offset:6400
ds_read_b128 a[244:247],v241 offset:6400
s_waitcnt lgkmcnt(0)
s_barrier
v_mfma_f32_16x16x32_bf16 v[160:163],a[200:203],a[80:83],0
v_mfma_f32_16x16x32_bf16 v[164:167],a[200:203],a[84:87],0
v_mfma_f32_16x16x32_bf16 v[168:171],a[200:203],a[88:91],0
v_mfma_f32_16x16x32_bf16 v[172:175],a[200:203],a[92:95],0
v_mfma_f32_16x16x32_bf16 v[176:179],a[200:203],a[96:99],0
v_mfma_f32_16x16x32_bf16 v[160:163],a[204:207],a[100:103],v[160:163]
v_mfma_f32_16x16x32_bf16 v[164:167],a[204:207],a[104:107],v[164:167]
v_mfma_f32_16x16x32_bf16 v[168:171],a[204:207],a[108:111],v[168:171]
v_mfma_f32_16x16x32_bf16 v[172:175],a[204:207],a[112:115],v[172:175]
v_mfma_f32_16x16x32_bf16 v[176:179],a[204:207],a[116:119],v[176:179]
v_mfma_f32_16x16x32_bf16 v[160:163],a[208:211],a[120:123],v[160:163]
v_mfma_f32_16x16x32_bf16 v[164:167],a[208:211],a[124:127],v[164:167]
v_mfma_f32_16x16x32_bf16 v[168:171],a[208:211],a[128:131],v[168:171]
v_mfma_f32_16x16x32_bf16 v[172:175],a[208:211],a[132:135],v[172:175]
v_mfma_f32_16x16x32_bf16 v[176:179],a[208:211],a[136:139],v[176:179]
v_mfma_f32_16x16x32_bf16 v[160:163],a[212:215],a[140:143],v[160:163]
v_mfma_f32_16x16x32_bf16 v[164:167],a[212:215],a[144:147],v[164:167]
v_mfma_f32_16x16x32_bf16 v[168:171],a[212:215],a[148:151],v[168:171]
v_mfma_f32_16x16x32_bf16 v[172:175],a[212:215],a[152:155],v[172:175]
v_mfma_f32_16x16x32_bf16 v[176:179],a[212:215],a[156:159],v[176:179]
v_mfma_f32_16x16x32_bf16 v[160:163],a[216:219],a[160:163],v[160:163]
v_mfma_f32_16x16x32_bf16 v[164:167],a[216:219],a[164:167],v[164:167]
v_mfma_f32_16x16x32_bf16 v[168:171],a[216:219],a[168:171],v[168:171]
v_mfma_f32_16x16x32_bf16 v[172:175],a[216:219],a[172:175],v[172:175]
v_mfma_f32_16x16x32_bf16 v[176:179],a[216:219],a[176:179],v[176:179]
v_mfma_f32_16x16x32_bf16 v[160:163],a[220:223],a[180:183],v[160:163]
v_mfma_f32_16x16x32_bf16 v[164:167],a[220:223],a[184:187],v[164:167]
v_mfma_f32_16x16x32_bf16 v[168:171],a[220:223],a[188:191],v[168:171]
v_mfma_f32_16x16x32_bf16 v[172:175],a[220:223],a[192:195],v[172:175]
v_mfma_f32_16x16x32_bf16 v[176:179],a[220:223],a[196:199],v[176:179]
v_mfma_f32_16x16x32_bf16 v[180:183],a[224:227],a[80:83],0
v_mfma_f32_16x16x32_bf16 v[184:187],a[224:227],a[84:87],0
v_mfma_f32_16x16x32_bf16 v[188:191],a[224:227],a[88:91],0
v_mfma_f32_16x16x32_bf16 v[192:195],a[224:227],a[92:95],0
v_mfma_f32_16x16x32_bf16 v[196:199],a[224:227],a[96:99],0
v_mfma_f32_16x16x32_bf16 v[180:183],a[228:231],a[100:103],v[180:183]
v_mfma_f32_16x16x32_bf16 v[184:187],a[228:231],a[104:107],v[184:187]
v_mfma_f32_16x16x32_bf16 v[188:191],a[228:231],a[108:111],v[188:191]
v_mfma_f32_16x16x32_bf16 v[192:195],a[228:231],a[112:115],v[192:195]
v_mfma_f32_16x16x32_bf16 v[196:199],a[228:231],a[116:119],v[196:199]
v_mfma_f32_16x16x32_bf16 v[180:183],a[232:235],a[120:123],v[180:183]
v_mfma_f32_16x16x32_bf16 v[184:187],a[232:235],a[124:127],v[184:187]
v_mfma_f32_16x16x32_bf16 v[188:191],a[232:235],a[128:131],v[188:191]
v_mfma_f32_16x16x32_bf16 v[192:195],a[232:235],a[132:135],v[192:195]
v_mfma_f32_16x16x32_bf16 v[196:199],a[232:235],a[136:139],v[196:199]
v_mfma_f32_16x16x32_bf16 v[180:183],a[236:239],a[140:143],v[180:183]
v_mfma_f32_16x16x32_bf16 v[184:187],a[236:239],a[144:147],v[184:187]
v_mfma_f32_16x16x32_bf16 v[188:191],a[236:239],a[148:151],v[188:191]
v_mfma_f32_16x16x32_bf16 v[192:195],a[236:239],a[152:155],v[192:195]
v_mfma_f32_16x16x32_bf16 v[196:199],a[236:239],a[156:159],v[196:199]
v_mfma_f32_16x16x32_bf16 v[180:183],a[240:243],a[160:163],v[180:183]
v_mfma_f32_16x16x32_bf16 v[184:187],a[240:243],a[164:167],v[184:187]
v_mfma_f32_16x16x32_bf16 v[188:191],a[240:243],a[168:171],v[188:191]
v_mfma_f32_16x16x32_bf16 v[192:195],a[240:243],a[172:175],v[192:195]
v_mfma_f32_16x16x32_bf16 v[196:199],a[240:243],a[176:179],v[196:199]
v_mfma_f32_16x16x32_bf16 v[180:183],a[244:247],a[180:183],v[180:183]
v_mfma_f32_16x16x32_bf16 v[184:187],a[244:247],a[184:187],v[184:187]
v_mfma_f32_16x16x32_bf16 v[188:191],a[244:247],a[188:191],v[188:191]
v_mfma_f32_16x16x32_bf16 v[192:195],a[244:247],a[192:195],v[192:195]
v_mfma_f32_16x16x32_bf16 v[196:199],a[244:247],a[196:199],v[196:199]
s_nop 7
v_max3_f32 v200,v200,v160,v161
v_max3_f32 v200,v200,v162,v163
v_max3_f32 v200,v200,v180,v181
v_max3_f32 v200,v200,v182,v183
v_max3_f32 v201,v201,v164,v165
v_max3_f32 v201,v201,v166,v167
v_max3_f32 v201,v201,v184,v185
v_max3_f32 v201,v201,v186,v187
v_max3_f32 v202,v202,v168,v169
v_max3_f32 v202,v202,v170,v171
v_max3_f32 v202,v202,v188,v189
v_max3_f32 v202,v202,v190,v191
v_max3_f32 v203,v203,v172,v173
v_max3_f32 v203,v203,v174,v175
v_max3_f32 v203,v203,v192,v193
v_max3_f32 v203,v203,v194,v195
v_max3_f32 v204,v204,v176,v177
v_max3_f32 v204,v204,v178,v179
v_max3_f32 v204,v204,v196,v197
v_max3_f32 v204,v204,v198,v199
s_sub_u32 s51,s51,1
s_cmp_lg_u32 s51,0
s_cbranch_scc1 LSLOOP_%=
v_mov_b32 v210,v200
s_nop 1
v_permlane32_swap_b32 v210,v200
v_max_f32 v200,v200,v210
v_mov_b32 v210,v200
s_nop 1
v_permlane16_swap_b32 v210,v200
v_max_f32 v200,v200,v210
v_sub_f32 v220,0,v200
v_sub_f32 v221,0,v200
v_sub_f32 v222,0,v200
v_sub_f32 v223,0,v200
v_mov_b32 v210,v201
s_nop 1
v_permlane32_swap_b32 v210,v201
v_max_f32 v201,v201,v210
v_mov_b32 v210,v201
s_nop 1
v_permlane16_swap_b32 v210,v201
v_max_f32 v201,v201,v210
v_sub_f32 v224,0,v201
v_sub_f32 v225,0,v201
v_sub_f32 v226,0,v201
v_sub_f32 v227,0,v201
v_mov_b32 v210,v202
s_nop 1
v_permlane32_swap_b32 v210,v202
v_max_f32 v202,v202,v210
v_mov_b32 v210,v202
s_nop 1
v_permlane16_swap_b32 v210,v202
v_max_f32 v202,v202,v210
v_sub_f32 v228,0,v202
v_sub_f32 v229,0,v202
v_sub_f32 v230,0,v202
v_sub_f32 v231,0,v202
v_mov_b32 v210,v203
s_nop 1
v_permlane32_swap_b32 v210,v203
v_max_f32 v203,v203,v210
v_mov_b32 v210,v203
s_nop 1
v_permlane16_swap_b32 v210,v203
v_max_f32 v203,v203,v210
v_sub_f32 v232,0,v203
v_sub_f32 v233,0,v203
v_sub_f32 v234,0,v203
v_sub_f32 v235,0,v203
v_mov_b32 v210,v204
s_nop 1
v_permlane32_swap_b32 v210,v204
v_max_f32 v204,v204,v210
v_mov_b32 v210,v204
s_nop 1
v_permlane16_swap_b32 v210,v204
v_max_f32 v204,v204,v210
v_sub_f32 v236,0,v204
v_sub_f32 v237,0,v204
v_sub_f32 v238,0,v204
v_sub_f32 v239,0,v204
s_lshl_b32 s68,s48,5
s_lshl_b32 s69,s49,1
s_add_u32 s68,s68,s69
s_add_u32 s68,s68,s18
s_mul_i32 s69,s68,196608
s_cmp_eq_u32 s62,0
s_cselect_b64 s[28:29],s[78:79],s[88:89]
s_add_u32 s28,s28,s69
s_addc_u32 s29,s29,0
s_cmp_eq_u32 s62,0
s_cselect_b64 s[40:41],s[80:81],s[90:91]
s_add_u32 s40,s40,s69
s_addc_u32 s41,s41,0
s_mov_b32 s30,196608
s_mov_b32 s42,196608
s_mul_i32 s54,s19,6144
s_mov_b32 s55,s54
s_add_u32 m0,s56,0
s_nop 0
buffer_load_dwordx4 v250,s[28:31],s54 offen nt lds
buffer_load_dwordx4 v251,s[28:31],s54 offen offset:1024 nt lds
buffer_load_dwordx4 v252,s[28:31],s54 offen offset:2048 nt lds
buffer_load_dwordx4 v250,s[28:31],s54 offen offset:3072 nt lds
s_add_u32 m0,s56,4096
s_add_u32 s68,s54,4096
buffer_load_dwordx4 v251,s[28:31],s68 offen nt lds
buffer_load_dwordx4 v252,s[28:31],s68 offen offset:1024 nt lds
s_add_u32 s54,s54,12288
s_add_u32 m0,s56,24576
s_nop 0
buffer_load_dwordx4 v250,s[28:31],s54 offen nt lds
buffer_load_dwordx4 v251,s[28:31],s54 offen offset:1024 nt lds
buffer_load_dwordx4 v252,s[28:31],s54 offen offset:2048 nt lds
buffer_load_dwordx4 v250,s[28:31],s54 offen offset:3072 nt lds
s_add_u32 m0,s56,28672
s_add_u32 s68,s54,4096
buffer_load_dwordx4 v251,s[28:31],s68 offen nt lds
buffer_load_dwordx4 v252,s[28:31],s68 offen offset:1024 nt lds
s_add_u32 s54,s54,12288
s_add_u32 m0,s56,49152
s_nop 0
buffer_load_dwordx4 v250,s[28:31],s54 offen nt lds
buffer_load_dwordx4 v251,s[28:31],s54 offen offset:1024 nt lds
buffer_load_dwordx4 v252,s[28:31],s54 offen offset:2048 nt lds
buffer_load_dwordx4 v250,s[28:31],s54 offen offset:3072 nt lds
s_add_u32 m0,s56,53248
s_add_u32 s68,s54,4096
buffer_load_dwordx4 v251,s[28:31],s68 offen nt lds
buffer_load_dwordx4 v252,s[28:31],s68 offen offset:1024 nt lds
s_add_u32 s54,s54,12288
s_add_u32 m0,s57,0
s_nop 0
buffer_load_dwordx4 v250,s[40:43],s55 offen nt lds
buffer_load_dwordx4 v251,s[40:43],s55 offen offset:1024 nt lds
buffer_load_dwordx4 v252,s[40:43],s55 offen offset:2048 nt lds
buffer_load_dwordx4 v250,s[40:43],s55 offen offset:3072 nt lds
s_add_u32 m0,s57,4096
s_add_u32 s68,s55,4096
buffer_load_dwordx4 v251,s[40:43],s68 offen nt lds
buffer_load_dwordx4 v252,s[40:43],s68 offen offset:1024 nt lds
s_add_u32 s55,s55,12288
s_add_u32 m0,s57,24576
s_nop 0
buffer_load_dwordx4 v250,s[40:43],s55 offen nt lds
buffer_load_dwordx4 v251,s[40:43],s55 offen offset:1024 nt lds
buffer_load_dwordx4 v252,s[40:43],s55 offen offset:2048 nt lds
buffer_load_dwordx4 v250,s[40:43],s55 offen offset:3072 nt lds
s_add_u32 m0,s57,28672
s_add_u32 s68,s55,4096
buffer_load_dwordx4 v251,s[40:43],s68 offen nt lds
buffer_load_dwordx4 v252,s[40:43],s68 offen offset:1024 nt lds
s_add_u32 s55,s55,12288
s_branch LAPRO_%=
LEND_%=:
s_waitcnt vmcnt(0)
LCODEEND_%=:
)ASM"
    :: "s"(__builtin_amdgcn_kernarg_segment_ptr()), "s"(wid), "s"(blockIdx.x), "s"((unsigned)(size_t)lds) : "v0","v1","v2","v3","v4","v5","v6","v7","v8","v9","v10","v11","v12","v13","v14","v15","v16","v17","v18","v19","v20","v21","v22","v23","v24","v25","v26","v27","v28","v29","v30","v31","v32","v33","v34","v35","v36","v37","v38","v39","v40","v41","v42","v43","v44","v45","v46","v47","v48","v49","v50","v51","v52","v53","v54","v55","v56","v57","v58","v59","v60","v61","v62","v63","v64","v65","v66","v67","v68","v69","v70","v71","v72","v73","v74","v75","v76","v77","v78","v79","v80","v81","v82","v83","v84","v85","v86","v87","v88","v89","v90","v91","v92","v93","v94","v95","v96","v97","v98","v99","v100","v101","v102","v103","v104","v105","v106","v107","v108","v109","v110","v111","v112","v113","v114","v115","v116","v117","v118","v119","v120","v121","v122","v123","v124","v125","v126","v127","v128","v129","v130","v131","v132","v133","v134","v135","v136","v137","v138","v139","v140","v141","v142","v143","v144","v145","v146","v147","v148","v149","v150","v151","v152","v153","v154","v155","v156","v157","v158","v159","v160","v161","v162","v163","v164","v165","v166","v167","v168","v169","v170","v171","v172","v173","v174","v175","v176","v177","v178","v179","v180","v181","v182","v183","v184","v185","v186","v187","v188","v189","v190","v191","v192","v193","v194","v195","v196","v197","v198","v199","v200","v201","v202","v203","v204","v205","v206","v207","v208","v209","v210","v211","v212","v213","v214","v215","v216","v217","v218","v219","v220","v221","v222","v223","v224","v225","v226","v227","v228","v229","v230","v231","v232","v233","v234","v235","v236","v237","v238","v239","v240","v241","v242","v243","v244","v245","v246","v247","v248","v249","v250","v251","v252","v253","v254","v255","a0","a1","a2","a3","a4","a5","a6","a7","a8","a9","a10","a11","a12","a13","a14","a15","a16","a17","a18","a19","a20","a21","a22","a23","a24","a25","a26","a27","a28","a29","a30","a31","a32","a33","a34","a35","a36","a37","a38","a39","a40","a41","a42","a43","a44","a45","a46","a47","a48","a49","a50","a51","a52","a53","a54","a55","a56","a57","a58","a59","a60","a61","a62","a63","a64","a65","a66","a67","a68","a69","a70","a71","a72","a73","a74","a75","a76","a77","a78","a79","a80","a81","a82","a83","a84","a85","a86","a87","a88","a89","a90","a91","a92","a93","a94","a95","a96","a97","a98","a99","a100","a101","a102","a103","a104","a105","a106","a107","a108","a109","a110","a111","a112","a113","a114","a115","a116","a117","a118","a119","a120","a121","a122","a123","a124","a125","a126","a127","a128","a129","a130","a131","a132","a133","a134","a135","a136","a137","a138","a139","a140","a141","a142","a143","a144","a145","a146","a147","a148","a149","a150","a151","a152","a153","a154","a155","a156","a157","a158","a159","a160","a161","a162","a163","a164","a165","a166","a167","a168","a169","a170","a171","a172","a173","a174","a175","a176","a177","a178","a179","a180","a181","a182","a183","a184","a185","a186","a187","a188","a189","a190","a191","a192","a193","a194","a195","a196","a197","a198","a199","a200","a201","a202","a203","a204","a205","a206","a207","a208","a209","a210","a211","a212","a213","a214","a215","a216","a217","a218","a219","a220","a221","a222","a223","a224","a225","a226","a227","a228","a229","a230","a231","a232","a233","a234","a235","a236","a237","a238","a239","a240","a241","a242","a243","a244","a245","a246","a247","a248","a249","a250","a251","a252","a253","a254","a255","s5","s6","s7","s8","s9","s10","s11","s12","s13","s14","s15","s16","s17","s18","s19","s20","s21","s22","s23","s24","s25","s26","s27","s28","s29","s30","s31","s33","s34","s35","s36","s37","s38","s39","s40","s41","s42","s43","s44","s45","s46","s47","s48","s49","s50","s51","s52","s53","s54","s55","s56","s57","s58","s59","s60","s61","s62","s63","s64","s65","s66","s67","s68","s69","s70","s71","s72","s73","s74","s75","s76","s77","s78","s79","s80","s81","s82","s83","s84","s85","s86","s87","s88","s89","s90","s91","s92","s93","s94","s95","s96","s97","s98","s99","vcc","scc","memory");
}

#define WS_DIAG_OFF(ws_size) ((((ws_size - 94437376) & ~(size_t)65535)) + 92340224)
extern "C" void kernel_launch(void* const* d_in, const int* in_sizes, int n_in, void* d_out, int out_size, void* d_ws, size_t ws_size, hipStream_t stream) {
  d_ws = (char*)d_ws + ((ws_size - 94437376) & ~(size_t)65535);
  hipMemsetAsync(d_ws, 0, 49152, stream);
  fused_attn<<<256, 256, 0, stream>>>(d_in[0], d_in[1], d_in[2], d_in[3], d_in[4], d_in[5], d_in[6], d_in[7], d_in[8], d_in[9], d_out, d_ws);
}
```
